```python
import torch

def _softmax(x, axis=-1):
    return torch.softmax(x, dim=axis)
def _gelu(x):
    return torch.nn.functional.gelu(x, approximate="tanh")
def _silu(x):
    return torch.nn.functional.silu(x)
def _sigmoid(x):
    return torch.sigmoid(x)
def _layernorm(x, axis=-1, eps=1e-5):
    m = x.mean(dim=axis, keepdim=True)
    v = x.var(dim=axis, keepdim=True, unbiased=False)
    return (x - m) / torch.sqrt(v + eps)
def _scatter_add(tgt, idx, src, axis=0):
    return tgt.clone().index_add_(axis, idx, src)
def _segment_sum(src, seg, n):
    out = torch.zeros((n,) + src.shape[1:], dtype=src.dtype)
    return out.index_add_(0, seg, src)
def _pad(x, pw):
    flat = tuple(v for pair in reversed(pw) for v in pair)
    return torch.nn.functional.pad(x, flat)

kernel_name = 'parallel_sum__ssm_conv_s4d__dense_activation_mish__prompt'

def setup_inputs():

    _w = (lambda a: (lambda t: t.to(torch.bfloat16) if t.is_floating_point() else t)(torch.as_tensor(a)))
    def _A_setup():
        Bsz, L, D, N = 1, 2048, 640, 128
        return {
            "u": _w((lambda *s: torch.randn(*s, dtype=torch.bfloat16) * 0.05)(Bsz, L, D)),
            "log_A_real": _w((lambda *s: torch.randn(*s, dtype=torch.bfloat16) * 3.2)(D, N) * 0.5),
            "A_imag": _w((lambda *s: torch.randn(*s, dtype=torch.bfloat16) * 0.05)(D, N) * 3.14),
            "C_real": _w((lambda *s: torch.randn(*s, dtype=torch.bfloat16) * 0.2)(D, N) / N ** 0.5),
            "C_imag": _w((lambda *s: torch.randn(*s, dtype=torch.bfloat16) * 0.2)(D, N) / N ** 0.5),
            "log_dt": _w((lambda *s: torch.randn(*s, dtype=torch.bfloat16) * 0.05)(D) * 0.5 - 4.0),
            "D_skip": _w((lambda *s: torch.randn(*s, dtype=torch.bfloat16) * 0.05)(D)),
        }
    _A_in = _A_setup()

    def _B_setup():
        B, S, D, F = 1, 2048, 640, 1728
        return {
            "x": _w((lambda *s: torch.randn(*s, dtype=torch.bfloat16) * 0.05)(B, S, D)),
            "w1": _w((lambda *s: torch.randn(*s, dtype=torch.bfloat16) * 0.8)(D, F) * (D ** -0.5)),
            "b1": _w((lambda *s: torch.randn(*s, dtype=torch.bfloat16) * 0.2)(F)),
            "w2": _w((lambda *s: torch.randn(*s, dtype=torch.bfloat16) * 0.2)(F, D) * (F ** -0.5)),
        }
    _B_in = _B_setup()

    return {
        "A_u": _A_in["u"],
        "A_log_A_real": _A_in["log_A_real"],
        "A_A_imag": _A_in["A_imag"],
        "A_C_real": _A_in["C_real"],
        "A_C_imag": _A_in["C_imag"],
        "A_log_dt": _A_in["log_dt"],
        "A_D_skip": _A_in["D_skip"],
        "B_x": _B_in["x"],
        "B_w1": _B_in["w1"],
        "B_b1": _B_in["b1"],
        "B_w2": _B_in["w2"],
    }

def reference(A_u, A_log_A_real, A_A_imag, A_C_real, A_C_imag, A_log_dt, A_D_skip, B_x, B_w1, B_b1, B_w2):
    def _A_ref(u, log_A_real, A_imag, C_real, C_imag, log_dt, D_skip):
        L = u.shape[1]
        dt = torch.exp(log_dt)
        A = -torch.exp(log_A_real) + 1j * A_imag
        C = (C_real + 1j * C_imag) * dt[:, None]
        pos = (lambda x, dt: x.to(dt))(torch.arange(L), log_dt.dtype)
        P = torch.exp((A * dt[:, None])[:, :, None] * pos[None, None, :])
        K = 2.0 * torch.einsum("dn,dnl->dl", C, P).real
        L2 = 2 * L
        u_t = (lambda x, *d: x.permute(*d))(u, 0, 2, 1)
        Kf = (lambda x, *a, **k: torch.fft.rfft(x.float(), *a, **k))(K, L2)
        Uf = (lambda x, *a, **k: torch.fft.rfft(x.float(), *a, **k))(u_t, L2)
        y = (lambda x, *a, **k: torch.fft.irfft(x, *a, **k).to(torch.bfloat16))(Uf * Kf[None, :, :], L2)[:, :, :L]
        y = (lambda x, *d: x.permute(*d))(y, 0, 2, 1)
        return y + u * D_skip[None, None, :]

    def _B_ref(x, w1, b1, w2):
        h = torch.einsum("bsd,df->bsf", x, w1) + b1
        sp = torch.log1p(torch.exp(-torch.abs(h))) + torch.where(h > 0, h, torch.zeros_like(h))
        act = h * torch.tanh(sp)
        return torch.einsum("bsf,fd->bsd", act, w2)

    x = A_u
    xn = _layernorm(x, axis=-1)
    return x + _A_ref(xn, A_log_A_real, A_A_imag, A_C_real, A_C_imag, A_log_dt, A_D_skip) + _B_ref(xn, B_w1, B_b1, B_w2)
```

```cpp
#include <hip/hip_runtime.h>
#include <hip/hip_bf16.h>

#define SEQ 2048
#define DM 640
#define NS 128
#define FF 1728

typedef unsigned short u16;
typedef short s4 __attribute__((ext_vector_type(4)));
typedef short s8 __attribute__((ext_vector_type(8)));
typedef _Float16 h8 __attribute__((ext_vector_type(8)));
typedef float f4 __attribute__((ext_vector_type(4)));
typedef unsigned int u32;
typedef unsigned int u32x4 __attribute__((ext_vector_type(4)));
typedef unsigned int u32x2 __attribute__((ext_vector_type(2)));

__device__ __forceinline__ float bf2f(u16 v) { return __uint_as_float(((u32)v) << 16); }
__device__ __forceinline__ u16 f2bf(float f) {
  u32 u = __float_as_uint(f);
  u32 r = (u + 0x7FFFu + ((u >> 16) & 1u)) >> 16;
  return (u16)r;
}
__device__ __forceinline__ float bfr(float f) { return bf2f(f2bf(f)); }

__global__ __launch_bounds__(256) void ln_kernel(const u16* __restrict__ u, u16* __restrict__ xn,
                                                 u16* __restrict__ xT) {
  __shared__ __attribute__((aligned(16))) u16 tile[16][DM + 8];
  const int wave = threadIdx.x >> 6, lane = threadIdx.x & 63;
  const int r = wave * 4 + (lane >> 4), q = lane & 15;
  const int row = blockIdx.x * 16 + r;
  float v[40];
  const u32x4* src = (const u32x4*)(u + (size_t)row * DM);
#pragma unroll
  for (int i = 0; i < 5; ++i) {
    u32x4 w = src[q + 16 * i];
#pragma unroll
    for (int j = 0; j < 4; ++j) {
      v[i * 8 + 2 * j] = __uint_as_float(w[j] << 16);
      v[i * 8 + 2 * j + 1] = __uint_as_float(w[j] & 0xFFFF0000u);
    }
  }
  float s = 0.f;
#pragma unroll
  for (int i = 0; i < 40; ++i) s += v[i];
#pragma unroll
  for (int o = 1; o < 16; o <<= 1) s += __shfl_xor(s, o);
  const float mean = s * (1.0f / DM);
  float ss = 0.f;
#pragma unroll
  for (int i = 0; i < 40; ++i) { float d = v[i] - mean; ss += d * d; }
#pragma unroll
  for (int o = 1; o < 16; o <<= 1) ss += __shfl_xor(ss, o);
  const float var = ss * (1.0f / DM);
  const float mb = bfr(mean);
  const float vb = bfr(var);
  const float den = bfr(sqrtf(bfr(vb + 1e-5f)));
  u32x4* dst = (u32x4*)(xn + (size_t)row * DM);
#pragma unroll
  for (int i = 0; i < 5; ++i) {
    u32x4 w, hh;
#pragma unroll
    for (int j = 0; j < 4; ++j) {
      float a = bfr(bfr(v[i * 8 + 2 * j] - mb) / den);
      float b = bfr(bfr(v[i * 8 + 2 * j + 1] - mb) / den);
      w[j] = (__float_as_uint(a) >> 16) | (__float_as_uint(b) & 0xFFFF0000u);
      _Float16 ha = (_Float16)a, hb = (_Float16)b;
      hh[j] = (u32)__builtin_bit_cast(u16, ha) | ((u32)__builtin_bit_cast(u16, hb) << 16);
    }
    dst[q + 16 * i] = w;
    *(u32x4*)&tile[r][(q + 16 * i) * 8] = hh;
  }
  __syncthreads();
#pragma unroll
  for (int i = 0; i < 5; ++i) {
    int idx = threadIdx.x + 256 * i;
    int d = idx >> 1, c = idx & 1;
    u32x4 w;
#pragma unroll
    for (int j = 0; j < 4; ++j) {
      w[j] = (u32)tile[c * 8 + 2 * j][d] | ((u32)tile[c * 8 + 2 * j + 1][d] << 16);
    }
    *(u32x4*)(xT + (size_t)d * SEQ + blockIdx.x * 16 + c * 8) = w;
  }
}

#define KRLEN 2080
__device__ __forceinline__ void cexp_p(float arl2, float w, float p, float& re, float& im) {
  float mag = __builtin_amdgcn_exp2f(arl2 * p);
  float rev = w * p;
  rev = rev - floorf(rev);
  re = mag * __builtin_amdgcn_cosf(rev);
  im = mag * __builtin_amdgcn_sinf(rev);
}

__global__ __launch_bounds__(256) void kgen_kernel(const u16* __restrict__ lar, const u16* __restrict__ aim,
                                                   const u16* __restrict__ cre, const u16* __restrict__ cim,
                                                   const u16* __restrict__ logdt, u16* __restrict__ Kr,
                                                   float* __restrict__ kscale) {
  __shared__ __attribute__((aligned(16))) float Ah[48][260];
  __shared__ __attribute__((aligned(16))) float Bl[16][260];
  __shared__ __attribute__((aligned(16))) float red[4][768];
  __shared__ float Kp[768];
  __shared__ float wmax[4];
  const int d = blockIdx.x, t = threadIdx.x, n = t & 127, half = t >> 7;
  const int wave = t >> 6, lane = t & 63;
  const float dt = bfr(expf(bf2f(logdt[d])));
  const float ar = -bfr(expf(bf2f(lar[d * NS + n]))) * dt;
  const float ai = bf2f(aim[d * NS + n]) * dt;
  const float cr = bf2f(cre[d * NS + n]) * dt;
  const float ci = bf2f(cim[d * NS + n]) * dt;
  const float arl2 = ar * 1.4426950408889634f;
  const float w = ai * 0.15915494309189535f;
  const int r0 = half ? 21 : 0, r1 = half ? 41 : 21;
  for (int row = r0; row < r1; ++row) {
    int p;
    if (row < 16) p = (row >> 3) * 128 + (row & 7);
    else if (row < 24) p = (2 + ((row - 16) >> 2)) * 128 + ((row - 16) & 3) * 2;
    else if (row < 32) p = (4 + ((row - 24) >> 1)) * 128 + ((row - 24) & 1) * 4;
    else p = (8 + (row - 32)) * 128;
    float er, ei;
    cexp_p(arl2, w, (float)p, er, ei);
    float hr = cr * er - ci * ei;
    float hi = cr * ei + ci * er;
    Ah[row][n] = hr;
    Ah[row][128 + n] = -hi;
  }
  if (half) {
    for (int row = 41; row < 48; ++row) { Ah[row][n] = 0.f; Ah[row][128 + n] = 0.f; }
  }
  for (int j = half * 8; j < half * 8 + 8; ++j) {
    float er, ei;
    cexp_p(arl2, w, (float)(8 * j), er, ei);
    Bl[j][n] = er;
    Bl[j][128 + n] = ei;
  }
  __syncthreads();
  {
    const int i = lane & 15, g = lane >> 4;
    f4 acc[3];
#pragma unroll
    for (int tt = 0; tt < 3; ++tt) acc[tt] = (f4){0.f, 0.f, 0.f, 0.f};
#pragma unroll
    for (int qq = 0; qq < 4; ++qq) {
      const int k0 = 64 * wave + 16 * qq + 4 * g;
      f4 b = *(const f4*)&Bl[i][k0];
#pragma unroll
      for (int tt = 0; tt < 3; ++tt) {
        f4 a = *(const f4*)&Ah[tt * 16 + i][k0];
#pragma unroll
        for (int e = 0; e < 4; ++e) acc[tt] = __builtin_amdgcn_mfma_f32_16x16x4f32(a[e], b[e], acc[tt], 0, 0, 0);
      }
    }
#pragma unroll
    for (int tt = 0; tt < 3; ++tt)
#pragma unroll
      for (int r = 0; r < 4; ++r) red[wave][(tt * 16 + 4 * g + r) * 16 + i] = acc[tt][r];
  }
  __syncthreads();
  float mx = 0.f;
#pragma unroll
  for (int i = 0; i < 3; ++i) {
    int idx = t + 256 * i;
    float s = 2.0f * (((red[0][idx] + red[1][idx]) + red[2][idx]) + red[3][idx]);
    Kp[idx] = s;
    mx = fmaxf(mx, fabsf(s));
  }
#pragma unroll
  for (int o = 1; o < 64; o <<= 1) mx = fmaxf(mx, __shfl_xor(mx, o));
  if (lane == 0) wmax[wave] = mx;
  __syncthreads();
  mx = fmaxf(fmaxf(wmax[0], wmax[1]), fmaxf(wmax[2], wmax[3]));
  int ex = 0;
  if (mx > 0.f && mx < 3.0e38f) { (void)frexpf(mx, &ex); }
  int e = 15 - ex;
  e = e > 100 ? 100 : (e < -100 ? -100 : e);
  const float sc = ldexpf(1.0f, e);
  if (t == 0) kscale[d] = ldexpf(1.0f, -e);
  for (int c = t; c < KRLEN / 8; c += 256) {
    u32x4 o;
#pragma unroll
    for (int j = 0; j < 4; ++j) {
      u32 pk = 0;
#pragma unroll
      for (int h = 0; h < 2; ++h) {
        int tt = c * 8 + 2 * j + h;
        int l = 2047 - tt;
        float val = 0.f;
        if (l >= 0) {
          int p = (int)bfr((float)l);
          int col = (p >> 3) & 15, ii = p >> 7, r = p & 7;
          int row;
          if (ii < 2) row = ii * 8 + r;
          else if (ii < 4) row = 16 + (ii - 2) * 4 + (r >> 1);
          else if (ii < 8) row = 24 + (ii - 4) * 2 + (r >> 2);
          else row = 32 + (ii - 8);
          val = Kp[row * 16 + col] * sc;
        }
        _Float16 hv = (_Float16)val;
        pk |= ((u32)__builtin_bit_cast(u16, hv)) << (16 * h);
      }
      o[j] = pk;
    }
    *(u32x4*)(Kr + (size_t)d * KRLEN + c * 8) = o;
  }
}

#define KCPITCH 2080
__global__ __launch_bounds__(256) void conv_kernel(const u16* __restrict__ xT, const u16* __restrict__ Kr,
                                                   const float* __restrict__ kscale, u16* __restrict__ yT) {
  __shared__ __attribute__((aligned(16))) u16 Kc[8 * KCPITCH];
  __shared__ __attribute__((aligned(16))) u16 xs[256 + SEQ];
  const int d = blockIdx.x, t = threadIdx.x, wave = t >> 6, lane = t & 63;
  {
    u32x4 v = *(const u32x4*)(xT + (size_t)d * SEQ + t * 8);
    *(u32x4*)&xs[256 + t * 8] = v;
    if (t < 32) *(u32x4*)&xs[t * 8] = (u32x4){0u, 0u, 0u, 0u};
  }
  for (int c = t; c < 258; c += 256) {
    u32x4 lo = *(const u32x4*)(Kr + (size_t)d * KRLEN + c * 8);
    u32x4 hi = *(const u32x4*)(Kr + (size_t)d * KRLEN + c * 8 + 8);
    u32 wv[8] = {lo[0], lo[1], lo[2], lo[3], hi[0], hi[1], hi[2], hi[3]};
#pragma unroll
    for (int s = 0; s < 8; ++s) {
      u32x4 o;
#pragma unroll
      for (int j = 0; j < 4; ++j) {
        if ((s & 1) == 0) o[j] = wv[j + s / 2];
        else o[j] = __builtin_amdgcn_alignbit(wv[j + s / 2 + 1], wv[j + s / 2], 16);
      }
      *(u32x4*)&Kc[s * KCPITCH + c * 8] = o;
    }
  }
  __syncthreads();
  const int b = lane & 15, g = lane >> 4;
  const int s = (7 - b) & 7;
  f4 acc[8];
#pragma unroll
  for (int i = 0; i < 8; ++i) acc[i] = (f4){0.f, 0.f, 0.f, 0.f};
  const int abase = s * KCPITCH + (b < 8 ? 2024 : 2016) + 8 * g;
  const int bbase = 256 - 16 + 16 * b + 8 * g;
#pragma unroll
  for (int rr = 0; rr < 2; ++rr) {
    const int rp = wave * 2 + rr;
    h8 A[8], B[8];
#pragma unroll
    for (int u = 0; u < 8; ++u) A[u] = *(const h8*)&Kc[abase - 32 * rp - 256 * u];
#pragma unroll
    for (int v = 0; v < 8; ++v) B[v] = *(const h8*)&xs[bbase - 32 * rp + 256 * v];
#pragma unroll
    for (int u = 0; u < 8; ++u)
#pragma unroll
      for (int v = 0; v + u < 8; ++v)
        acc[u + v] = __builtin_amdgcn_mfma_f32_16x16x32_f16(A[u], B[v], acc[u + v], 0, 0, 0);
  }
  __syncthreads();
  float* red = (float*)Kc;
#pragma unroll
  for (int tt = 0; tt < 8; ++tt) *(f4*)&red[((wave * 8 + tt) * 64 + lane) * 4] = acc[tt];
  __syncthreads();
  const float sc = kscale[d];
#pragma unroll
  for (int i = 0; i < 2; ++i) {
    const int tt = wave * 2 + i;
    f4 s0 = *(const f4*)&red[((0 * 8 + tt) * 64 + lane) * 4];
    f4 s1 = *(const f4*)&red[((1 * 8 + tt) * 64 + lane) * 4];
    f4 s2 = *(const f4*)&red[((2 * 8 + tt) * 64 + lane) * 4];
    f4 s3 = *(const f4*)&red[((3 * 8 + tt) * 64 + lane) * 4];
    f4 sum = ((s0 + s1) + (s2 + s3)) * sc;
    u32x2 o;
    o[0] = (u32)f2bf(sum[0]) | ((u32)f2bf(sum[1]) << 16);
    o[1] = (u32)f2bf(sum[2]) | ((u32)f2bf(sum[3]) << 16);
    *(u32x2*)(yT + (size_t)d * SEQ + 256 * tt + 16 * b + 4 * g) = o;
  }
}

__device__ __forceinline__ float mish_bf(float h) {
  float e = bfr(expf(-fabsf(h)));
  float l = bfr(log1pf(e));
  float sp = bfr(l + (h > 0.f ? h : 0.f));
  float th = bfr(tanhf(sp));
  return bfr(h * th);
}

template <int BM, int BN, int KT, int NT, int EPI>
__global__ __launch_bounds__(256) void gemm_kernel(const u16* __restrict__ A, const u16* __restrict__ B,
                                                   const u16* __restrict__ bias, u16* __restrict__ out,
                                                   const u16* __restrict__ xin, const u16* __restrict__ xn,
                                                   const u16* __restrict__ yT, const u16* __restrict__ dskip) {
  constexpr int BK = 64;
  constexpr int AP = BK + 8;
  constexpr int BP = BN + 8;
  constexpr int MT = BM / 64;
  constexpr int NTL = BN / 16;
  constexpr int NKT = KT / BK;
  constexpr int ACH = BM * BK / 8 / 256;
  constexpr int BCHT = BK * BN / 8;
  constexpr int BCH = (BCHT + 255) / 256;
  __shared__ __attribute__((aligned(16))) u16 As[2][BM * AP];
  __shared__ __attribute__((aligned(16))) u16 Bs[2][BK * BP];
  const int t = threadIdx.x, wave = t >> 6, lane = t & 63;
  const int nblk = (NT + BN - 1) / BN;
  const int bm = blockIdx.x / nblk, bn = blockIdx.x % nblk;
  const int m0 = bm * BM;
  int n0 = bn * BN;
  if (n0 + BN > NT) n0 = NT - BN;
  u32x4 ra[ACH], rb[BCH];
  auto gload = [&](int kt) {
#pragma unroll
    for (int i = 0; i < ACH; ++i) {
      int c = t + 256 * i;
      int r = c >> 3, cc = c & 7;
      ra[i] = *(const u32x4*)(A + (size_t)(m0 + r) * KT + kt * BK + cc * 8);
    }
#pragma unroll
    for (int i = 0; i < BCH; ++i) {
      int c = t + 256 * i;
      if (c < BCHT) {
        int r = c / (BN / 8), cc = c % (BN / 8);
        rb[i] = *(const u32x4*)(B + (size_t)(kt * BK + r) * NT + n0 + cc * 8);
      }
    }
  };
  auto swrite = [&](int buf) {
#pragma unroll
    for (int i = 0; i < ACH; ++i) {
      int c = t + 256 * i;
      int r = c >> 3, cc = c & 7;
      *(u32x4*)&As[buf][r * AP + cc * 8] = ra[i];
    }
#pragma unroll
    for (int i = 0; i < BCH; ++i) {
      int c = t + 256 * i;
      if (c < BCHT) {
        int r = c / (BN / 8), cc = c % (BN / 8);
        *(u32x4*)&Bs[buf][r * BP + cc * 8] = rb[i];
      }
    }
  };
  f4 acc[MT][NTL];
#pragma unroll
  for (int i = 0; i < MT; ++i)
#pragma unroll
    for (int j = 0; j < NTL; ++j) acc[i][j] = (f4){0.f, 0.f, 0.f, 0.f};
  const int fr = lane & 15, g = lane >> 4;
  const int tq = (lane & 15) >> 2, tp = lane & 3;
  gload(0);
  swrite(0);
  __syncthreads();
  for (int kt = 0; kt < NKT; ++kt) {
    const int buf = kt & 1;
    if (kt + 1 < NKT) gload(kt + 1);
#pragma unroll
    for (int ks = 0; ks < 2; ++ks) {
      s8 af[MT];
#pragma unroll
      for (int i = 0; i < MT; ++i)
        af[i] = *(const s8*)&As[buf][(wave * (BM / 4) + i * 16 + fr) * AP + ks * 32 + g * 8];
#pragma unroll
      for (int j = 0; j < NTL; ++j) {
        const u16* p0 = &Bs[buf][(ks * 32 + 8 * g + tq) * BP + j * 16 + tp * 4];
        s4 b0 = __builtin_amdgcn_ds_read_tr16_b64_v4i16((__attribute__((address_space(3))) s4*)p0);
        s4 b1 = __builtin_amdgcn_ds_read_tr16_b64_v4i16((__attribute__((address_space(3))) s4*)(p0 + 4 * BP));
        s8 bf = {b0[0], b0[1], b0[2], b0[3], b1[0], b1[1], b1[2], b1[3]};
#pragma unroll
        for (int i = 0; i < MT; ++i)
          acc[i][j] = __builtin_amdgcn_mfma_f32_16x16x32_bf16(bf, af[i], acc[i][j], 0, 0, 0);
      }
    }
    if (kt + 1 < NKT) swrite(buf ^ 1);
    __syncthreads();
  }
#pragma unroll
  for (int i = 0; i < MT; ++i) {
    const int m = m0 + wave * (BM / 4) + i * 16 + fr;
#pragma unroll
    for (int j = 0; j < NTL; ++j) {
      const int n = n0 + j * 16 + 4 * g;
      float o[4];
      if (EPI == 0) {
        u32x2 bb = *(const u32x2*)(bias + n);
        float bv[4] = {__uint_as_float(bb[0] << 16), __uint_as_float(bb[0] & 0xFFFF0000u),
                       __uint_as_float(bb[1] << 16), __uint_as_float(bb[1] & 0xFFFF0000u)};
#pragma unroll
        for (int r = 0; r < 4; ++r) {
          float h = bfr(bfr(acc[i][j][r]) + bv[r]);
          o[r] = mish_bf(h);
        }
      } else {
        u32x2 xx = *(const u32x2*)(xin + (size_t)m * NT + n);
        u32x2 nn = *(const u32x2*)(xn + (size_t)m * NT + n);
        u32x2 dd = *(const u32x2*)(dskip + n);
        float xv[4] = {__uint_as_float(xx[0] << 16), __uint_as_float(xx[0] & 0xFFFF0000u),
                       __uint_as_float(xx[1] << 16), __uint_as_float(xx[1] & 0xFFFF0000u)};
        float nv[4] = {__uint_as_float(nn[0] << 16), __uint_as_float(nn[0] & 0xFFFF0000u),
                       __uint_as_float(nn[1] << 16), __uint_as_float(nn[1] & 0xFFFF0000u)};
        float dv[4] = {__uint_as_float(dd[0] << 16), __uint_as_float(dd[0] & 0xFFFF0000u),
                       __uint_as_float(dd[1] << 16), __uint_as_float(dd[1] & 0xFFFF0000u)};
#pragma unroll
        for (int r = 0; r < 4; ++r) {
          float y = bf2f(yT[(size_t)(n + r) * SEQ + m]);
          float a = bfr(y + bfr(nv[r] * dv[r]));
          float s1 = bfr(xv[r] + a);
          o[r] = bfr(s1 + bfr(acc[i][j][r]));
        }
      }
      u32x2 ov;
      ov[0] = (__float_as_uint(o[0]) >> 16) | (__float_as_uint(o[1]) & 0xFFFF0000u);
      ov[1] = (__float_as_uint(o[2]) >> 16) | (__float_as_uint(o[3]) & 0xFFFF0000u);
      *(u32x2*)(out + (size_t)m * NT + n) = ov;
    }
  }
}

extern "C" void kernel_launch(void* const* d_in, const int* in_sizes, int n_in,
                              void* d_out, int out_size, void* d_ws, size_t ws_size,
                              hipStream_t stream) {
  const u16* u = (const u16*)d_in[0];
  const u16* lar = (const u16*)d_in[1];
  const u16* aim = (const u16*)d_in[2];
  const u16* cre = (const u16*)d_in[3];
  const u16* cim = (const u16*)d_in[4];
  const u16* logdt = (const u16*)d_in[5];
  const u16* dskip = (const u16*)d_in[6];
  const u16* w1 = (const u16*)d_in[8];
  const u16* b1 = (const u16*)d_in[9];
  const u16* w2 = (const u16*)d_in[10];
  u16* out = (u16*)d_out;
  char* ws = (char*)d_ws;
  if (ws_size < (16u << 20) + (size_t)SEQ * FF * 2) return;
  u16* xn = (u16*)(ws);
  u16* xT = (u16*)(ws + (4u << 20));
  u16* Kr = (u16*)(ws + (8u << 20));
  float* ksc = (float*)(ws + (12u << 20));
  u16* yT = (u16*)(ws + (13u << 20));
  u16* act = (u16*)(ws + (16u << 20));

  ln_kernel<<<SEQ / 16, 256, 0, stream>>>(u, xn, xT);
  kgen_kernel<<<DM, 256, 0, stream>>>(lar, aim, cre, cim, logdt, Kr, ksc);
  conv_kernel<<<DM, 256, 0, stream>>>(xT, Kr, ksc, yT);
  gemm_kernel<128, 112, DM, FF, 0><<<16 * 16, 256, 0, stream>>>(xn, w1, b1, act, nullptr, nullptr, nullptr, nullptr);
  gemm_kernel<64, 80, FF, DM, 1><<<32 * 8, 256, 0, stream>>>(act, w2, nullptr, out, u, xn, yT, dskip);
}
```

```cpp
#include <hip/hip_runtime.h>
#include <hip/hip_bf16.h>

#define SEQ 2048
#define DM 640
#define NS 128
#define FF 1728

typedef unsigned short u16;
typedef short s4 __attribute__((ext_vector_type(4)));
typedef short s8 __attribute__((ext_vector_type(8)));
typedef _Float16 h8 __attribute__((ext_vector_type(8)));
typedef float f4 __attribute__((ext_vector_type(4)));
typedef unsigned int u32;
typedef unsigned int u32x4 __attribute__((ext_vector_type(4)));
typedef unsigned int u32x2 __attribute__((ext_vector_type(2)));

__device__ __forceinline__ float bf2f(u16 v) { return __uint_as_float(((u32)v) << 16); }
__device__ __forceinline__ u16 f2bf(float f) {
  u32 u = __float_as_uint(f);
  u32 r = (u + 0x7FFFu + ((u >> 16) & 1u)) >> 16;
  return (u16)r;
}
__device__ __forceinline__ float bfr(float f) { return bf2f(f2bf(f)); }

typedef __bf16 bf2_t __attribute__((ext_vector_type(2)));
typedef float f2_t __attribute__((ext_vector_type(2)));
__device__ __forceinline__ u32 pk_bf(float a, float b) {
  f2_t v = {a, b};
  bf2_t r = __builtin_convertvector(v, bf2_t);
  return __builtin_bit_cast(u32, r);
}
__device__ __forceinline__ float lo_f(u32 w) { return __uint_as_float(w << 16); }
__device__ __forceinline__ float hi_f(u32 w) { return __uint_as_float(w & 0xFFFF0000u); }

#define WAIT_V(n) asm volatile("s_waitcnt vmcnt(%0)" ::"n"(n) : "memory")
#define RAW_BARRIER() do { asm volatile("s_waitcnt lgkmcnt(0)" ::: "memory"); __builtin_amdgcn_s_barrier(); } while (0)
__device__ __forceinline__ void glds16(const void* gsrc, const void* lp) {
  unsigned keep;
  unsigned lds_dst = (unsigned)(size_t)(const __attribute__((address_space(3))) char*)lp;
  asm volatile("s_mov_b32 %0, m0\n\ts_mov_b32 m0, %2\n\ts_nop 0\n\tglobal_load_lds_dwordx4 %1, off\n\ts_mov_b32 m0, %0"
               : "=&s"(keep) : "v"(gsrc), "s"(lds_dst) : "memory");
}
#define GLDS(gp, lp) glds16((const void*)(gp), (const void*)(lp))

#define LUT_E0 111
#define LUT_E1 134
#define LUT_HALF ((LUT_E1 - LUT_E0) * 128)
#define LUT_N (2 * LUT_HALF)

__device__ __forceinline__ float mish_bf(float h) {
  float e = bfr(expf(-fabsf(h)));
  float l = bfr(log1pf(e));
  float sp = bfr(l + (h > 0.f ? h : 0.f));
  float th = bfr(tanhf(sp));
  return bfr(h * th);
}

__global__ __launch_bounds__(256) void ln_kernel(const u16* __restrict__ u, u16* __restrict__ xnP,
                                                 u16* __restrict__ xT, float2* __restrict__ stats,
                                                 u16* __restrict__ lut) {
  __shared__ __attribute__((aligned(16))) u16 tile[16][DM + 8];
  if (blockIdx.x >= SEQ / 16) {
    int idx = (blockIdx.x - SEQ / 16) * 256 + threadIdx.x;
    if (idx < LUT_N) {
      int sgn = idx >= LUT_HALF;
      int a = idx - sgn * LUT_HALF + (LUT_E0 << 7);
      float h = bf2f((u16)(a | (sgn << 15)));
      lut[idx] = f2bf(mish_bf(h));
    } else if (idx < 6144) {
      lut[idx] = 0;
    }
    return;
  }
  const int wave = threadIdx.x >> 6, lane = threadIdx.x & 63;
  const int r = wave * 4 + (lane >> 4), q = lane & 15;
  const int row = blockIdx.x * 16 + r;
  float v[40];
  const u32x4* src = (const u32x4*)(u + (size_t)row * DM);
#pragma unroll
  for (int i = 0; i < 5; ++i) {
    u32x4 w = src[q + 16 * i];
#pragma unroll
    for (int j = 0; j < 4; ++j) {
      v[i * 8 + 2 * j] = lo_f(w[j]);
      v[i * 8 + 2 * j + 1] = hi_f(w[j]);
    }
  }
  float s = 0.f;
#pragma unroll
  for (int i = 0; i < 40; ++i) s += v[i];
#pragma unroll
  for (int o = 1; o < 16; o <<= 1) s += __shfl_xor(s, o);
  const float mean = s * (1.0f / DM);
  float ss = 0.f;
#pragma unroll
  for (int i = 0; i < 40; ++i) { float d = v[i] - mean; ss += d * d; }
#pragma unroll
  for (int o = 1; o < 16; o <<= 1) ss += __shfl_xor(ss, o);
  const float var = ss * (1.0f / DM);
  const float mb = bfr(mean);
  const float vb = bfr(var);
  const float den = bfr(sqrtf(bfr(vb + 1e-5f)));
  if (q == 0) stats[row] = make_float2(mb, den);
#pragma unroll
  for (int i = 0; i < 5; ++i) {
    u32x4 w;
#pragma unroll
    for (int j = 0; j < 4; ++j) {
      u32 dd = pk_bf(v[i * 8 + 2 * j] - mb, v[i * 8 + 2 * j + 1] - mb);
      w[j] = pk_bf(lo_f(dd) / den, hi_f(dd) / den);
    }
    *(u32x4*)&tile[r][(q + 16 * i) * 8] = w;
  }
  __syncthreads();
#pragma unroll
  for (int i = 0; i < 5; ++i) {
    int idx = threadIdx.x + 256 * i;
    int kb = idx >> 6, ln = idx & 63, g = ln >> 4, fr = ln & 15;
    u32x2 a = *(const u32x2*)&tile[fr][kb * 32 + 4 * g];
    u32x2 b = *(const u32x2*)&tile[fr][kb * 32 + 16 + 4 * g];
    u32x4 w = {a[0], a[1], b[0], b[1]};
    *(u32x4*)(xnP + ((size_t)blockIdx.x * 1280 + idx) * 8) = w;
  }
#pragma unroll
  for (int i = 0; i < 5; ++i) {
    int idx = threadIdx.x + 256 * i;
    int d = idx >> 1, c = idx & 1;
    u32x4 w;
#pragma unroll
    for (int j = 0; j < 4; ++j) {
      _Float16 ha = (_Float16)bf2f(tile[c * 8 + 2 * j][d]);
      _Float16 hb = (_Float16)bf2f(tile[c * 8 + 2 * j + 1][d]);
      w[j] = (u32)__builtin_bit_cast(u16, ha) | ((u32)__builtin_bit_cast(u16, hb) << 16);
    }
    *(u32x4*)(xT + (size_t)d * SEQ + blockIdx.x * 16 + c * 8) = w;
  }
}

#define KRLEN 2080
__device__ __forceinline__ void cexp_p(float arl2, float w, float p, float& re, float& im) {
  float mag = __builtin_amdgcn_exp2f(arl2 * p);
  float rev = w * p;
  rev = rev - floorf(rev);
  re = mag * __builtin_amdgcn_cosf(rev);
  im = mag * __builtin_amdgcn_sinf(rev);
}

__global__ __launch_bounds__(256) void kgen_kernel(const u16* __restrict__ lar, const u16* __restrict__ aim,
                                                   const u16* __restrict__ cre, const u16* __restrict__ cim,
                                                   const u16* __restrict__ logdt, u16* __restrict__ Kr,
                                                   float* __restrict__ kscale) {
  __shared__ __attribute__((aligned(16))) float Ah[48][260];
  __shared__ __attribute__((aligned(16))) float Bl[16][260];
  __shared__ __attribute__((aligned(16))) float red[4][768];
  __shared__ float Kp[768];
  __shared__ float wmax[4];
  const int d = blockIdx.x, t = threadIdx.x, n = t & 127, half = t >> 7;
  const int wave = t >> 6, lane = t & 63;
  const float dt = bfr(expf(bf2f(logdt[d])));
  const float ar = -bfr(expf(bf2f(lar[d * NS + n]))) * dt;
  const float ai = bf2f(aim[d * NS + n]) * dt;
  const float cr = bf2f(cre[d * NS + n]) * dt;
  const float ci = bf2f(cim[d * NS + n]) * dt;
  const float arl2 = ar * 1.4426950408889634f;
  const float w = ai * 0.15915494309189535f;
  const int r0 = half ? 21 : 0, r1 = half ? 41 : 21;
  for (int row = r0; row < r1; ++row) {
    int p;
    if (row < 16) p = (row >> 3) * 128 + (row & 7);
    else if (row < 24) p = (2 + ((row - 16) >> 2)) * 128 + ((row - 16) & 3) * 2;
    else if (row < 32) p = (4 + ((row - 24) >> 1)) * 128 + ((row - 24) & 1) * 4;
    else p = (8 + (row - 32)) * 128;
    float er, ei;
    cexp_p(arl2, w, (float)p, er, ei);
    float hr = cr * er - ci * ei;
    float hi = cr * ei + ci * er;
    Ah[row][n] = hr;
    Ah[row][128 + n] = -hi;
  }
  if (half) {
    for (int row = 41; row < 48; ++row) { Ah[row][n] = 0.f; Ah[row][128 + n] = 0.f; }
  }
  for (int j = half * 8; j < half * 8 + 8; ++j) {
    float er, ei;
    cexp_p(arl2, w, (float)(8 * j), er, ei);
    Bl[j][n] = er;
    Bl[j][128 + n] = ei;
  }
  __syncthreads();
  {
    const int i = lane & 15, g = lane >> 4;
    f4 acc[3];
#pragma unroll
    for (int tt = 0; tt < 3; ++tt) acc[tt] = (f4){0.f, 0.f, 0.f, 0.f};
#pragma unroll
    for (int qq = 0; qq < 4; ++qq) {
      const int k0 = 64 * wave + 16 * qq + 4 * g;
      f4 b = *(const f4*)&Bl[i][k0];
#pragma unroll
      for (int tt = 0; tt < 3; ++tt) {
        f4 a = *(const f4*)&Ah[tt * 16 + i][k0];
#pragma unroll
        for (int e = 0; e < 4; ++e) acc[tt] = __builtin_amdgcn_mfma_f32_16x16x4f32(a[e], b[e], acc[tt], 0, 0, 0);
      }
    }
#pragma unroll
    for (int tt = 0; tt < 3; ++tt)
#pragma unroll
      for (int r = 0; r < 4; ++r) red[wave][(tt * 16 + 4 * g + r) * 16 + i] = acc[tt][r];
  }
  __syncthreads();
  float mx = 0.f;
#pragma unroll
  for (int i = 0; i < 3; ++i) {
    int idx = t + 256 * i;
    float s = 2.0f * (((red[0][idx] + red[1][idx]) + red[2][idx]) + red[3][idx]);
    Kp[idx] = s;
    mx = fmaxf(mx, fabsf(s));
  }
#pragma unroll
  for (int o = 1; o < 64; o <<= 1) mx = fmaxf(mx, __shfl_xor(mx, o));
  if (lane == 0) wmax[wave] = mx;
  __syncthreads();
  mx = fmaxf(fmaxf(wmax[0], wmax[1]), fmaxf(wmax[2], wmax[3]));
  int ex = 0;
  if (mx > 0.f && mx < 3.0e38f) { (void)frexpf(mx, &ex); }
  int e = 15 - ex;
  e = e > 100 ? 100 : (e < -100 ? -100 : e);
  const float sc = ldexpf(1.0f, e);
  if (t == 0) kscale[d] = ldexpf(1.0f, -e);
  for (int c = t; c < KRLEN / 8; c += 256) {
    u32x4 o;
#pragma unroll
    for (int j = 0; j < 4; ++j) {
      u32 pk = 0;
#pragma unroll
      for (int h = 0; h < 2; ++h) {
        int tt = c * 8 + 2 * j + h;
        int l = 2047 - tt;
        float val = 0.f;
        if (l >= 0) {
          int p = (int)bfr((float)l);
          int col = (p >> 3) & 15, ii = p >> 7, r = p & 7;
          int row;
          if (ii < 2) row = ii * 8 + r;
          else if (ii < 4) row = 16 + (ii - 2) * 4 + (r >> 1);
          else if (ii < 8) row = 24 + (ii - 4) * 2 + (r >> 2);
          else row = 32 + (ii - 8);
          val = Kp[row * 16 + col] * sc;
        }
        _Float16 hv = (_Float16)val;
        pk |= ((u32)__builtin_bit_cast(u16, hv)) << (16 * h);
      }
      o[j] = pk;
    }
    *(u32x4*)(Kr + (size_t)d * KRLEN + c * 8) = o;
  }
}

#define KCPITCH 2080
__global__ __launch_bounds__(256) void conv_kernel(const u16* __restrict__ xT, const u16* __restrict__ Kr,
                                                   const float* __restrict__ kscale, u16* __restrict__ yT) {
  __shared__ __attribute__((aligned(16))) u16 Kc[8 * KCPITCH];
  __shared__ __attribute__((aligned(16))) u16 xs[256 + SEQ];
  const int d = blockIdx.x, t = threadIdx.x, wave = t >> 6, lane = t & 63;
  {
    u32x4 v = *(const u32x4*)(xT + (size_t)d * SEQ + t * 8);
    *(u32x4*)&xs[256 + t * 8] = v;
    if (t < 32) *(u32x4*)&xs[t * 8] = (u32x4){0u, 0u, 0u, 0u};
  }
  for (int c = t; c < 258; c += 256) {
    u32x4 lo = *(const u32x4*)(Kr + (size_t)d * KRLEN + c * 8);
    u32x4 hi = *(const u32x4*)(Kr + (size_t)d * KRLEN + c * 8 + 8);
    u32 wv[8] = {lo[0], lo[1], lo[2], lo[3], hi[0], hi[1], hi[2], hi[3]};
#pragma unroll
    for (int s = 0; s < 8; ++s) {
      u32x4 o;
#pragma unroll
      for (int j = 0; j < 4; ++j) {
        if ((s & 1) == 0) o[j] = wv[j + s / 2];
        else o[j] = __builtin_amdgcn_alignbit(wv[j + s / 2 + 1], wv[j + s / 2], 16);
      }
      *(u32x4*)&Kc[s * KCPITCH + c * 8] = o;
    }
  }
  __syncthreads();
  const int b = lane & 15, g = lane >> 4;
  const int s = (7 - b) & 7;
  f4 acc[8];
#pragma unroll
  for (int i = 0; i < 8; ++i) acc[i] = (f4){0.f, 0.f, 0.f, 0.f};
  const int abase = s * KCPITCH + (b < 8 ? 2024 : 2016) + 8 * g;
  const int bbase = 256 - 16 + 16 * b + 8 * g;
#pragma unroll
  for (int rr = 0; rr < 2; ++rr) {
    const int rp = wave * 2 + rr;
    h8 A[8], B[8];
#pragma unroll
    for (int u = 0; u < 8; ++u) A[u] = *(const h8*)&Kc[abase - 32 * rp - 256 * u];
#pragma unroll
    for (int v = 0; v < 8; ++v) B[v] = *(const h8*)&xs[bbase - 32 * rp + 256 * v];
#pragma unroll
    for (int u = 0; u < 8; ++u)
#pragma unroll
      for (int v = 0; v + u < 8; ++v)
        acc[u + v] = __builtin_amdgcn_mfma_f32_16x16x32_f16(A[u], B[v], acc[u + v], 0, 0, 0);
  }
  __syncthreads();
  float* red = (float*)Kc;
#pragma unroll
  for (int tt = 0; tt < 8; ++tt) *(f4*)&red[((wave * 8 + tt) * 64 + lane) * 4] = acc[tt];
  __syncthreads();
  const float sc = kscale[d];
#pragma unroll
  for (int i = 0; i < 2; ++i) {
    const int tt = wave * 2 + i;
    f4 s0 = *(const f4*)&red[((0 * 8 + tt) * 64 + lane) * 4];
    f4 s1 = *(const f4*)&red[((1 * 8 + tt) * 64 + lane) * 4];
    f4 s2 = *(const f4*)&red[((2 * 8 + tt) * 64 + lane) * 4];
    f4 s3 = *(const f4*)&red[((3 * 8 + tt) * 64 + lane) * 4];
    f4 sum = ((s0 + s1) + (s2 + s3)) * sc;
    u32x2 o;
    o[0] = (u32)f2bf(sum[0]) | ((u32)f2bf(sum[1]) << 16);
    o[1] = (u32)f2bf(sum[2]) | ((u32)f2bf(sum[3]) << 16);
    *(u32x2*)(yT + (size_t)d * SEQ + 256 * tt + 16 * b + 4 * g) = o;
  }
}

#define TRREAD(p) __builtin_amdgcn_ds_read_tr16_b64_v4i16((__attribute__((address_space(3))) s4*)(p))

template <int ND>
__global__ __launch_bounds__(256) void gemm1_kernel(const u16* __restrict__ xnP, const u16* __restrict__ w1,
                                                    const u16* __restrict__ b1, const u16* __restrict__ lut,
                                                    u16* __restrict__ actP) {
  constexpr int STG = 30 * 1024;
  constexpr int AUX = ND * STG;
  constexpr int NKT = DM / 64;
  __shared__ __attribute__((aligned(1024))) char shm[ND * STG + 13 * 1024];
  const int t = threadIdx.x, lane = t & 63;
  const int wave = __builtin_amdgcn_readfirstlane(t >> 6);
  const int xcd = blockIdx.x & 7, idx = blockIdx.x >> 3;
  const int bm = (xcd >> 1) * 4 + (idx & 3), bn = (xcd & 1) * 8 + (idx >> 2);
  const int mt0 = bm * 8;
  int n0 = bn * 112;
  if (n0 + 112 > FF) n0 = FF - 112;
#pragma unroll
  for (int i = 0; i < 3; ++i) GLDS(lut + (wave + 4 * i) * 512 + lane * 8, shm + AUX + (wave + 4 * i) * 1024);
  if (wave == 0) GLDS(b1 + n0 + (lane < 14 ? lane : 13) * 8, shm + AUX + 12 * 1024);
  const u16* pa[4];
  const u16* pb[4];
#pragma unroll
  for (int i = 0; i < 4; ++i) {
    int p = wave + 4 * i;
    pa[i] = xnP + ((size_t)(mt0 + (p >> 1)) * 20 + (p & 1)) * 512 + lane * 8;
    int c = p * 64 + lane;
    if (c > 14 * 64 - 1) c = 14 * 64 - 1;
    int rr = c / 14, cc = c % 14;
    pb[i] = w1 + (size_t)rr * FF + n0 + cc * 8;
  }
  auto stage = [&](int kt, int buf) {
    char* sb = shm + buf * STG;
#pragma unroll
    for (int i = 0; i < 4; ++i) GLDS(pa[i] + kt * 1024, sb + (wave + 4 * i) * 1024);
#pragma unroll
    for (int i = 0; i < 3; ++i) GLDS(pb[i] + (size_t)kt * 64 * FF, sb + 16384 + (wave + 4 * i) * 1024);
    if (wave < 2) GLDS(pb[3] + (size_t)kt * 64 * FF, sb + 16384 + (wave + 12) * 1024);
  };
  f4 acc[2][7];
#pragma unroll
  for (int i = 0; i < 2; ++i)
#pragma unroll
    for (int j = 0; j < 7; ++j) acc[i][j] = (f4){0.f, 0.f, 0.f, 0.f};
  const int fr = lane & 15, g = lane >> 4, tq = (lane & 15) >> 2, tp = lane & 3;
  const int aoff = wave * 4096 + lane * 16;
  const int boff = 16384 + (4 * g + tq) * 224 + tp * 8;
#pragma unroll
  for (int s = 0; s < ND - 1; ++s) stage(s, s);
#pragma unroll
  for (int kt = 0; kt < NKT; ++kt) {
    constexpr int dummy = 0;
    const int rem = (NKT - 1 - kt) < (ND - 2) ? (NKT - 1 - kt) : (ND - 2);
    if (wave < 2) {
      switch (rem) { case 0: WAIT_V(0); break; case 1: WAIT_V(8); break; case 2: WAIT_V(16); break; case 3: WAIT_V(24); break; default: WAIT_V(32); }
    } else {
      switch (rem) { case 0: WAIT_V(0); break; case 1: WAIT_V(7); break; case 2: WAIT_V(14); break; case 3: WAIT_V(21); break; default: WAIT_V(28); }
    }
    RAW_BARRIER();
    if (kt + ND - 1 < NKT) stage(kt + ND - 1, (kt + ND - 1) % ND);
    const char* sb = shm + (kt % ND) * STG;
#pragma unroll
    for (int ks = 0; ks < 2; ++ks) {
      s8 af[2];
#pragma unroll
      for (int i = 0; i < 2; ++i) af[i] = *(const s8*)(sb + aoff + i * 2048 + ks * 1024);
#pragma unroll
      for (int j = 0; j < 7; ++j) {
        const char* p0 = sb + boff + ks * 32 * 224 + j * 32;
        s4 b0 = TRREAD(p0);
        s4 b1v = TRREAD(p0 + 16 * 224);
        s8 bf = {b0[0], b0[1], b0[2], b0[3], b1v[0], b1v[1], b1v[2], b1v[3]};
#pragma unroll
        for (int i = 0; i < 2; ++i)
          acc[i][j] = __builtin_amdgcn_mfma_f32_16x16x32_bf16(bf, af[i], acc[i][j], 0, 0, 0);
      }
    }
  }
  const u16* slut = (const u16*)(shm + AUX);
  const u16* sbias = (const u16*)(shm + AUX + 12 * 1024);
#pragma unroll
  for (int j = 0; j < 7; ++j) {
    u32x2 bb = *(const u32x2*)(sbias + j * 16 + 4 * g);
    const float bv[4] = {lo_f(bb[0]), hi_f(bb[0]), lo_f(bb[1]), hi_f(bb[1])};
    const int n = n0 + j * 16;
    const int kb = n >> 5, half = (n >> 4) & 1;
#pragma unroll
    for (int i = 0; i < 2; ++i) {
      u32 t0 = pk_bf(acc[i][j][0], acc[i][j][1]);
      u32 t1 = pk_bf(acc[i][j][2], acc[i][j][3]);
      u32 h0 = pk_bf(lo_f(t0) + bv[0], hi_f(t0) + bv[1]);
      u32 h1 = pk_bf(lo_f(t1) + bv[2], hi_f(t1) + bv[3]);
      u32 hb[4] = {h0 & 0xFFFFu, h0 >> 16, h1 & 0xFFFFu, h1 >> 16};
      u32 res[4];
#pragma unroll
      for (int r = 0; r < 4; ++r) {
        u32 a = hb[r] & 0x7FFFu;
        u32 li = a - (LUT_E0 << 7);
        bool in = li < (u32)LUT_HALF;
        u32 v = slut[(in ? li : 0u) + (hb[r] >> 15) * LUT_HALF];
        float h = __uint_as_float(hb[r] << 16);
        float fb = (a < (LUT_E0 << 7)) ? h * 0.59765625f : (h > 0.f ? h : h * 0.0f);
        u32 fbb = pk_bf(fb, fb) & 0xFFFFu;
        res[r] = in ? v : fbb;
      }
      u32x2 ov = {res[0] | (res[1] << 16), res[2] | (res[3] << 16)};
      const int mt = mt0 + wave * 2 + i;
      *(u32x2*)(actP + (((size_t)mt * 54 + kb) * 64 + lane) * 8 + half * 4) = ov;
    }
  }
}

template <int ND>
__global__ __launch_bounds__(256) void gemm2_kernel(const u16* __restrict__ actP, const u16* __restrict__ w2,
                                                    const u16* __restrict__ xin, const float2* __restrict__ stats,
                                                    const u16* __restrict__ yT, const u16* __restrict__ dskip,
                                                    u16* __restrict__ out) {
  constexpr int STG = 18 * 1024;
  constexpr int AUX = ND * STG;
  constexpr int NKT = FF / 64;
  __shared__ __attribute__((aligned(1024))) char shm[ND * STG + 21 * 1024];
  const int t = threadIdx.x, lane = t & 63;
  const int wave = __builtin_amdgcn_readfirstlane(t >> 6);
  const int xcd = blockIdx.x & 7, idx = blockIdx.x >> 3;
  const int bm = (xcd >> 1) * 8 + (idx & 7), bn = (xcd & 1) * 4 + (idx >> 3);
  const int mt0 = bm * 4, m0 = bm * 64, n0 = bn * 80;
#pragma unroll
  for (int i = 0; i < 5; ++i) {
    int p = wave + 4 * i;
    int c = (p < 10 ? p : p - 10) * 64 + lane;
    const u16* src;
    if (p < 10) src = xin + (size_t)(m0 + c / 10) * DM + n0 + (c % 10) * 8;
    else src = yT + (size_t)(n0 + (c >> 3)) * SEQ + m0 + (c & 7) * 8;
    GLDS(src, shm + AUX + p * 1024);
  }
  if (wave == 0) {
    const u16* src = (lane < 32) ? (const u16*)(stats + m0) + lane * 8 : dskip + n0 + (lane < 42 ? lane - 32 : 9) * 8;
    GLDS(src, shm + AUX + 20 * 1024);
  }
  const u16* pa[2];
  const u16* pb[3];
#pragma unroll
  for (int i = 0; i < 2; ++i) {
    int p = wave + 4 * i;
    pa[i] = actP + ((size_t)(mt0 + (p >> 1)) * 54 + (p & 1)) * 512 + lane * 8;
  }
#pragma unroll
  for (int i = 0; i < 3; ++i) {
    int c = (wave + 4 * i) * 64 + lane;
    if (c > 639) c = 639;
    pb[i] = w2 + (size_t)(c / 10) * DM + n0 + (c % 10) * 8;
  }
  auto stage = [&](int kt, int buf) {
    char* sb = shm + buf * STG;
#pragma unroll
    for (int i = 0; i < 2; ++i) GLDS(pa[i] + kt * 1024, sb + (wave + 4 * i) * 1024);
#pragma unroll
    for (int i = 0; i < 2; ++i) GLDS(pb[i] + (size_t)kt * 64 * DM, sb + 8192 + (wave + 4 * i) * 1024);
    if (wave < 2) GLDS(pb[2] + (size_t)kt * 64 * DM, sb + 8192 + (wave + 8) * 1024);
  };
  f4 acc[5];
#pragma unroll
  for (int j = 0; j < 5; ++j) acc[j] = (f4){0.f, 0.f, 0.f, 0.f};
  const int fr = lane & 15, g = lane >> 4, tq = (lane & 15) >> 2, tp = lane & 3;
  const int aoff = wave * 2048 + lane * 16;
  const int boff = 8192 + (4 * g + tq) * 160 + tp * 8;
#pragma unroll
  for (int s = 0; s < ND - 1; ++s) stage(s, s);
#pragma unroll
  for (int kt = 0; kt < NKT; ++kt) {
    const int rem = (NKT - 1 - kt) < (ND - 2) ? (NKT - 1 - kt) : (ND - 2);
    if (wave < 2) {
      switch (rem) { case 0: WAIT_V(0); break; case 1: WAIT_V(5); break; case 2: WAIT_V(10); break; case 3: WAIT_V(15); break;
                     case 4: WAIT_V(20); break; case 5: WAIT_V(25); break; default: WAIT_V(30); }
    } else {
      switch (rem) { case 0: WAIT_V(0); break; case 1: WAIT_V(4); break; case 2: WAIT_V(8); break; case 3: WAIT_V(12); break;
                     case 4: WAIT_V(16); break; case 5: WAIT_V(20); break; default: WAIT_V(24); }
    }
    RAW_BARRIER();
    if (kt + ND - 1 < NKT) stage(kt + ND - 1, (kt + ND - 1) % ND);
    const char* sb = shm + (kt % ND) * STG;
#pragma unroll
    for (int ks = 0; ks < 2; ++ks) {
      s8 af = *(const s8*)(sb + aoff + ks * 1024);
#pragma unroll
      for (int j = 0; j < 5; ++j) {
        const char* p0 = sb + boff + ks * 32 * 160 + j * 32;
        s4 b0 = TRREAD(p0);
        s4 b1v = TRREAD(p0 + 16 * 160);
        s8 bf = {b0[0], b0[1], b0[2], b0[3], b1v[0], b1v[1], b1v[2], b1v[3]};
        acc[j] = __builtin_amdgcn_mfma_f32_16x16x32_bf16(bf, af, acc[j], 0, 0, 0);
      }
    }
  }
  const char* sx = shm + AUX;
  const char* sy = shm + AUX + 10 * 1024;
  const char* sm = shm + AUX + 20 * 1024;
  const int ml = wave * 16 + fr;
  const float2 st = *(const float2*)(sm + ml * 8);
#pragma unroll
  for (int j = 0; j < 5; ++j) {
    u32x2 xx = *(const u32x2*)(sx + ml * 160 + (j * 16 + 4 * g) * 2);
    u32x2 dd = *(const u32x2*)(sm + 512 + (j * 16 + 4 * g) * 2);
    s4 yy = TRREAD(sy + (j * 16 + 4 * g + tq) * 128 + (wave * 16 + 4 * tp) * 2);
    const float xv[4] = {lo_f(xx[0]), hi_f(xx[0]), lo_f(xx[1]), hi_f(xx[1])};
    const float dv[4] = {lo_f(dd[0]), hi_f(dd[0]), lo_f(dd[1]), hi_f(dd[1])};
    float o[4];
#pragma unroll
    for (int r = 0; r < 4; ++r) {
      float xn = bfr(bfr(xv[r] - st.x) / st.y);
      float y = bf2f((u16)yy[r]);
      float a = bfr(y + bfr(xn * dv[r]));
      float s1 = bfr(xv[r] + a);
      o[r] = s1 + bfr(acc[j][r]);
    }
    u32x2 ov = {pk_bf(o[0], o[1]), pk_bf(o[2], o[3])};
    *(u32x2*)(out + (size_t)(m0 + ml) * DM + n0 + j * 16 + 4 * g) = ov;
  }
}

extern "C" void kernel_launch(void* const* d_in, const int* in_sizes, int n_in,
                              void* d_out, int out_size, void* d_ws, size_t ws_size,
                              hipStream_t stream) {
  const u16* u = (const u16*)d_in[0];
  const u16* lar = (const u16*)d_in[1];
  const u16* aim = (const u16*)d_in[2];
  const u16* cre = (const u16*)d_in[3];
  const u16* cim = (const u16*)d_in[4];
  const u16* logdt = (const u16*)d_in[5];
  const u16* dskip = (const u16*)d_in[6];
  const u16* w1 = (const u16*)d_in[8];
  const u16* b1 = (const u16*)d_in[9];
  const u16* w2 = (const u16*)d_in[10];
  u16* out = (u16*)d_out;
  char* ws = (char*)d_ws;
  if (ws_size < (16u << 20) + (size_t)SEQ * FF * 2) return;
  u16* xnP = (u16*)(ws);
  u16* xT = (u16*)(ws + (4u << 20));
  u16* Kr = (u16*)(ws + (8u << 20));
  float* ksc = (float*)(ws + (12u << 20));
  float2* stats = (float2*)(ws + (12u << 20) + 4096);
  u16* lut = (u16*)(ws + (12u << 20) + 32768);
  u16* yT = (u16*)(ws + (13u << 20));
  u16* actP = (u16*)(ws + (16u << 20));

  ln_kernel<<<SEQ / 16 + 24, 256, 0, stream>>>(u, xnP, xT, stats, lut);
  kgen_kernel<<<DM, 256, 0, stream>>>(lar, aim, cre, cim, logdt, Kr, ksc);
  conv_kernel<<<DM, 256, 0, stream>>>(xT, Kr, ksc, yT);
  gemm1_kernel<4><<<256, 256, 0, stream>>>(xnP, w1, b1, lut, actP);
  gemm2_kernel<7><<<256, 256, 0, stream>>>(actP, w2, u, stats, yT, dskip, out);
}
```

```cpp
#include <hip/hip_runtime.h>
#include <hip/hip_bf16.h>

#define SEQ 2048
#define DM 640
#define NS 128
#define FF 1728

typedef unsigned short u16;
typedef short s4 __attribute__((ext_vector_type(4)));
typedef short s8 __attribute__((ext_vector_type(8)));
typedef _Float16 h8 __attribute__((ext_vector_type(8)));
typedef float f4 __attribute__((ext_vector_type(4)));
typedef unsigned int u32;
typedef unsigned int u32x4 __attribute__((ext_vector_type(4)));
typedef unsigned int u32x2 __attribute__((ext_vector_type(2)));

__device__ __forceinline__ float bf2f(u16 v) { return __uint_as_float(((u32)v) << 16); }
__device__ __forceinline__ u16 f2bf(float f) {
  u32 u = __float_as_uint(f);
  u32 r = (u + 0x7FFFu + ((u >> 16) & 1u)) >> 16;
  return (u16)r;
}
__device__ __forceinline__ float bfr(float f) { return bf2f(f2bf(f)); }

typedef __bf16 bf2_t __attribute__((ext_vector_type(2)));
typedef float f2_t __attribute__((ext_vector_type(2)));
__device__ __forceinline__ u32 pk_bf(float a, float b) {
  f2_t v = {a, b};
  bf2_t r = __builtin_convertvector(v, bf2_t);
  return __builtin_bit_cast(u32, r);
}
__device__ __forceinline__ float lo_f(u32 w) { return __uint_as_float(w << 16); }
__device__ __forceinline__ float hi_f(u32 w) { return __uint_as_float(w & 0xFFFF0000u); }

#define WAIT_V(n) asm volatile("s_waitcnt vmcnt(%0)" ::"n"(n) : "memory")
#define RAW_BARRIER() do { asm volatile("s_waitcnt lgkmcnt(0)" ::: "memory"); __builtin_amdgcn_s_barrier(); } while (0)
__device__ __forceinline__ void glds16(const void* gsrc, const void* lp) {
  unsigned keep;
  unsigned lds_dst = (unsigned)(size_t)(const __attribute__((address_space(3))) char*)lp;
  asm volatile("s_mov_b32 %0, m0\n\ts_mov_b32 m0, %2\n\ts_nop 0\n\tglobal_load_lds_dwordx4 %1, off\n\ts_mov_b32 m0, %0"
               : "=&s"(keep) : "v"(gsrc), "s"(lds_dst) : "memory");
}
#define GLDS(gp, lp) glds16((const void*)(gp), (const void*)(lp))

#define LUT_E0 111
#define LUT_E1 134
#define LUT_HALF ((LUT_E1 - LUT_E0) * 128)
#define LUT_N (2 * LUT_HALF)

__device__ __forceinline__ float mish_bf(float h) {
  float e = bfr(expf(-fabsf(h)));
  float l = bfr(log1pf(e));
  float sp = bfr(l + (h > 0.f ? h : 0.f));
  float th = bfr(tanhf(sp));
  return bfr(h * th);
}

__global__ __launch_bounds__(256) void ln_kernel(const u16* __restrict__ u, u16* __restrict__ xnP,
                                                 u16* __restrict__ xT, float2* __restrict__ stats,
                                                 u16* __restrict__ lut) {
  __shared__ __attribute__((aligned(16))) u16 tile[16][DM + 8];
  if (blockIdx.x >= SEQ / 16) {
    int idx = (blockIdx.x - SEQ / 16) * 256 + threadIdx.x;
    if (idx < LUT_N) {
      int sgn = idx >= LUT_HALF;
      int a = idx - sgn * LUT_HALF + (LUT_E0 << 7);
      float h = bf2f((u16)(a | (sgn << 15)));
      lut[idx] = f2bf(mish_bf(h));
    } else if (idx < 6144) {
      lut[idx] = 0;
    }
    return;
  }
  const int wave = threadIdx.x >> 6, lane = threadIdx.x & 63;
  const int r = wave * 4 + (lane >> 4), q = lane & 15;
  const int row = blockIdx.x * 16 + r;
  float v[40];
  const u32x4* src = (const u32x4*)(u + (size_t)row * DM);
#pragma unroll
  for (int i = 0; i < 5; ++i) {
    u32x4 w = src[q + 16 * i];
#pragma unroll
    for (int j = 0; j < 4; ++j) {
      v[i * 8 + 2 * j] = lo_f(w[j]);
      v[i * 8 + 2 * j + 1] = hi_f(w[j]);
    }
  }
  float s = 0.f;
#pragma unroll
  for (int i = 0; i < 40; ++i) s += v[i];
#pragma unroll
  for (int o = 1; o < 16; o <<= 1) s += __shfl_xor(s, o);
  const float mean = s * (1.0f / DM);
  float ss = 0.f;
#pragma unroll
  for (int i = 0; i < 40; ++i) { float d = v[i] - mean; ss += d * d; }
#pragma unroll
  for (int o = 1; o < 16; o <<= 1) ss += __shfl_xor(ss, o);
  const float var = ss * (1.0f / DM);
  const float mb = bfr(mean);
  const float vb = bfr(var);
  const float den = bfr(sqrtf(bfr(vb + 1e-5f)));
  if (q == 0) stats[row] = make_float2(mb, den);
#pragma unroll
  for (int i = 0; i < 5; ++i) {
    u32x4 w;
#pragma unroll
    for (int j = 0; j < 4; ++j) {
      u32 dd = pk_bf(v[i * 8 + 2 * j] - mb, v[i * 8 + 2 * j + 1] - mb);
      w[j] = pk_bf(lo_f(dd) / den, hi_f(dd) / den);
    }
    *(u32x4*)&tile[r][(q + 16 * i) * 8] = w;
  }
  __syncthreads();
#pragma unroll
  for (int i = 0; i < 5; ++i) {
    int idx = threadIdx.x + 256 * i;
    int kb = idx >> 6, ln = idx & 63, g = ln >> 4, fr = ln & 15;
    u32x2 a = *(const u32x2*)&tile[fr][kb * 32 + 4 * g];
    u32x2 b = *(const u32x2*)&tile[fr][kb * 32 + 16 + 4 * g];
    u32x4 w = {a[0], a[1], b[0], b[1]};
    *(u32x4*)(xnP + ((size_t)blockIdx.x * 1280 + idx) * 8) = w;
  }
#pragma unroll
  for (int i = 0; i < 5; ++i) {
    int idx = threadIdx.x + 256 * i;
    int d = idx >> 1, c = idx & 1;
    u32x4 w;
#pragma unroll
    for (int j = 0; j < 4; ++j) {
      _Float16 ha = (_Float16)bf2f(tile[c * 8 + 2 * j][d]);
      _Float16 hb = (_Float16)bf2f(tile[c * 8 + 2 * j + 1][d]);
      w[j] = (u32)__builtin_bit_cast(u16, ha) | ((u32)__builtin_bit_cast(u16, hb) << 16);
    }
    *(u32x4*)(xT + (size_t)d * SEQ + blockIdx.x * 16 + c * 8) = w;
  }
}

#define KRLEN 2080
__device__ __forceinline__ void cexp_p(float arl2, float w, float p, float& re, float& im) {
  float mag = __builtin_amdgcn_exp2f(arl2 * p);
  float rev = w * p;
  rev = rev - floorf(rev);
  re = mag * __builtin_amdgcn_cosf(rev);
  im = mag * __builtin_amdgcn_sinf(rev);
}

__global__ __launch_bounds__(256) void kgen_kernel(const u16* __restrict__ lar, const u16* __restrict__ aim,
                                                   const u16* __restrict__ cre, const u16* __restrict__ cim,
                                                   const u16* __restrict__ logdt, u16* __restrict__ Kr,
                                                   float* __restrict__ kscale) {
  __shared__ __attribute__((aligned(16))) float Ah[48][260];
  __shared__ __attribute__((aligned(16))) float Bl[16][260];
  __shared__ __attribute__((aligned(16))) float red[4][768];
  __shared__ float Kp[768];
  __shared__ float wmax[4];
  const int d = blockIdx.x, t = threadIdx.x, n = t & 127, half = t >> 7;
  const int wave = t >> 6, lane = t & 63;
  const float dt = bfr(expf(bf2f(logdt[d])));
  const float ar = -bfr(expf(bf2f(lar[d * NS + n]))) * dt;
  const float ai = bf2f(aim[d * NS + n]) * dt;
  const float cr = bf2f(cre[d * NS + n]) * dt;
  const float ci = bf2f(cim[d * NS + n]) * dt;
  const float arl2 = ar * 1.4426950408889634f;
  const float w = ai * 0.15915494309189535f;
  const int r0 = half ? 21 : 0, r1 = half ? 41 : 21;
  for (int row = r0; row < r1; ++row) {
    int p;
    if (row < 16) p = (row >> 3) * 128 + (row & 7);
    else if (row < 24) p = (2 + ((row - 16) >> 2)) * 128 + ((row - 16) & 3) * 2;
    else if (row < 32) p = (4 + ((row - 24) >> 1)) * 128 + ((row - 24) & 1) * 4;
    else p = (8 + (row - 32)) * 128;
    float er, ei;
    cexp_p(arl2, w, (float)p, er, ei);
    float hr = cr * er - ci * ei;
    float hi = cr * ei + ci * er;
    Ah[row][n] = hr;
    Ah[row][128 + n] = -hi;
  }
  if (half) {
    for (int row = 41; row < 48; ++row) { Ah[row][n] = 0.f; Ah[row][128 + n] = 0.f; }
  }
  for (int j = half * 8; j < half * 8 + 8; ++j) {
    float er, ei;
    cexp_p(arl2, w, (float)(8 * j), er, ei);
    Bl[j][n] = er;
    Bl[j][128 + n] = ei;
  }
  __syncthreads();
  {
    const int i = lane & 15, g = lane >> 4;
    f4 acc[3];
#pragma unroll
    for (int tt = 0; tt < 3; ++tt) acc[tt] = (f4){0.f, 0.f, 0.f, 0.f};
#pragma unroll
    for (int qq = 0; qq < 4; ++qq) {
      const int k0 = 64 * wave + 16 * qq + 4 * g;
      f4 b = *(const f4*)&Bl[i][k0];
#pragma unroll
      for (int tt = 0; tt < 3; ++tt) {
        f4 a = *(const f4*)&Ah[tt * 16 + i][k0];
#pragma unroll
        for (int e = 0; e < 4; ++e) acc[tt] = __builtin_amdgcn_mfma_f32_16x16x4f32(a[e], b[e], acc[tt], 0, 0, 0);
      }
    }
#pragma unroll
    for (int tt = 0; tt < 3; ++tt)
#pragma unroll
      for (int r = 0; r < 4; ++r) red[wave][(tt * 16 + 4 * g + r) * 16 + i] = acc[tt][r];
  }
  __syncthreads();
  float mx = 0.f;
#pragma unroll
  for (int i = 0; i < 3; ++i) {
    int idx = t + 256 * i;
    float s = 2.0f * (((red[0][idx] + red[1][idx]) + red[2][idx]) + red[3][idx]);
    Kp[idx] = s;
    mx = fmaxf(mx, fabsf(s));
  }
#pragma unroll
  for (int o = 1; o < 64; o <<= 1) mx = fmaxf(mx, __shfl_xor(mx, o));
  if (lane == 0) wmax[wave] = mx;
  __syncthreads();
  mx = fmaxf(fmaxf(wmax[0], wmax[1]), fmaxf(wmax[2], wmax[3]));
  int ex = 0;
  if (mx > 0.f && mx < 3.0e38f) { (void)frexpf(mx, &ex); }
  int e = 15 - ex;
  e = e > 100 ? 100 : (e < -100 ? -100 : e);
  const float sc = ldexpf(1.0f, e);
  if (t == 0) kscale[d] = ldexpf(1.0f, -e);
  for (int c = t; c < KRLEN / 8; c += 256) {
    u32x4 o;
#pragma unroll
    for (int j = 0; j < 4; ++j) {
      u32 pk = 0;
#pragma unroll
      for (int h = 0; h < 2; ++h) {
        int tt = c * 8 + 2 * j + h;
        int l = 2047 - tt;
        float val = 0.f;
        if (l >= 0) {
          int p = (int)bfr((float)l);
          int col = (p >> 3) & 15, ii = p >> 7, r = p & 7;
          int row;
          if (ii < 2) row = ii * 8 + r;
          else if (ii < 4) row = 16 + (ii - 2) * 4 + (r >> 1);
          else if (ii < 8) row = 24 + (ii - 4) * 2 + (r >> 2);
          else row = 32 + (ii - 8);
          val = Kp[row * 16 + col] * sc;
        }
        _Float16 hv = (_Float16)val;
        pk |= ((u32)__builtin_bit_cast(u16, hv)) << (16 * h);
      }
      o[j] = pk;
    }
    *(u32x4*)(Kr + (size_t)d * KRLEN + c * 8) = o;
  }
}

#define KCPITCH 2080
__global__ __launch_bounds__(256) void conv_kernel(const u16* __restrict__ xT, const u16* __restrict__ Kr,
                                                   const float* __restrict__ kscale, u16* __restrict__ yT) {
  __shared__ __attribute__((aligned(16))) u16 Kc[8 * KCPITCH];
  __shared__ __attribute__((aligned(16))) u16 xs[256 + SEQ];
  const int d = blockIdx.x, t = threadIdx.x, wave = t >> 6, lane = t & 63;
  {
    u32x4 v = *(const u32x4*)(xT + (size_t)d * SEQ + t * 8);
    *(u32x4*)&xs[256 + t * 8] = v;
    if (t < 32) *(u32x4*)&xs[t * 8] = (u32x4){0u, 0u, 0u, 0u};
  }
  for (int c = t; c < 258; c += 256) {
    u32x4 lo = *(const u32x4*)(Kr + (size_t)d * KRLEN + c * 8);
    u32x4 hi = *(const u32x4*)(Kr + (size_t)d * KRLEN + c * 8 + 8);
    u32 wv[8] = {lo[0], lo[1], lo[2], lo[3], hi[0], hi[1], hi[2], hi[3]};
#pragma unroll
    for (int s = 0; s < 8; ++s) {
      u32x4 o;
#pragma unroll
      for (int j = 0; j < 4; ++j) {
        if ((s & 1) == 0) o[j] = wv[j + s / 2];
        else o[j] = __builtin_amdgcn_alignbit(wv[j + s / 2 + 1], wv[j + s / 2], 16);
      }
      *(u32x4*)&Kc[s * KCPITCH + c * 8] = o;
    }
  }
  __syncthreads();
  const int b = lane & 15, g = lane >> 4;
  const int s = (7 - b) & 7;
  f4 acc[8];
#pragma unroll
  for (int i = 0; i < 8; ++i) acc[i] = (f4){0.f, 0.f, 0.f, 0.f};
  const int abase = s * KCPITCH + (b < 8 ? 2024 : 2016) + 8 * g;
  const int bbase = 256 - 16 + 16 * b + 8 * g;
#pragma unroll
  for (int rr = 0; rr < 2; ++rr) {
    const int rp = wave * 2 + rr;
    h8 A[8], B[8];
#pragma unroll
    for (int u = 0; u < 8; ++u) A[u] = *(const h8*)&Kc[abase - 32 * rp - 256 * u];
#pragma unroll
    for (int v = 0; v < 8; ++v) B[v] = *(const h8*)&xs[bbase - 32 * rp + 256 * v];
#pragma unroll
    for (int u = 0; u < 8; ++u)
#pragma unroll
      for (int v = 0; v + u < 8; ++v)
        acc[u + v] = __builtin_amdgcn_mfma_f32_16x16x32_f16(A[u], B[v], acc[u + v], 0, 0, 0);
  }
  __syncthreads();
  float* red = (float*)Kc;
#pragma unroll
  for (int tt = 0; tt < 8; ++tt) *(f4*)&red[((wave * 8 + tt) * 64 + lane) * 4] = acc[tt];
  __syncthreads();
  const float sc = kscale[d];
#pragma unroll
  for (int i = 0; i < 2; ++i) {
    const int tt = wave * 2 + i;
    f4 s0 = *(const f4*)&red[((0 * 8 + tt) * 64 + lane) * 4];
    f4 s1 = *(const f4*)&red[((1 * 8 + tt) * 64 + lane) * 4];
    f4 s2 = *(const f4*)&red[((2 * 8 + tt) * 64 + lane) * 4];
    f4 s3 = *(const f4*)&red[((3 * 8 + tt) * 64 + lane) * 4];
    f4 sum = ((s0 + s1) + (s2 + s3)) * sc;
    u32x2 o;
    o[0] = (u32)f2bf(sum[0]) | ((u32)f2bf(sum[1]) << 16);
    o[1] = (u32)f2bf(sum[2]) | ((u32)f2bf(sum[3]) << 16);
    *(u32x2*)(yT + (size_t)d * SEQ + 256 * tt + 16 * b + 4 * g) = o;
  }
}

#define TRREAD(p) __builtin_amdgcn_ds_read_tr16_b64_v4i16((__attribute__((address_space(3))) s4*)(p))
#define STAMP(k)

__device__ __forceinline__ void wait_vm(int n) {
  switch (n) {
    case 0: WAIT_V(0); break; case 1: WAIT_V(1); break; case 2: WAIT_V(2); break; case 3: WAIT_V(3); break;
    case 4: WAIT_V(4); break; case 5: WAIT_V(5); break; case 6: WAIT_V(6); break; case 7: WAIT_V(7); break;
    case 8: WAIT_V(8); break; case 9: WAIT_V(9); break; case 10: WAIT_V(10); break; case 11: WAIT_V(11); break;
    case 12: WAIT_V(12); break; case 13: WAIT_V(13); break; case 14: WAIT_V(14); break; default: WAIT_V(15); break;
  }
}

template <int ND>
__global__ __launch_bounds__(512) void gemm1_kernel(const u16* __restrict__ xnP, const u16* __restrict__ w1,
                                                    const u16* __restrict__ b1, const u16* __restrict__ lut,
                                                    u16* __restrict__ actP) {
  constexpr int STG = 30 * 1024;
  constexpr int AUX = ND * STG;
  constexpr int NKT = DM / 64;
  __shared__ __attribute__((aligned(1024))) char shm[ND * STG + 13 * 1024];
  const int t = threadIdx.x, lane = t & 63;
  const int wave = __builtin_amdgcn_readfirstlane(t >> 6);
  STAMP(0);
  const int xcd = blockIdx.x & 7, idx = blockIdx.x >> 3;
  const int mi = idx & 3, ni = idx >> 2;
  const int bm = (xcd >> 1) * 4 + mi, bn = (xcd & 1) * 8 + ni;
  const int phase = ((5 * ni + 10 * mi) >> 2) % NKT;
  const int mt0 = bm * 8;
  int n0 = bn * 112;
  if (n0 + 112 > FF) n0 = FF - 112;
  const u16* pa[2];
  const u16* pb[2];
#pragma unroll
  for (int i = 0; i < 2; ++i) {
    int p = wave + 8 * i;
    pa[i] = xnP + ((size_t)(mt0 + (p >> 1)) * 20 + (p & 1)) * 512 + lane * 8;
    int c = p * 64 + lane;
    if (c > 14 * 64 - 1) c = 14 * 64 - 1;
    int rr = c / 14, cc = c % 14;
    pb[i] = w1 + (size_t)rr * FF + n0 + cc * 8;
  }
  const int cnt = wave < 6 ? 4 : 3;
  auto stage = [&](int kt, int buf) {
    int ko = kt + phase;
    if (ko >= NKT) ko -= NKT;
    char* sb = shm + buf * STG;
#pragma unroll
    for (int i = 0; i < 2; ++i) GLDS(pa[i] + ko * 1024, sb + (wave + 8 * i) * 1024);
    GLDS(pb[0] + (size_t)ko * 64 * FF, sb + 16384 + wave * 1024);
    if (wave < 6) GLDS(pb[1] + (size_t)ko * 64 * FF, sb + 16384 + (wave + 8) * 1024);
  };
  const int wm = wave & 3, wn = wave >> 2;
  f4 acc[2][4];
#pragma unroll
  for (int i = 0; i < 2; ++i)
#pragma unroll
    for (int j = 0; j < 4; ++j) acc[i][j] = (f4){0.f, 0.f, 0.f, 0.f};
  const int fr = lane & 15, g = lane >> 4, tq = (lane & 15) >> 2, tp = lane & 3;
  const int aoff = wm * 4096 + lane * 16;
  const int boff = 16384 + (4 * g + tq) * 224 + tp * 8 + wn * 128;
#pragma nounroll
  for (int s = 0; s < ND - 1; ++s) stage(s, s);
  GLDS(lut + wave * 512 + lane * 8, shm + AUX + wave * 1024);
  if (wave < 4) GLDS(lut + (wave + 8) * 512 + lane * 8, shm + AUX + (wave + 8) * 1024);
  else GLDS(b1 + n0 + (lane < 14 ? lane : 13) * 8, shm + AUX + 12 * 1024);
  int cur = 0, nxt = ND - 1;
  STAMP(1);
#pragma nounroll
  for (int kt = 0; kt < NKT; ++kt) {
    const int rem = (NKT - 1 - kt) < (ND - 2) ? (NKT - 1 - kt) : (ND - 2);
    wait_vm(rem * cnt + (kt < ND - 1 ? 2 : 0));
    RAW_BARRIER();
    if (kt == 0) STAMP(2);
    if (kt + ND - 1 < NKT) stage(kt + ND - 1, nxt);
    const char* sb = shm + cur * STG;
    cur = cur == ND - 1 ? 0 : cur + 1;
    nxt = nxt == ND - 1 ? 0 : nxt + 1;
#pragma unroll
    for (int ks = 0; ks < 2; ++ks) {
      s8 af[2];
#pragma unroll
      for (int i = 0; i < 2; ++i) af[i] = *(const s8*)(sb + aoff + i * 2048 + ks * 1024);
#pragma unroll
      for (int j = 0; j < 4; ++j) {
        if (j < 3 || wn == 0) {
          const char* p0 = sb + boff + ks * 32 * 224 + j * 32;
          s4 b0 = TRREAD(p0);
          s4 b1v = TRREAD(p0 + 16 * 224);
          s8 bf = {b0[0], b0[1], b0[2], b0[3], b1v[0], b1v[1], b1v[2], b1v[3]};
#pragma unroll
          for (int i = 0; i < 2; ++i)
            acc[i][j] = __builtin_amdgcn_mfma_f32_16x16x32_bf16(bf, af[i], acc[i][j], 0, 0, 0);
        }
      }
    }
  }
  STAMP(7);
  const u16* slut = (const u16*)(shm + AUX);
  const u16* sbias = (const u16*)(shm + AUX + 12 * 1024);
#pragma unroll
  for (int j = 0; j < 4; ++j) {
    if (j < 3 || wn == 0) {
      u32x2 bb = *(const u32x2*)(sbias + wn * 64 + j * 16 + 4 * g);
      const float bv[4] = {lo_f(bb[0]), hi_f(bb[0]), lo_f(bb[1]), hi_f(bb[1])};
      const int n = n0 + wn * 64 + j * 16;
      const int kb = n >> 5, half = (n >> 4) & 1;
#pragma unroll
      for (int i = 0; i < 2; ++i) {
        u32 t0 = pk_bf(acc[i][j][0], acc[i][j][1]);
        u32 t1 = pk_bf(acc[i][j][2], acc[i][j][3]);
        u32 h0 = pk_bf(lo_f(t0) + bv[0], hi_f(t0) + bv[1]);
        u32 h1 = pk_bf(lo_f(t1) + bv[2], hi_f(t1) + bv[3]);
        u32 hb[4] = {h0 & 0xFFFFu, h0 >> 16, h1 & 0xFFFFu, h1 >> 16};
        u32 res[4];
#pragma unroll
        for (int r = 0; r < 4; ++r) {
          u32 a = hb[r] & 0x7FFFu;
          u32 li = a - (LUT_E0 << 7);
          bool in = li < (u32)LUT_HALF;
          u32 v = slut[(in ? li : 0u) + (hb[r] >> 15) * LUT_HALF];
          float h = __uint_as_float(hb[r] << 16);
          float fb = (a < (LUT_E0 << 7)) ? h * 0.59765625f : (h > 0.f ? h : h * 0.0f);
          u32 fbb = pk_bf(fb, fb) & 0xFFFFu;
          res[r] = in ? v : fbb;
        }
        u32x2 ov = {res[0] | (res[1] << 16), res[2] | (res[3] << 16)};
        const int mt = mt0 + wm * 2 + i;
        if (n >= bn * 112) *(u32x2*)(actP + (((size_t)mt * 54 + kb) * 64 + lane) * 8 + half * 4) = ov;
      }
    }
  }
  STAMP(8);
}

template <int ND>
__global__ __launch_bounds__(512) void gemm2_kernel(const u16* __restrict__ actP, const u16* __restrict__ w2,
                                                    const u16* __restrict__ xin, const float2* __restrict__ stats,
                                                    const u16* __restrict__ yT, const u16* __restrict__ dskip,
                                                    u16* __restrict__ out) {
  constexpr int STG = 18 * 1024;
  constexpr int AUX = ND * STG;
  constexpr int NKT = FF / 64;
  __shared__ __attribute__((aligned(1024))) char shm[ND * STG + 21 * 1024];
  const int t = threadIdx.x, lane = t & 63;
  const int wave = __builtin_amdgcn_readfirstlane(t >> 6);
  const int xcd = blockIdx.x & 7, idx = blockIdx.x >> 3;
  const int mi = idx & 7, ni = idx >> 3;
  const int bm = (xcd >> 1) * 8 + mi, bn = (xcd & 1) * 4 + ni;
  const int phase = ((27 * mi + 54 * ni) >> 3) % NKT;
  const int mt0 = bm * 4, m0 = bm * 64, n0 = bn * 80;
  STAMP(0);
  const u16* pa = actP + ((size_t)(mt0 + (wave >> 1)) * 54 + (wave & 1)) * 512 + lane * 8;
  const u16* pb[2];
#pragma unroll
  for (int i = 0; i < 2; ++i) {
    int c = (wave + 8 * i) * 64 + lane;
    if (c > 639) c = 639;
    pb[i] = w2 + (size_t)(c / 10) * DM + n0 + (c % 10) * 8;
  }
  const int cnt = wave < 2 ? 3 : 2;
  auto stage = [&](int kt, int buf) {
    int ko = kt + phase;
    if (ko >= NKT) ko -= NKT;
    char* sb = shm + buf * STG;
    GLDS(pa + ko * 1024, sb + wave * 1024);
    GLDS(pb[0] + (size_t)ko * 64 * DM, sb + 8192 + wave * 1024);
    if (wave < 2) GLDS(pb[1] + (size_t)ko * 64 * DM, sb + 8192 + (wave + 8) * 1024);
  };
  const int wm = wave & 3, wn = wave >> 2;
  f4 acc[3];
#pragma unroll
  for (int j = 0; j < 3; ++j) acc[j] = (f4){0.f, 0.f, 0.f, 0.f};
  const int fr = lane & 15, g = lane >> 4, tq = (lane & 15) >> 2, tp = lane & 3;
  const int aoff = wm * 2048 + lane * 16;
  const int boff = 8192 + (4 * g + tq) * 160 + tp * 8 + wn * 96;
#pragma nounroll
  for (int s = 0; s < ND - 1; ++s) stage(s, s);
  int cur = 0, nxt = ND - 1;
  STAMP(1);
#pragma nounroll
  for (int kt = 0; kt < NKT; ++kt) {
    const int rem = (NKT - 1 - kt) < (ND - 2) ? (NKT - 1 - kt) : (ND - 2);
    wait_vm(rem * cnt);
    RAW_BARRIER();
    if (kt == 0) STAMP(2);
    if (kt == 4) STAMP(3);
    if (kt == 8) STAMP(4);
    if (kt == 16) STAMP(5);
    if (kt == 26) STAMP(6);
    if (kt + ND - 1 < NKT) stage(kt + ND - 1, nxt);
    else if (kt + ND - 1 == NKT) {
#pragma unroll
      for (int i = 0; i < 3; ++i) {
        int p = wave + 8 * i;
        if (p > 20) p = 20;
        int c = (p < 10 ? p : p - 10) * 64 + lane;
        const u16* src;
        if (p < 10) src = xin + (size_t)(m0 + c / 10) * DM + n0 + (c % 10) * 8;
        else if (p < 20) src = yT + (size_t)(n0 + (c >> 3)) * SEQ + m0 + (c & 7) * 8;
        else src = (lane < 32) ? (const u16*)(stats + m0) + lane * 8 : dskip + n0 + (lane < 42 ? lane - 32 : 9) * 8;
        GLDS(src, shm + AUX + p * 1024);
      }
    }
    const char* sb = shm + cur * STG;
    cur = cur == ND - 1 ? 0 : cur + 1;
    nxt = nxt == ND - 1 ? 0 : nxt + 1;
#pragma unroll
    for (int ks = 0; ks < 2; ++ks) {
      s8 af = *(const s8*)(sb + aoff + ks * 1024);
#pragma unroll
      for (int j = 0; j < 3; ++j) {
        if (j < 2 || wn == 0) {
          const char* p0 = sb + boff + ks * 32 * 160 + j * 32;
          s4 b0 = TRREAD(p0);
          s4 b1v = TRREAD(p0 + 16 * 160);
          s8 bf = {b0[0], b0[1], b0[2], b0[3], b1v[0], b1v[1], b1v[2], b1v[3]};
          acc[j] = __builtin_amdgcn_mfma_f32_16x16x32_bf16(bf, af, acc[j], 0, 0, 0);
        }
      }
    }
  }
  STAMP(7);
  const char* sx = shm + AUX;
  const char* sy = shm + AUX + 10 * 1024;
  const char* sm = shm + AUX + 20 * 1024;
  const int ml = wm * 16 + fr;
  const float2 st = *(const float2*)(sm + ml * 8);
#pragma unroll
  for (int j = 0; j < 3; ++j) {
    if (j < 2 || wn == 0) {
      const int nl = wn * 48 + j * 16;
      u32x2 xx = *(const u32x2*)(sx + ml * 160 + (nl + 4 * g) * 2);
      u32x2 dd = *(const u32x2*)(sm + 512 + (nl + 4 * g) * 2);
      s4 yy = TRREAD(sy + (nl + 4 * g + tq) * 128 + (wm * 16 + 4 * tp) * 2);
      const float xv[4] = {lo_f(xx[0]), hi_f(xx[0]), lo_f(xx[1]), hi_f(xx[1])};
      const float dv[4] = {lo_f(dd[0]), hi_f(dd[0]), lo_f(dd[1]), hi_f(dd[1])};
      float o[4];
#pragma unroll
      for (int r = 0; r < 4; ++r) {
        float xn = bfr(bfr(xv[r] - st.x) / st.y);
        float y = bf2f((u16)yy[r]);
        float a = bfr(y + bfr(xn * dv[r]));
        float s1 = bfr(xv[r] + a);
        o[r] = s1 + bfr(acc[j][r]);
      }
      u32x2 ov = {pk_bf(o[0], o[1]), pk_bf(o[2], o[3])};
      *(u32x2*)(out + (size_t)(m0 + ml) * DM + n0 + nl + 4 * g) = ov;
    }
  }
  STAMP(8);
}

extern "C" void kernel_launch(void* const* d_in, const int* in_sizes, int n_in,
                              void* d_out, int out_size, void* d_ws, size_t ws_size,
                              hipStream_t stream) {
  const u16* u = (const u16*)d_in[0];
  const u16* lar = (const u16*)d_in[1];
  const u16* aim = (const u16*)d_in[2];
  const u16* cre = (const u16*)d_in[3];
  const u16* cim = (const u16*)d_in[4];
  const u16* logdt = (const u16*)d_in[5];
  const u16* dskip = (const u16*)d_in[6];
  const u16* w1 = (const u16*)d_in[8];
  const u16* b1 = (const u16*)d_in[9];
  const u16* w2 = (const u16*)d_in[10];
  u16* out = (u16*)d_out;
  char* ws = (char*)d_ws;
  if (ws_size < (16u << 20) + (size_t)SEQ * FF * 2) return;
  u16* xnP = (u16*)(ws);
  u16* xT = (u16*)(ws + (4u << 20));
  u16* Kr = (u16*)(ws + (8u << 20));
  float* ksc = (float*)(ws + (12u << 20));
  float2* stats = (float2*)(ws + (12u << 20) + 4096);
  u16* lut = (u16*)(ws + (12u << 20) + 32768);
  u16* yT = (u16*)(ws + (13u << 20));
  u16* actP = (u16*)(ws + (16u << 20));

  ln_kernel<<<SEQ / 16 + 24, 256, 0, stream>>>(u, xnP, xT, stats, lut);
  kgen_kernel<<<DM, 256, 0, stream>>>(lar, aim, cre, cim, logdt, Kr, ksc);
  conv_kernel<<<DM, 256, 0, stream>>>(xT, Kr, ksc, yT);
  gemm1_kernel<4><<<256, 512, 0, stream>>>(xnP, w1, b1, lut, actP);
  gemm2_kernel<7><<<256, 512, 0, stream>>>(actP, w2, u, stats, yT, dskip, out);
}
```

```cpp
#include <hip/hip_runtime.h>
#include <hip/hip_bf16.h>

#define SEQ 2048
#define DM 640
#define NS 128
#define FF 1728

typedef unsigned short u16;
typedef short s4 __attribute__((ext_vector_type(4)));
typedef short s8 __attribute__((ext_vector_type(8)));
typedef _Float16 h8 __attribute__((ext_vector_type(8)));
typedef float f4 __attribute__((ext_vector_type(4)));
typedef unsigned int u32;
typedef unsigned int u32x4 __attribute__((ext_vector_type(4)));
typedef unsigned int u32x2 __attribute__((ext_vector_type(2)));

__device__ __forceinline__ float bf2f(u16 v) { return __uint_as_float(((u32)v) << 16); }
__device__ __forceinline__ u16 f2bf(float f) {
  u32 u = __float_as_uint(f);
  u32 r = (u + 0x7FFFu + ((u >> 16) & 1u)) >> 16;
  return (u16)r;
}
__device__ __forceinline__ float bfr(float f) { return bf2f(f2bf(f)); }

typedef __bf16 bf2_t __attribute__((ext_vector_type(2)));
typedef float f2_t __attribute__((ext_vector_type(2)));
__device__ __forceinline__ u32 pk_bf(float a, float b) {
  f2_t v = {a, b};
  bf2_t r = __builtin_convertvector(v, bf2_t);
  return __builtin_bit_cast(u32, r);
}
__device__ __forceinline__ float lo_f(u32 w) { return __uint_as_float(w << 16); }
__device__ __forceinline__ float hi_f(u32 w) { return __uint_as_float(w & 0xFFFF0000u); }

#define WAIT_V(n) asm volatile("s_waitcnt vmcnt(%0)" ::"n"(n) : "memory")
#define RAW_BARRIER() do { asm volatile("s_waitcnt lgkmcnt(0)" ::: "memory"); __builtin_amdgcn_s_barrier(); } while (0)
__device__ __forceinline__ void glds16(const void* gsrc, const void* lp) {
  unsigned keep;
  unsigned lds_dst = (unsigned)(size_t)(const __attribute__((address_space(3))) char*)lp;
  asm volatile("s_mov_b32 %0, m0\n\ts_mov_b32 m0, %2\n\ts_nop 0\n\tglobal_load_lds_dwordx4 %1, off\n\ts_mov_b32 m0, %0"
               : "=&s"(keep) : "v"(gsrc), "s"(lds_dst) : "memory");
}
#define GLDS(gp, lp) glds16((const void*)(gp), (const void*)(lp))

#define LUT_E0 111
#define LUT_E1 134
#define LUT_HALF ((LUT_E1 - LUT_E0) * 128)
#define LUT_N (2 * LUT_HALF)

__device__ __forceinline__ float mish_bf(float h) {
  float e = bfr(expf(-fabsf(h)));
  float l = bfr(log1pf(e));
  float sp = bfr(l + (h > 0.f ? h : 0.f));
  float th = bfr(tanhf(sp));
  return bfr(h * th);
}

#define KRLEN 2080
#define WFENCE() do { asm volatile("" ::: "memory"); __builtin_amdgcn_wave_barrier(); asm volatile("" ::: "memory"); } while (0)
__device__ __forceinline__ void cexp_p(float arl2, float w, float p, float& re, float& im) {
  float mag = __builtin_amdgcn_exp2f(arl2 * p);
  float rev = w * p;
  rev = rev - floorf(rev);
  re = mag * __builtin_amdgcn_cosf(rev);
  im = mag * __builtin_amdgcn_sinf(rev);
}
__device__ __forceinline__ void cmul(float ar, float ai, float br, float bi, float& cr, float& ci) {
  float r = fmaf(ar, br, -(ai * bi));
  float i = fmaf(ar, bi, ai * br);
  cr = r; ci = i;
}
__device__ __forceinline__ u32x2 split2(float a, float b) {
  u32 h = pk_bf(a, b);
  u32 l = pk_bf(a - lo_f(h), b - hi_f(h));
  return (u32x2){h, l};
}

#define K1_LN 256
#define K1_KG 320
#define K1_LUT 24
#define WPITCH 544
__global__ __launch_bounds__(256) void k1_kernel(const u16* __restrict__ u, u16* __restrict__ xnP,
                                                 u16* __restrict__ xT, float2* __restrict__ stats,
                                                 u16* __restrict__ lut,
                                                 const u16* __restrict__ lar, const u16* __restrict__ aim,
                                                 const u16* __restrict__ cre, const u16* __restrict__ cim,
                                                 const u16* __restrict__ logdt, u16* __restrict__ Kr,
                                                 float* __restrict__ kscale) {
  __shared__ __attribute__((aligned(16))) char k1s[4 * 16 * WPITCH];
  const int t = threadIdx.x, lane = t & 63;
  const int wave = __builtin_amdgcn_readfirstlane(t >> 6);
  if (blockIdx.x < K1_LN) {
    u16 (*tile)[DM + 8] = (u16 (*)[DM + 8])k1s;
    const int r = wave * 2 + (lane >> 5), q = lane & 31;
    const int row = blockIdx.x * 8 + r;
    float v[20];
    const u32x2* src = (const u32x2*)(u + (size_t)row * DM);
#pragma unroll
    for (int i = 0; i < 5; ++i) {
      u32x2 w = src[q + 32 * i];
      v[i * 4 + 0] = lo_f(w[0]); v[i * 4 + 1] = hi_f(w[0]);
      v[i * 4 + 2] = lo_f(w[1]); v[i * 4 + 3] = hi_f(w[1]);
    }
    float s = 0.f;
#pragma unroll
    for (int i = 0; i < 20; ++i) s += v[i];
#pragma unroll
    for (int o = 1; o < 32; o <<= 1) s += __shfl_xor(s, o);
    const float mean = s * (1.0f / DM);
    float ss = 0.f;
#pragma unroll
    for (int i = 0; i < 20; ++i) { float d = v[i] - mean; ss += d * d; }
#pragma unroll
    for (int o = 1; o < 32; o <<= 1) ss += __shfl_xor(ss, o);
    const float var = ss * (1.0f / DM);
    const float mb = bfr(mean);
    const float vb = bfr(var);
    const float den = bfr(sqrtf(bfr(vb + 1e-5f)));
    const float rden = 1.0f / den;
    if (q == 0) stats[row] = make_float2(mb, rden);
#pragma unroll
    for (int i = 0; i < 5; ++i) {
      u32 d0 = pk_bf(v[i * 4 + 0] - mb, v[i * 4 + 1] - mb);
      u32 d1 = pk_bf(v[i * 4 + 2] - mb, v[i * 4 + 3] - mb);
      u32x2 w = {pk_bf(lo_f(d0) * rden, hi_f(d0) * rden), pk_bf(lo_f(d1) * rden, hi_f(d1) * rden)};
      *(u32x2*)&tile[r][(q + 32 * i) * 4] = w;
    }
    __syncthreads();
    const int mt = blockIdx.x >> 1, half = blockIdx.x & 1;
#pragma unroll
    for (int i = 0; i < 3; ++i) {
      int idx = t + 256 * i;
      if (idx < 640) {
        int kb = idx >> 5, g = (idx >> 3) & 3, fr = idx & 7;
        u32x2 a = *(const u32x2*)&tile[fr][kb * 32 + 4 * g];
        u32x2 b = *(const u32x2*)&tile[fr][kb * 32 + 16 + 4 * g];
        u32x4 w = {a[0], a[1], b[0], b[1]};
        *(u32x4*)(xnP + (((size_t)mt * 20 + kb) * 64 + g * 16 + half * 8 + fr) * 8) = w;
        u32x4 z;
#pragma unroll
        for (int j = 0; j < 4; ++j) {
          _Float16 ha = (_Float16)bf2f(tile[2 * j][idx]);
          _Float16 hb = (_Float16)bf2f(tile[2 * j + 1][idx]);
          z[j] = (u32)__builtin_bit_cast(u16, ha) | ((u32)__builtin_bit_cast(u16, hb) << 16);
        }
        *(u32x4*)(xT + ((size_t)blockIdx.x * DM + idx) * 8) = z;
      }
    }
    return;
  }
  if (blockIdx.x >= K1_LN + K1_KG) {
    int idx = (blockIdx.x - (K1_LN + K1_KG)) * 256 + t;
    if (idx < LUT_N) {
      int sgn = idx >= LUT_HALF;
      int a = idx - sgn * LUT_HALF + (LUT_E0 << 7);
      float h = bf2f((u16)(a | (sgn << 15)));
      lut[idx] = f2bf(mish_bf(h));
    } else if (idx < 6144) {
      lut[idx] = 0;
    }
    return;
  }
  const int ch = (blockIdx.x - K1_LN) * 2 + (wave >> 1);
  const int n = (wave & 1) * 64 + lane;
  char* W = k1s + wave * 16 * WPITCH;
  const float dt = bfr(expf(bf2f(logdt[ch])));
  const float ar = -bfr(expf(bf2f(lar[ch * NS + n]))) * dt;
  const float ai = bf2f(aim[ch * NS + n]) * dt;
  const float c0r = bf2f(cre[ch * NS + lane]) * dt, c0i = bf2f(cim[ch * NS + lane]) * dt;
  const float c1r = bf2f(cre[ch * NS + 64 + lane]) * dt, c1i = bf2f(cim[ch * NS + 64 + lane]) * dt;
  const float cr = (wave & 1) ? c1r : c0r, ci = (wave & 1) ? c1i : c0i;
  float bs = (fabsf(c0r) + fabsf(c0i)) + (fabsf(c1r) + fabsf(c1i));
#pragma unroll
  for (int o = 1; o < 64; o <<= 1) bs += __shfl_xor(bs, o);
  const float arl2 = ar * 1.4426950408889634f;
  const float w = ai * 0.15915494309189535f;
  float e1r, e1i, e8r, e8i, eHr, eHi;
  cexp_p(arl2, w, 1.0f, e1r, e1i);
  cexp_p(arl2, w, 8.0f, e8r, e8i);
  cexp_p(arl2, w, 128.0f, eHr, eHi);
  const int fi = lane & 15, g = lane >> 4;
  char* wr = W + lane * 8;
  const char* rd = W + fi * WPITCH + g * 16;
  {
    float lr = 1.f, li = 0.f;
    *(u32x2*)(wr) = split2(1.f, 0.f);
#pragma unroll
    for (int j = 1; j < 16; ++j) {
      cmul(lr, li, e8r, e8i, lr, li);
      *(u32x2*)(wr + j * WPITCH) = split2(lr, -li);
    }
  }
  WFENCE();
  u32x4 Bf[8], Bs[8];
#pragma unroll
  for (int kb = 0; kb < 8; ++kb) {
    Bf[kb] = *(const u32x4*)(rd + kb * 64);
    Bs[kb] = (u32x4){Bf[kb][1], Bf[kb][0], Bf[kb][3], Bf[kb][2]};
  }
  WFENCE();
  float Rr[8], Ri[8];
  Rr[0] = 1.f; Ri[0] = 0.f; Rr[1] = e1r; Ri[1] = e1i;
#pragma unroll
  for (int r = 2; r < 8; ++r) cmul(Rr[r - 1], Ri[r - 1], e1r, e1i, Rr[r], Ri[r]);
  f4 acc[3];
  float gr = cr, gi = ci;
  {
#pragma unroll
    for (int ii = 0; ii < 2; ++ii) {
      *(u32x2*)(wr + (ii * 8) * WPITCH) = split2(gr, gi);
#pragma unroll
      for (int r = 1; r < 8; ++r) {
        float hr, hi;
        cmul(gr, gi, Rr[r], Ri[r], hr, hi);
        *(u32x2*)(wr + (ii * 8 + r) * WPITCH) = split2(hr, hi);
      }
      cmul(gr, gi, eHr, eHi, gr, gi);
    }
    WFENCE();
    acc[0] = (f4){0.f, 0.f, 0.f, 0.f};
#pragma unroll
    for (int kb = 0; kb < 8; ++kb) {
      u32x4 a = *(const u32x4*)(rd + kb * 64);
      acc[0] = __builtin_amdgcn_mfma_f32_16x16x32_bf16(__builtin_bit_cast(s8, a), __builtin_bit_cast(s8, Bf[kb]), acc[0], 0, 0, 0);
      acc[0] = __builtin_amdgcn_mfma_f32_16x16x32_bf16(__builtin_bit_cast(s8, a), __builtin_bit_cast(s8, Bs[kb]), acc[0], 0, 0, 0);
    }
  }
  WFENCE();
  {
#pragma unroll
    for (int ii = 2; ii < 4; ++ii) {
      *(u32x2*)(wr + ((ii - 2) * 4) * WPITCH) = split2(gr, gi);
#pragma unroll
      for (int r = 1; r < 4; ++r) {
        float hr, hi;
        cmul(gr, gi, Rr[2 * r], Ri[2 * r], hr, hi);
        *(u32x2*)(wr + ((ii - 2) * 4 + r) * WPITCH) = split2(hr, hi);
      }
      cmul(gr, gi, eHr, eHi, gr, gi);
    }
#pragma unroll
    for (int ii = 4; ii < 8; ++ii) {
      *(u32x2*)(wr + (8 + (ii - 4) * 2) * WPITCH) = split2(gr, gi);
      float hr, hi;
      cmul(gr, gi, Rr[4], Ri[4], hr, hi);
      *(u32x2*)(wr + (8 + (ii - 4) * 2 + 1) * WPITCH) = split2(hr, hi);
      cmul(gr, gi, eHr, eHi, gr, gi);
    }
    WFENCE();
    acc[1] = (f4){0.f, 0.f, 0.f, 0.f};
#pragma unroll
    for (int kb = 0; kb < 8; ++kb) {
      u32x4 a = *(const u32x4*)(rd + kb * 64);
      acc[1] = __builtin_amdgcn_mfma_f32_16x16x32_bf16(__builtin_bit_cast(s8, a), __builtin_bit_cast(s8, Bf[kb]), acc[1], 0, 0, 0);
      acc[1] = __builtin_amdgcn_mfma_f32_16x16x32_bf16(__builtin_bit_cast(s8, a), __builtin_bit_cast(s8, Bs[kb]), acc[1], 0, 0, 0);
    }
  }
  WFENCE();
  {
#pragma unroll
    for (int ii = 8; ii < 17; ++ii) {
      *(u32x2*)(wr + (ii - 8) * WPITCH) = split2(gr, gi);
      if (ii < 16) cmul(gr, gi, eHr, eHi, gr, gi);
    }
    WFENCE();
    acc[2] = (f4){0.f, 0.f, 0.f, 0.f};
#pragma unroll
    for (int kb = 0; kb < 8; ++kb) {
      u32x4 a = *(const u32x4*)(rd + kb * 64);
      acc[2] = __builtin_amdgcn_mfma_f32_16x16x32_bf16(__builtin_bit_cast(s8, a), __builtin_bit_cast(s8, Bf[kb]), acc[2], 0, 0, 0);
      acc[2] = __builtin_amdgcn_mfma_f32_16x16x32_bf16(__builtin_bit_cast(s8, a), __builtin_bit_cast(s8, Bs[kb]), acc[2], 0, 0, 0);
    }
  }
  WFENCE();
  {
    float* P = (float*)W;
    const int i0 = (g >> 1) * 128 + (g & 1) * 4 + 8 * fi;
    const int i2 = 512 + 64 * g + fi;
#pragma unroll
    for (int r = 0; r < 4; ++r) {
      P[i0 + r] = acc[0][r];
      int i1 = g < 2 ? 256 + 64 * g + r + 4 * fi : 384 + 32 * (2 * (g - 2) + (r >> 1)) + (r & 1) + 2 * fi;
      P[i1] = acc[1][r];
      P[i2 + 16 * r] = acc[2][r];
    }
  }
  __syncthreads();
  const float bound = 2.0f * bs;
  int ex = 0;
  if (bound > 0.f && bound < 3.0e38f) { (void)frexpf(bound, &ex); }
  int e = 15 - ex;
  e = e > 100 ? 100 : (e < -100 ? -100 : e);
  const float sc2 = ldexpf(2.0f, e);
  if ((t & 127) == 0) kscale[ch] = ldexpf(1.0f, -e);
  const float* P0 = (const float*)(k1s + (wave & 2) * 16 * WPITCH);
  const float* P1 = (const float*)(k1s + ((wave & 2) + 1) * 16 * WPITCH);
  const int tl = t & 127;
#pragma unroll
  for (int i = 0; i < 2; ++i) {
    const int c = tl + 128 * i;
    const int m = 255 - c;
    const int sg = m >= 128 ? 3 : (m >= 64 ? 2 : (m >= 32 ? 1 : 0));
    const int start = sg == 0 ? 0 : (128 << sg);
    const int base = sg == 0 ? 0 : 128 + 128 * sg;
    const int one = sg ? 1 : 0;
    const int hm = ((1 << sg) >> 1) - one;
    u32x4 o;
#pragma unroll
    for (int j = 0; j < 4; ++j) {
      u32 pk = 0;
#pragma unroll
      for (int h = 0; h < 2; ++h) {
        int q = 8 * m + 7 - (2 * j + h) - start;
        int idx = base + ((q + hm + ((q >> sg) & one)) >> sg);
        float val = (P0[idx] + P1[idx]) * sc2;
        _Float16 hv = (_Float16)val;
        pk |= ((u32)__builtin_bit_cast(u16, hv)) << (16 * h);
      }
      o[j] = pk;
    }
    *(u32x4*)(Kr + (size_t)ch * KRLEN + c * 8) = o;
  }
  if (tl < 4) *(u32x4*)(Kr + (size_t)ch * KRLEN + (256 + tl) * 8) = (u32x4){0u, 0u, 0u, 0u};
}
#define KCPITCH 2080
__global__ __launch_bounds__(256) void conv_kernel(const u16* __restrict__ xT, const u16* __restrict__ Kr,
                                                   const float* __restrict__ kscale, u16* __restrict__ yT) {
  __shared__ __attribute__((aligned(16))) u16 Kc[8 * KCPITCH];
  __shared__ __attribute__((aligned(16))) u16 xs[256 + SEQ];
  const int d = blockIdx.x, t = threadIdx.x, wave = t >> 6, lane = t & 63;
  {
    u32x4 v = *(const u32x4*)(xT + ((size_t)t * DM + d) * 8);
    *(u32x4*)&xs[256 + t * 8] = v;
    if (t < 32) *(u32x4*)&xs[t * 8] = (u32x4){0u, 0u, 0u, 0u};
  }
  for (int c = t; c < 258; c += 256) {
    u32x4 lo = *(const u32x4*)(Kr + (size_t)d * KRLEN + c * 8);
    u32x4 hi = *(const u32x4*)(Kr + (size_t)d * KRLEN + c * 8 + 8);
    u32 wv[8] = {lo[0], lo[1], lo[2], lo[3], hi[0], hi[1], hi[2], hi[3]};
#pragma unroll
    for (int s = 0; s < 8; ++s) {
      u32x4 o;
#pragma unroll
      for (int j = 0; j < 4; ++j) {
        if ((s & 1) == 0) o[j] = wv[j + s / 2];
        else o[j] = __builtin_amdgcn_alignbit(wv[j + s / 2 + 1], wv[j + s / 2], 16);
      }
      *(u32x4*)&Kc[s * KCPITCH + c * 8] = o;
    }
  }
  __syncthreads();
  const int b = lane & 15, g = lane >> 4;
  const int s = (7 - b) & 7;
  f4 acc[8];
#pragma unroll
  for (int i = 0; i < 8; ++i) acc[i] = (f4){0.f, 0.f, 0.f, 0.f};
  const int abase = s * KCPITCH + (b < 8 ? 2024 : 2016) + 8 * g;
  const int bbase = 256 - 16 + 16 * b + 8 * g;
#pragma unroll
  for (int rr = 0; rr < 2; ++rr) {
    const int rp = wave * 2 + rr;
    h8 A[8], B[8];
#pragma unroll
    for (int u = 0; u < 8; ++u) A[u] = *(const h8*)&Kc[abase - 32 * rp - 256 * u];
#pragma unroll
    for (int v = 0; v < 8; ++v) B[v] = *(const h8*)&xs[bbase - 32 * rp + 256 * v];
#pragma unroll
    for (int u = 0; u < 8; ++u)
#pragma unroll
      for (int v = 0; v + u < 8; ++v)
        acc[u + v] = __builtin_amdgcn_mfma_f32_16x16x32_f16(A[u], B[v], acc[u + v], 0, 0, 0);
  }
  __syncthreads();
  float* red = (float*)Kc;
#pragma unroll
  for (int tt = 0; tt < 8; ++tt) *(f4*)&red[((wave * 8 + tt) * 64 + lane) * 4] = acc[tt];
  __syncthreads();
  const float sc = kscale[d];
#pragma unroll
  for (int i = 0; i < 2; ++i) {
    const int tt = wave * 2 + i;
    f4 s0 = *(const f4*)&red[((0 * 8 + tt) * 64 + lane) * 4];
    f4 s1 = *(const f4*)&red[((1 * 8 + tt) * 64 + lane) * 4];
    f4 s2 = *(const f4*)&red[((2 * 8 + tt) * 64 + lane) * 4];
    f4 s3 = *(const f4*)&red[((3 * 8 + tt) * 64 + lane) * 4];
    f4 sum = ((s0 + s1) + (s2 + s3)) * sc;
    u32x2 o;
    o[0] = (u32)f2bf(sum[0]) | ((u32)f2bf(sum[1]) << 16);
    o[1] = (u32)f2bf(sum[2]) | ((u32)f2bf(sum[3]) << 16);
    *(u32x2*)(yT + (size_t)d * SEQ + 256 * tt + 16 * b + 4 * g) = o;
  }
}

#define TRREAD(p) __builtin_amdgcn_ds_read_tr16_b64_v4i16((__attribute__((address_space(3))) s4*)(p))
#define STAMP(k)

__device__ __forceinline__ void wait_vm(int n) {
  switch (n) {
    case 0: WAIT_V(0); break; case 1: WAIT_V(1); break; case 2: WAIT_V(2); break; case 3: WAIT_V(3); break;
    case 4: WAIT_V(4); break; case 5: WAIT_V(5); break; case 6: WAIT_V(6); break; case 7: WAIT_V(7); break;
    case 8: WAIT_V(8); break; case 9: WAIT_V(9); break; case 10: WAIT_V(10); break; case 11: WAIT_V(11); break;
    case 12: WAIT_V(12); break; case 13: WAIT_V(13); break; case 14: WAIT_V(14); break; default: WAIT_V(15); break;
  }
}

template <int ND>
__global__ __launch_bounds__(512) void gemm1_kernel(const u16* __restrict__ xnP, const u16* __restrict__ w1,
                                                    const u16* __restrict__ b1, const u16* __restrict__ lut,
                                                    u16* __restrict__ actP) {
  constexpr int STG = 30 * 1024;
  constexpr int AUX = ND * STG;
  constexpr int NKT = DM / 64;
  __shared__ __attribute__((aligned(1024))) char shm[ND * STG + 13 * 1024];
  const int t = threadIdx.x, lane = t & 63;
  const int wave = __builtin_amdgcn_readfirstlane(t >> 6);
  STAMP(0);
  const int xcd = blockIdx.x & 7, idx = blockIdx.x >> 3;
  const int mi = idx & 3, ni = idx >> 2;
  const int bm = (xcd >> 1) * 4 + mi, bn = (xcd & 1) * 8 + ni;
  const int phase = ((5 * ni + 10 * mi) >> 2) % NKT;
  const int mt0 = bm * 8;
  int n0 = bn * 112;
  if (n0 + 112 > FF) n0 = FF - 112;
  const u16* pa[2];
  const u16* pb[2];
#pragma unroll
  for (int i = 0; i < 2; ++i) {
    int p = wave + 8 * i;
    pa[i] = xnP + ((size_t)(mt0 + (p >> 1)) * 20 + (p & 1)) * 512 + lane * 8;
    int c = p * 64 + lane;
    if (c > 14 * 64 - 1) c = 14 * 64 - 1;
    int rr = c / 14, cc = c % 14;
    pb[i] = w1 + (size_t)rr * FF + n0 + cc * 8;
  }
  const int cnt = wave < 6 ? 4 : 3;
  auto stage = [&](int kt, int buf) {
    int ko = kt + phase;
    if (ko >= NKT) ko -= NKT;
    char* sb = shm + buf * STG;
#pragma unroll
    for (int i = 0; i < 2; ++i) GLDS(pa[i] + ko * 1024, sb + (wave + 8 * i) * 1024);
    GLDS(pb[0] + (size_t)ko * 64 * FF, sb + 16384 + wave * 1024);
    if (wave < 6) GLDS(pb[1] + (size_t)ko * 64 * FF, sb + 16384 + (wave + 8) * 1024);
  };
  const int wm = wave & 3, wn = wave >> 2;
  f4 acc[2][4];
#pragma unroll
  for (int i = 0; i < 2; ++i)
#pragma unroll
    for (int j = 0; j < 4; ++j) acc[i][j] = (f4){0.f, 0.f, 0.f, 0.f};
  const int fr = lane & 15, g = lane >> 4, tq = (lane & 15) >> 2, tp = lane & 3;
  const int aoff = wm * 4096 + lane * 16;
  const int boff = 16384 + (4 * g + tq) * 224 + tp * 8 + wn * 128;
#pragma nounroll
  for (int s = 0; s < ND - 1; ++s) stage(s, s);
  GLDS(lut + wave * 512 + lane * 8, shm + AUX + wave * 1024);
  if (wave < 4) GLDS(lut + (wave + 8) * 512 + lane * 8, shm + AUX + (wave + 8) * 1024);
  else GLDS(b1 + n0 + (lane < 14 ? lane : 13) * 8, shm + AUX + 12 * 1024);
  int cur = 0, nxt = ND - 1;
  STAMP(1);
#pragma nounroll
  for (int kt = 0; kt < NKT; ++kt) {
    const int rem = (NKT - 1 - kt) < (ND - 2) ? (NKT - 1 - kt) : (ND - 2);
    wait_vm(rem * cnt + (kt < ND - 1 ? 2 : 0));
    RAW_BARRIER();
    if (kt == 0) STAMP(2);
    if (kt + ND - 1 < NKT) stage(kt + ND - 1, nxt);
    const char* sb = shm + cur * STG;
    cur = cur == ND - 1 ? 0 : cur + 1;
    nxt = nxt == ND - 1 ? 0 : nxt + 1;
#pragma unroll
    for (int ks = 0; ks < 2; ++ks) {
      s8 af[2];
#pragma unroll
      for (int i = 0; i < 2; ++i) af[i] = *(const s8*)(sb + aoff + i * 2048 + ks * 1024);
#pragma unroll
      for (int j = 0; j < 4; ++j) {
        if (j < 3 || wn == 0) {
          const char* p0 = sb + boff + ks * 32 * 224 + j * 32;
          s4 b0 = TRREAD(p0);
          s4 b1v = TRREAD(p0 + 16 * 224);
          s8 bf = {b0[0], b0[1], b0[2], b0[3], b1v[0], b1v[1], b1v[2], b1v[3]};
#pragma unroll
          for (int i = 0; i < 2; ++i)
            acc[i][j] = __builtin_amdgcn_mfma_f32_16x16x32_bf16(bf, af[i], acc[i][j], 0, 0, 0);
        }
      }
    }
  }
  STAMP(7);
  const u16* slut = (const u16*)(shm + AUX);
  const u16* sbias = (const u16*)(shm + AUX + 12 * 1024);
#pragma unroll
  for (int j = 0; j < 4; ++j) {
    if (j < 3 || wn == 0) {
      u32x2 bb = *(const u32x2*)(sbias + wn * 64 + j * 16 + 4 * g);
      const float bv[4] = {lo_f(bb[0]), hi_f(bb[0]), lo_f(bb[1]), hi_f(bb[1])};
      const int n = n0 + wn * 64 + j * 16;
      const int kb = n >> 5, half = (n >> 4) & 1;
#pragma unroll
      for (int i = 0; i < 2; ++i) {
        u32 t0 = pk_bf(acc[i][j][0], acc[i][j][1]);
        u32 t1 = pk_bf(acc[i][j][2], acc[i][j][3]);
        u32 h0 = pk_bf(lo_f(t0) + bv[0], hi_f(t0) + bv[1]);
        u32 h1 = pk_bf(lo_f(t1) + bv[2], hi_f(t1) + bv[3]);
        u32 hb[4] = {h0 & 0xFFFFu, h0 >> 16, h1 & 0xFFFFu, h1 >> 16};
        u32 res[4];
#pragma unroll
        for (int r = 0; r < 4; ++r) {
          u32 a = hb[r] & 0x7FFFu;
          u32 li = a - (LUT_E0 << 7);
          bool in = li < (u32)LUT_HALF;
          u32 v = slut[(in ? li : 0u) + (hb[r] >> 15) * LUT_HALF];
          float h = __uint_as_float(hb[r] << 16);
          float fb = (a < (LUT_E0 << 7)) ? h * 0.59765625f : (h > 0.f ? h : h * 0.0f);
          u32 fbb = pk_bf(fb, fb) & 0xFFFFu;
          res[r] = in ? v : fbb;
        }
        u32x2 ov = {res[0] | (res[1] << 16), res[2] | (res[3] << 16)};
        const int mt = mt0 + wm * 2 + i;
        if (n >= bn * 112) *(u32x2*)(actP + (((size_t)mt * 54 + kb) * 64 + lane) * 8 + half * 4) = ov;
      }
    }
  }
  STAMP(8);
}

template <int ND>
__global__ __launch_bounds__(512) void gemm2_kernel(const u16* __restrict__ actP, const u16* __restrict__ w2,
                                                    const u16* __restrict__ xin, const float2* __restrict__ stats,
                                                    const u16* __restrict__ yT, const u16* __restrict__ dskip,
                                                    u16* __restrict__ out) {
  constexpr int STG = 18 * 1024;
  constexpr int AUX = ND * STG;
  constexpr int NKT = FF / 64;
  __shared__ __attribute__((aligned(1024))) char shm[ND * STG + 21 * 1024];
  const int t = threadIdx.x, lane = t & 63;
  const int wave = __builtin_amdgcn_readfirstlane(t >> 6);
  const int xcd = blockIdx.x & 7, idx = blockIdx.x >> 3;
  const int mi = idx & 7, ni = idx >> 3;
  const int bm = (xcd >> 1) * 8 + mi, bn = (xcd & 1) * 4 + ni;
  const int phase = ((27 * mi + 54 * ni) >> 3) % NKT;
  const int mt0 = bm * 4, m0 = bm * 64, n0 = bn * 80;
  STAMP(0);
  const u16* pa = actP + ((size_t)(mt0 + (wave >> 1)) * 54 + (wave & 1)) * 512 + lane * 8;
  const u16* pb[2];
#pragma unroll
  for (int i = 0; i < 2; ++i) {
    int c = (wave + 8 * i) * 64 + lane;
    if (c > 639) c = 639;
    pb[i] = w2 + (size_t)(c / 10) * DM + n0 + (c % 10) * 8;
  }
  const int cnt = wave < 2 ? 3 : 2;
  auto stage = [&](int kt, int buf) {
    int ko = kt + phase;
    if (ko >= NKT) ko -= NKT;
    char* sb = shm + buf * STG;
    GLDS(pa + ko * 1024, sb + wave * 1024);
    GLDS(pb[0] + (size_t)ko * 64 * DM, sb + 8192 + wave * 1024);
    if (wave < 2) GLDS(pb[1] + (size_t)ko * 64 * DM, sb + 8192 + (wave + 8) * 1024);
  };
  const int wm = wave & 3, wn = wave >> 2;
  f4 acc[3];
#pragma unroll
  for (int j = 0; j < 3; ++j) acc[j] = (f4){0.f, 0.f, 0.f, 0.f};
  const int fr = lane & 15, g = lane >> 4, tq = (lane & 15) >> 2, tp = lane & 3;
  const int aoff = wm * 2048 + lane * 16;
  const int boff = 8192 + (4 * g + tq) * 160 + tp * 8 + wn * 96;
#pragma nounroll
  for (int s = 0; s < ND - 1; ++s) stage(s, s);
  int cur = 0, nxt = ND - 1;
  STAMP(1);
#pragma nounroll
  for (int kt = 0; kt < NKT; ++kt) {
    const int rem = (NKT - 1 - kt) < (ND - 2) ? (NKT - 1 - kt) : (ND - 2);
    wait_vm(rem * cnt);
    RAW_BARRIER();
    if (kt == 0) STAMP(2);
    if (kt == 4) STAMP(3);
    if (kt == 8) STAMP(4);
    if (kt == 16) STAMP(5);
    if (kt == 26) STAMP(6);
    if (kt + ND - 1 < NKT) stage(kt + ND - 1, nxt);
    else if (kt + ND - 1 == NKT) {
#pragma unroll
      for (int i = 0; i < 3; ++i) {
        int p = wave + 8 * i;
        if (p > 20) p = 20;
        int c = (p < 10 ? p : p - 10) * 64 + lane;
        const u16* src;
        if (p < 10) src = xin + (size_t)(m0 + c / 10) * DM + n0 + (c % 10) * 8;
        else if (p < 20) src = yT + (size_t)(n0 + (c >> 3)) * SEQ + m0 + (c & 7) * 8;
        else src = (lane < 32) ? (const u16*)(stats + m0) + lane * 8 : dskip + n0 + (lane < 42 ? lane - 32 : 9) * 8;
        GLDS(src, shm + AUX + p * 1024);
      }
    }
    const char* sb = shm + cur * STG;
    cur = cur == ND - 1 ? 0 : cur + 1;
    nxt = nxt == ND - 1 ? 0 : nxt + 1;
#pragma unroll
    for (int ks = 0; ks < 2; ++ks) {
      s8 af = *(const s8*)(sb + aoff + ks * 1024);
#pragma unroll
      for (int j = 0; j < 3; ++j) {
        if (j < 2 || wn == 0) {
          const char* p0 = sb + boff + ks * 32 * 160 + j * 32;
          s4 b0 = TRREAD(p0);
          s4 b1v = TRREAD(p0 + 16 * 160);
          s8 bf = {b0[0], b0[1], b0[2], b0[3], b1v[0], b1v[1], b1v[2], b1v[3]};
          acc[j] = __builtin_amdgcn_mfma_f32_16x16x32_bf16(bf, af, acc[j], 0, 0, 0);
        }
      }
    }
  }
  STAMP(7);
  const char* sx = shm + AUX;
  const char* sy = shm + AUX + 10 * 1024;
  const char* sm = shm + AUX + 20 * 1024;
  const int ml = wm * 16 + fr;
  const float2 st = *(const float2*)(sm + ml * 8);
#pragma unroll
  for (int j = 0; j < 3; ++j) {
    if (j < 2 || wn == 0) {
      const int nl = wn * 48 + j * 16;
      u32x2 xx = *(const u32x2*)(sx + ml * 160 + (nl + 4 * g) * 2);
      u32x2 dd = *(const u32x2*)(sm + 512 + (nl + 4 * g) * 2);
      s4 yy = TRREAD(sy + (nl + 4 * g + tq) * 128 + (wm * 16 + 4 * tp) * 2);
      const float xv[4] = {lo_f(xx[0]), hi_f(xx[0]), lo_f(xx[1]), hi_f(xx[1])};
      const float dv[4] = {lo_f(dd[0]), hi_f(dd[0]), lo_f(dd[1]), hi_f(dd[1])};
      float o[4];
#pragma unroll
      for (int r = 0; r < 4; ++r) {
        float xn = bfr(bfr(xv[r] - st.x) * st.y);
        float y = bf2f((u16)yy[r]);
        float a = bfr(y + bfr(xn * dv[r]));
        float s1 = bfr(xv[r] + a);
        o[r] = s1 + bfr(acc[j][r]);
      }
      u32x2 ov = {pk_bf(o[0], o[1]), pk_bf(o[2], o[3])};
      *(u32x2*)(out + (size_t)(m0 + ml) * DM + n0 + nl + 4 * g) = ov;
    }
  }
  STAMP(8);
}

extern "C" void kernel_launch(void* const* d_in, const int* in_sizes, int n_in,
                              void* d_out, int out_size, void* d_ws, size_t ws_size,
                              hipStream_t stream) {
  const u16* u = (const u16*)d_in[0];
  const u16* lar = (const u16*)d_in[1];
  const u16* aim = (const u16*)d_in[2];
  const u16* cre = (const u16*)d_in[3];
  const u16* cim = (const u16*)d_in[4];
  const u16* logdt = (const u16*)d_in[5];
  const u16* dskip = (const u16*)d_in[6];
  const u16* w1 = (const u16*)d_in[8];
  const u16* b1 = (const u16*)d_in[9];
  const u16* w2 = (const u16*)d_in[10];
  u16* out = (u16*)d_out;
  char* ws = (char*)d_ws;
  if (ws_size < (16u << 20) + (size_t)SEQ * FF * 2) return;
  u16* xnP = (u16*)(ws);
  u16* xT = (u16*)(ws + (4u << 20));
  u16* Kr = (u16*)(ws + (8u << 20));
  float* ksc = (float*)(ws + (12u << 20));
  float2* stats = (float2*)(ws + (12u << 20) + 4096);
  u16* lut = (u16*)(ws + (12u << 20) + 32768);
  u16* yT = (u16*)(ws + (13u << 20));
  u16* actP = (u16*)(ws + (16u << 20));

  k1_kernel<<<K1_LN + K1_KG + K1_LUT, 256, 0, stream>>>(u, xnP, xT, stats, lut, lar, aim, cre, cim, logdt, Kr, ksc);
  conv_kernel<<<DM, 256, 0, stream>>>(xT, Kr, ksc, yT);
  gemm1_kernel<4><<<256, 512, 0, stream>>>(xnP, w1, b1, lut, actP);
  gemm2_kernel<7><<<256, 512, 0, stream>>>(actP, w2, u, stats, yT, dskip, out);
}
```

```cpp
#include <hip/hip_runtime.h>
#include <hip/hip_bf16.h>

#define SEQ 2048
#define DM 640
#define NS 128
#define FF 1728

typedef unsigned short u16;
typedef short s4 __attribute__((ext_vector_type(4)));
typedef short s8 __attribute__((ext_vector_type(8)));
typedef _Float16 h8 __attribute__((ext_vector_type(8)));
typedef float f4 __attribute__((ext_vector_type(4)));
typedef unsigned int u32;
typedef unsigned int u32x4 __attribute__((ext_vector_type(4)));
typedef unsigned int u32x2 __attribute__((ext_vector_type(2)));

__device__ __forceinline__ float bf2f(u16 v) { return __uint_as_float(((u32)v) << 16); }
__device__ __forceinline__ u16 f2bf(float f) {
  u32 u = __float_as_uint(f);
  u32 r = (u + 0x7FFFu + ((u >> 16) & 1u)) >> 16;
  return (u16)r;
}
__device__ __forceinline__ float bfr(float f) { return bf2f(f2bf(f)); }

#define STAMPB(kid, base)
#define STAMP(k)
typedef __bf16 bf2_t __attribute__((ext_vector_type(2)));
typedef float f2_t __attribute__((ext_vector_type(2)));
__device__ __forceinline__ u32 pk_bf(float a, float b) {
  f2_t v = {a, b};
  bf2_t r = __builtin_convertvector(v, bf2_t);
  return __builtin_bit_cast(u32, r);
}
__device__ __forceinline__ float lo_f(u32 w) { return __uint_as_float(w << 16); }
__device__ __forceinline__ float hi_f(u32 w) { return __uint_as_float(w & 0xFFFF0000u); }

#define WAIT_V(n) asm volatile("s_waitcnt vmcnt(%0)" ::"n"(n) : "memory")
#define RAW_BARRIER() do { asm volatile("s_waitcnt lgkmcnt(0)" ::: "memory"); __builtin_amdgcn_s_barrier(); } while (0)
__device__ __forceinline__ void glds16(const void* gsrc, const void* lp) {
  unsigned keep;
  unsigned lds_dst = (unsigned)(size_t)(const __attribute__((address_space(3))) char*)lp;
  asm volatile("s_mov_b32 %0, m0\n\ts_mov_b32 m0, %2\n\ts_nop 0\n\tglobal_load_lds_dwordx4 %1, off\n\ts_mov_b32 m0, %0"
               : "=&s"(keep) : "v"(gsrc), "s"(lds_dst) : "memory");
}
#define GLDS(gp, lp) glds16((const void*)(gp), (const void*)(lp))

#define LUT_E0 111
#define LUT_E1 134
#define LUT_HALF ((LUT_E1 - LUT_E0) * 128)
#define LUT_N (2 * LUT_HALF)

__device__ __forceinline__ float mish_bf(float h) {
  float e = bfr(expf(-fabsf(h)));
  float l = bfr(log1pf(e));
  float sp = bfr(l + (h > 0.f ? h : 0.f));
  float th = bfr(tanhf(sp));
  return bfr(h * th);
}

#define KRLEN 2080
#define WFENCE() do { asm volatile("" ::: "memory"); __builtin_amdgcn_wave_barrier(); asm volatile("" ::: "memory"); } while (0)
__device__ __forceinline__ void cexp_p(float arl2, float w, float p, float& re, float& im) {
  float mag = __builtin_amdgcn_exp2f(arl2 * p);
  float rev = w * p;
  rev = rev - floorf(rev);
  re = mag * __builtin_amdgcn_cosf(rev);
  im = mag * __builtin_amdgcn_sinf(rev);
}
__device__ __forceinline__ void cmul(float ar, float ai, float br, float bi, float& cr, float& ci) {
  float r = fmaf(ar, br, -(ai * bi));
  float i = fmaf(ar, bi, ai * br);
  cr = r; ci = i;
}
__device__ __forceinline__ u32x2 split2(float a, float b) {
  u32 h = pk_bf(a, b);
  u32 l = pk_bf(a - lo_f(h), b - hi_f(h));
  return (u32x2){h, l};
}

#define K1_LN 256
#define K1_KG 320
#define K1_LUT 24
#define K1_RP 135
#define WPITCH 544
#define TRREAD(p) __builtin_amdgcn_ds_read_tr16_b64_v4i16((__attribute__((address_space(3))) s4*)(p))

__device__ __forceinline__ void repack_load(const u16* __restrict__ w, int N, int ku, int nu, char* lds, int tl) {
#pragma unroll
  for (int i = 0; i < 2; ++i) {
    int idx = tl + 256 * i, r = idx >> 3, c = idx & 7;
    u32x4 v = *(const u32x4*)(w + (size_t)(ku * 64 + r) * N + nu * 64 + c * 8);
    *(u32x4*)(lds + r * 160 + c * 16) = v;
  }
}
__device__ __forceinline__ void repack_store(u16* __restrict__ wP, int KB, int ku, int nu, const char* lds, int tl) {
  const int wl = tl >> 6, lane = tl & 63, g = lane >> 4, tq = (lane & 15) >> 2, tp = lane & 3;
#pragma unroll
  for (int kb2 = 0; kb2 < 2; ++kb2) {
    const char* p0 = lds + (kb2 * 32 + 4 * g + tq) * 160 + wl * 32 + tp * 8;
    s4 b0 = TRREAD(p0);
    s4 b1 = TRREAD(p0 + 16 * 160);
    s8 bf = {b0[0], b0[1], b0[2], b0[3], b1[0], b1[1], b1[2], b1[3]};
    *(s8*)(wP + (((size_t)(nu * 4 + wl) * KB + ku * 2 + kb2) * 64 + lane) * 8) = bf;
  }
}

__global__ __launch_bounds__(256) void k1_kernel(const u16* __restrict__ u, u16* __restrict__ xnP,
                                                 float2* __restrict__ stats,
                                                 u16* __restrict__ lut,
                                                 const u16* __restrict__ lar, const u16* __restrict__ aim,
                                                 const u16* __restrict__ cre, const u16* __restrict__ cim,
                                                 const u16* __restrict__ logdt, u16* __restrict__ Kr,
                                                 float* __restrict__ kscale,
                                                 const u16* __restrict__ w1, u16* __restrict__ w1P) {
  __shared__ __attribute__((aligned(16))) char k1s[4 * 16 * WPITCH];
  const int t = threadIdx.x, lane = t & 63;
  const int wave = __builtin_amdgcn_readfirstlane(t >> 6);
  STAMPB(0, xnP);
  STAMP(0);
  if (blockIdx.x < K1_LN) {
    u16 (*tile)[DM + 8] = (u16 (*)[DM + 8])k1s;
    const int r = wave * 2 + (lane >> 5), q = lane & 31;
    const int row = blockIdx.x * 8 + r;
    float v[20];
    const u32x2* src = (const u32x2*)(u + (size_t)row * DM);
#pragma unroll
    for (int i = 0; i < 5; ++i) {
      u32x2 w = src[q + 32 * i];
      v[i * 4 + 0] = lo_f(w[0]); v[i * 4 + 1] = hi_f(w[0]);
      v[i * 4 + 2] = lo_f(w[1]); v[i * 4 + 3] = hi_f(w[1]);
    }
    float s = 0.f;
#pragma unroll
    for (int i = 0; i < 20; ++i) s += v[i];
    STAMP(1);
#pragma unroll
    for (int o = 1; o < 32; o <<= 1) s += __shfl_xor(s, o);
    const float mean = s * (1.0f / DM);
    float ss = 0.f;
#pragma unroll
    for (int i = 0; i < 20; ++i) { float d = v[i] - mean; ss += d * d; }
#pragma unroll
    for (int o = 1; o < 32; o <<= 1) ss += __shfl_xor(ss, o);
    const float var = ss * (1.0f / DM);
    const float mb = bfr(mean);
    const float vb = bfr(var);
    const float den = bfr(sqrtf(bfr(vb + 1e-5f)));
    const float rden = 1.0f / den;
    if (q == 0) stats[row] = make_float2(mb, rden);
#pragma unroll
    for (int i = 0; i < 5; ++i) {
      u32 d0 = pk_bf(v[i * 4 + 0] - mb, v[i * 4 + 1] - mb);
      u32 d1 = pk_bf(v[i * 4 + 2] - mb, v[i * 4 + 3] - mb);
      u32x2 w = {pk_bf(lo_f(d0) * rden, hi_f(d0) * rden), pk_bf(lo_f(d1) * rden, hi_f(d1) * rden)};
      *(u32x2*)&tile[r][(q + 32 * i) * 4] = w;
    }
    __syncthreads();
    STAMP(2);
    const int mt = blockIdx.x >> 1, half = blockIdx.x & 1;
#pragma unroll
    for (int i = 0; i < 3; ++i) {
      int idx = t + 256 * i;
      if (idx < 640) {
        int kb = idx >> 5, g = (idx >> 3) & 3, fr = idx & 7;
        u32x2 a = *(const u32x2*)&tile[fr][kb * 32 + 4 * g];
        u32x2 b = *(const u32x2*)&tile[fr][kb * 32 + 16 + 4 * g];
        u32x4 w = {a[0], a[1], b[0], b[1]};
        *(u32x4*)(xnP + (((size_t)mt * 20 + kb) * 64 + g * 16 + half * 8 + fr) * 8) = w;
      }
    }
    STAMP(3);
    return;
  }
  if (blockIdx.x >= K1_LN + K1_KG + K1_LUT) {
    const int u0 = (blockIdx.x - (K1_LN + K1_KG + K1_LUT)) * 2;
    repack_load(w1, FF, u0 / 27, u0 % 27, k1s, t);
    repack_load(w1, FF, (u0 + 1) / 27, (u0 + 1) % 27, k1s + 10240, t);
    __syncthreads();
    repack_store(w1P, DM / 32, u0 / 27, u0 % 27, k1s, t);
    repack_store(w1P, DM / 32, (u0 + 1) / 27, (u0 + 1) % 27, k1s + 10240, t);
    STAMP(3);
    return;
  }
  if (blockIdx.x >= K1_LN + K1_KG) {
    int idx = (blockIdx.x - (K1_LN + K1_KG)) * 256 + t;
    if (idx < LUT_N) {
      int sgn = idx >= LUT_HALF;
      int a = idx - sgn * LUT_HALF + (LUT_E0 << 7);
      float h = bf2f((u16)(a | (sgn << 15)));
      lut[idx] = f2bf(mish_bf(h));
    } else if (idx < 6144) {
      lut[idx] = 0;
    }
    return;
  }
  const int ch = (blockIdx.x - K1_LN) * 2 + (wave >> 1);
  const int n = (wave & 1) * 64 + lane;
  char* W = k1s + wave * 16 * WPITCH;
  const float dt = bfr(expf(bf2f(logdt[ch])));
  const float ar = -bfr(expf(bf2f(lar[ch * NS + n]))) * dt;
  const float ai = bf2f(aim[ch * NS + n]) * dt;
  const float c0r = bf2f(cre[ch * NS + lane]) * dt, c0i = bf2f(cim[ch * NS + lane]) * dt;
  const float c1r = bf2f(cre[ch * NS + 64 + lane]) * dt, c1i = bf2f(cim[ch * NS + 64 + lane]) * dt;
  const float cr = (wave & 1) ? c1r : c0r, ci = (wave & 1) ? c1i : c0i;
  float bs = (fabsf(c0r) + fabsf(c0i)) + (fabsf(c1r) + fabsf(c1i));
#pragma unroll
  for (int o = 1; o < 64; o <<= 1) bs += __shfl_xor(bs, o);
  STAMP(1);
  const float arl2 = ar * 1.4426950408889634f;
  const float w = ai * 0.15915494309189535f;
  float e1r, e1i, e8r, e8i, eHr, eHi;
  cexp_p(arl2, w, 1.0f, e1r, e1i);
  cexp_p(arl2, w, 8.0f, e8r, e8i);
  cexp_p(arl2, w, 128.0f, eHr, eHi);
  const int fi = lane & 15, g = lane >> 4;
  char* wr = W + lane * 8;
  const char* rd = W + fi * WPITCH + g * 16;
  {
    float lr = 1.f, li = 0.f;
    *(u32x2*)(wr) = split2(1.f, 0.f);
#pragma unroll
    for (int j = 1; j < 16; ++j) {
      cmul(lr, li, e8r, e8i, lr, li);
      *(u32x2*)(wr + j * WPITCH) = split2(lr, -li);
    }
  }
  WFENCE();
  u32x4 Bf[8], Bs[8];
#pragma unroll
  for (int kb = 0; kb < 8; ++kb) {
    Bf[kb] = *(const u32x4*)(rd + kb * 64);
    Bs[kb] = (u32x4){Bf[kb][1], Bf[kb][0], Bf[kb][3], Bf[kb][2]};
  }
  WFENCE();
  STAMP(2);
  float Rr[8], Ri[8];
  Rr[0] = 1.f; Ri[0] = 0.f; Rr[1] = e1r; Ri[1] = e1i;
#pragma unroll
  for (int r = 2; r < 8; ++r) cmul(Rr[r - 1], Ri[r - 1], e1r, e1i, Rr[r], Ri[r]);
  f4 acc[3];
  float gr = cr, gi = ci;
  {
#pragma unroll
    for (int ii = 0; ii < 2; ++ii) {
      *(u32x2*)(wr + (ii * 8) * WPITCH) = split2(gr, gi);
#pragma unroll
      for (int r = 1; r < 8; ++r) {
        float hr, hi;
        cmul(gr, gi, Rr[r], Ri[r], hr, hi);
        *(u32x2*)(wr + (ii * 8 + r) * WPITCH) = split2(hr, hi);
      }
      cmul(gr, gi, eHr, eHi, gr, gi);
    }
    WFENCE();
    acc[0] = (f4){0.f, 0.f, 0.f, 0.f};
#pragma unroll
    for (int kb = 0; kb < 8; ++kb) {
      u32x4 a = *(const u32x4*)(rd + kb * 64);
      acc[0] = __builtin_amdgcn_mfma_f32_16x16x32_bf16(__builtin_bit_cast(s8, a), __builtin_bit_cast(s8, Bf[kb]), acc[0], 0, 0, 0);
      acc[0] = __builtin_amdgcn_mfma_f32_16x16x32_bf16(__builtin_bit_cast(s8, a), __builtin_bit_cast(s8, Bs[kb]), acc[0], 0, 0, 0);
    }
  }
  WFENCE();
  STAMP(3);
  {
#pragma unroll
    for (int ii = 2; ii < 4; ++ii) {
      *(u32x2*)(wr + ((ii - 2) * 4) * WPITCH) = split2(gr, gi);
#pragma unroll
      for (int r = 1; r < 4; ++r) {
        float hr, hi;
        cmul(gr, gi, Rr[2 * r], Ri[2 * r], hr, hi);
        *(u32x2*)(wr + ((ii - 2) * 4 + r) * WPITCH) = split2(hr, hi);
      }
      cmul(gr, gi, eHr, eHi, gr, gi);
    }
#pragma unroll
    for (int ii = 4; ii < 8; ++ii) {
      *(u32x2*)(wr + (8 + (ii - 4) * 2) * WPITCH) = split2(gr, gi);
      float hr, hi;
      cmul(gr, gi, Rr[4], Ri[4], hr, hi);
      *(u32x2*)(wr + (8 + (ii - 4) * 2 + 1) * WPITCH) = split2(hr, hi);
      cmul(gr, gi, eHr, eHi, gr, gi);
    }
    WFENCE();
    acc[1] = (f4){0.f, 0.f, 0.f, 0.f};
#pragma unroll
    for (int kb = 0; kb < 8; ++kb) {
      u32x4 a = *(const u32x4*)(rd + kb * 64);
      acc[1] = __builtin_amdgcn_mfma_f32_16x16x32_bf16(__builtin_bit_cast(s8, a), __builtin_bit_cast(s8, Bf[kb]), acc[1], 0, 0, 0);
      acc[1] = __builtin_amdgcn_mfma_f32_16x16x32_bf16(__builtin_bit_cast(s8, a), __builtin_bit_cast(s8, Bs[kb]), acc[1], 0, 0, 0);
    }
  }
  WFENCE();
  STAMP(4);
  {
#pragma unroll
    for (int ii = 8; ii < 17; ++ii) {
      *(u32x2*)(wr + (ii - 8) * WPITCH) = split2(gr, gi);
      if (ii < 16) cmul(gr, gi, eHr, eHi, gr, gi);
    }
    WFENCE();
    acc[2] = (f4){0.f, 0.f, 0.f, 0.f};
#pragma unroll
    for (int kb = 0; kb < 8; ++kb) {
      u32x4 a = *(const u32x4*)(rd + kb * 64);
      acc[2] = __builtin_amdgcn_mfma_f32_16x16x32_bf16(__builtin_bit_cast(s8, a), __builtin_bit_cast(s8, Bf[kb]), acc[2], 0, 0, 0);
      acc[2] = __builtin_amdgcn_mfma_f32_16x16x32_bf16(__builtin_bit_cast(s8, a), __builtin_bit_cast(s8, Bs[kb]), acc[2], 0, 0, 0);
    }
  }
  WFENCE();
  STAMP(5);
  {
    float* P = (float*)W;
    const int i0 = (g >> 1) * 128 + (g & 1) * 4 + 8 * fi;
    const int i2 = 512 + 64 * g + fi;
#pragma unroll
    for (int r = 0; r < 4; ++r) {
      P[i0 + r] = acc[0][r];
      int i1 = g < 2 ? 256 + 64 * g + r + 4 * fi : 384 + 32 * (2 * (g - 2) + (r >> 1)) + (r & 1) + 2 * fi;
      P[i1] = acc[1][r];
      P[i2 + 16 * r] = acc[2][r];
    }
  }
  __syncthreads();
  STAMP(6);
  const float bound = 2.0f * bs;
  int ex = 0;
  if (bound > 0.f && bound < 3.0e38f) { (void)frexpf(bound, &ex); }
  int e = 15 - ex;
  e = e > 100 ? 100 : (e < -100 ? -100 : e);
  const float sc2 = ldexpf(2.0f, e);
  if ((t & 127) == 0) kscale[ch] = ldexpf(1.0f, -e);
  const float* P0 = (const float*)(k1s + (wave & 2) * 16 * WPITCH);
  const float* P1 = (const float*)(k1s + ((wave & 2) + 1) * 16 * WPITCH);
  const int tl = t & 127;
#pragma unroll
  for (int i = 0; i < 2; ++i) {
    const int c = tl + 128 * i;
    const int m = 255 - c;
    const int sg = m >= 128 ? 3 : (m >= 64 ? 2 : (m >= 32 ? 1 : 0));
    const int start = sg == 0 ? 0 : (128 << sg);
    const int base = sg == 0 ? 0 : 128 + 128 * sg;
    const int one = sg ? 1 : 0;
    const int hm = ((1 << sg) >> 1) - one;
    u32x4 o;
#pragma unroll
    for (int j = 0; j < 4; ++j) {
      u32 pk = 0;
#pragma unroll
      for (int h = 0; h < 2; ++h) {
        int q = 8 * m + 7 - (2 * j + h) - start;
        int idx = base + ((q + hm + ((q >> sg) & one)) >> sg);
        float val = (P0[idx] + P1[idx]) * sc2;
        _Float16 hv = (_Float16)val;
        pk |= ((u32)__builtin_bit_cast(u16, hv)) << (16 * h);
      }
      o[j] = pk;
    }
    *(u32x4*)(Kr + (size_t)ch * KRLEN + c * 8) = o;
  }
  if (tl < 4) *(u32x4*)(Kr + (size_t)ch * KRLEN + (256 + tl) * 8) = (u32x4){0u, 0u, 0u, 0u};
  STAMP(7);
}
#define K2_G1 256
#define K2_CONV 320
#define K2_RP 135
#define KCPITCH 2080
#define CONV_BYTES (8 * KCPITCH * 2 + (256 + SEQ) * 2)
__global__ __launch_bounds__(512, 4) void k2_kernel(const u16* __restrict__ xnP, const u16* __restrict__ w1P,
                                                    const u16* __restrict__ b1, const u16* __restrict__ lut,
                                                    u16* __restrict__ actP, const u16* __restrict__ Kr,
                                                    const float* __restrict__ kscale, u16* __restrict__ yT,
                                                    const u16* __restrict__ w2, u16* __restrict__ w2P) {
  constexpr int ND = 5;
  constexpr int STG = 15 * 1024;
  constexpr int NKT = DM / 32;
  __shared__ __attribute__((aligned(1024))) char shm[ND * STG];
  const int t = threadIdx.x, lane = t & 63;
  const int wave = __builtin_amdgcn_readfirstlane(t >> 6);
  STAMPB(2, (char*)actP - (16u << 20));
  STAMP(0);
  if (blockIdx.x >= K2_G1 + K2_CONV) {
    const int un = (blockIdx.x - (K2_G1 + K2_CONV)) * 2 + (wave >> 2);
    const int ku = un / 10, nu = un % 10;
    char* lds = shm + (wave >> 2) * 10240;
    repack_load(w2, DM, ku, nu, lds, t & 255);
    __syncthreads();
    repack_store(w2P, FF / 32, ku, nu, lds, t & 255);
    STAMP(8);
    return;
  }
  if (blockIdx.x >= K2_G1) {
    const int cb = blockIdx.x - K2_G1;
    const int pair = (cb & 7) * 40 + (cb >> 3);
    const int hsel = wave >> 2, w4 = wave & 3, tl = t & 255;
    const int grp = pair >> 2, sub = pair & 3;
    const int kb = grp >> 2, gg = grp & 3;
    const int d0 = kb * 32 + (sub >> 1) * 16 + gg * 4 + (sub & 1) * 2;
    const int d = d0 + hsel;
    u16* Kc = (u16*)(shm + hsel * CONV_BYTES);
    u16* xs = Kc + 8 * KCPITCH;
    u16* xsA = (u16*)(shm) + 8 * KCPITCH;
    u16* xsB = (u16*)(shm + CONV_BYTES) + 8 * KCPITCH;
    u32 xv[4];
#pragma unroll
    for (int i = 0; i < 4; ++i) {
      int row = t + 512 * i;
      xv[i] = *(const u32*)(xnP + (((size_t)(row >> 4) * 20 + kb) * 64 + gg * 16 + (row & 15)) * 8 + sub * 2);
    }
    if (tl < 32) *(u32x4*)&xs[tl * 8] = (u32x4){0u, 0u, 0u, 0u};
    for (int c = tl; c < 258; c += 256) {
      u32x4 lo = *(const u32x4*)(Kr + (size_t)d * KRLEN + c * 8);
      u32x4 hi = *(const u32x4*)(Kr + (size_t)d * KRLEN + c * 8 + 8);
      u32 wv[8] = {lo[0], lo[1], lo[2], lo[3], hi[0], hi[1], hi[2], hi[3]};
#pragma unroll
      for (int s = 0; s < 8; ++s) {
        u32x4 o;
#pragma unroll
        for (int j = 0; j < 4; ++j) {
          if ((s & 1) == 0) o[j] = wv[j + s / 2];
          else o[j] = __builtin_amdgcn_alignbit(wv[j + s / 2 + 1], wv[j + s / 2], 16);
        }
        *(u32x4*)&Kc[s * KCPITCH + c * 8] = o;
      }
    }
#pragma unroll
    for (int i = 0; i < 4; ++i) {
      int row = t + 512 * i;
      _Float16 ha = (_Float16)lo_f(xv[i]);
      _Float16 hb = (_Float16)hi_f(xv[i]);
      xsA[256 + row] = __builtin_bit_cast(u16, ha);
      xsB[256 + row] = __builtin_bit_cast(u16, hb);
    }
    __syncthreads();
    STAMP(1);
    const int b = lane & 15, g = lane >> 4;
    const int s = (7 - b) & 7;
    f4 acc[8];
#pragma unroll
    for (int i = 0; i < 8; ++i) acc[i] = (f4){0.f, 0.f, 0.f, 0.f};
    const int abase = s * KCPITCH + (b < 8 ? 2024 : 2016) + 8 * g;
    const int bbase = 256 - 16 + 16 * b + 8 * g;
#pragma unroll
    for (int rr = 0; rr < 2; ++rr) {
      const int rp = w4 * 2 + rr;
      h8 A[8], B[8];
#pragma unroll
      for (int u = 0; u < 8; ++u) A[u] = *(const h8*)&Kc[abase - 32 * rp - 256 * u];
#pragma unroll
      for (int v = 0; v < 8; ++v) B[v] = *(const h8*)&xs[bbase - 32 * rp + 256 * v];
#pragma unroll
      for (int u = 0; u < 8; ++u)
#pragma unroll
        for (int v = 0; v + u < 8; ++v)
          acc[u + v] = __builtin_amdgcn_mfma_f32_16x16x32_f16(A[u], B[v], acc[u + v], 0, 0, 0);
    }
    __syncthreads();
    STAMP(2);
    float* red = (float*)Kc;
#pragma unroll
    for (int tt = 0; tt < 8; ++tt) *(f4*)&red[((w4 * 8 + tt) * 64 + lane) * 4] = acc[tt];
    __syncthreads();
    const float sc = kscale[d];
#pragma unroll
    for (int i = 0; i < 2; ++i) {
      const int tt = w4 * 2 + i;
      f4 s0 = *(const f4*)&red[((0 * 8 + tt) * 64 + lane) * 4];
      f4 s1 = *(const f4*)&red[((1 * 8 + tt) * 64 + lane) * 4];
      f4 s2 = *(const f4*)&red[((2 * 8 + tt) * 64 + lane) * 4];
      f4 s3 = *(const f4*)&red[((3 * 8 + tt) * 64 + lane) * 4];
      f4 sum = ((s0 + s1) + (s2 + s3)) * sc;
      u32x2 o = {pk_bf(sum[0], sum[1]), pk_bf(sum[2], sum[3])};
      *(u32x2*)(yT + (size_t)d * SEQ + 256 * tt + 16 * b + 4 * g) = o;
    }
    STAMP(8);
    return;
  }
  const int xcd = blockIdx.x & 7, idx = blockIdx.x >> 3;
  const int mi = idx & 3, ni = idx >> 2;
  const int bm = (xcd >> 1) * 4 + mi, bn = (xcd & 1) * 8 + ni;
  const int phase = ((5 * ni + 10 * mi) >> 1) % NKT;
  const int mt0 = bm * 8, nt0 = bn * 7;
  const u16* pa = xnP + (size_t)(mt0 + wave) * 20 * 512 + lane * 8;
  int ntl = nt0 + wave; if (ntl > 107) ntl = 107;
  const u16* pb = w1P + (size_t)ntl * 20 * 512 + lane * 8;
  auto stage = [&](int kt) {
    int ko = kt + phase;
    if (ko >= NKT) ko -= NKT;
    char* sb = shm + (kt % ND) * STG;
    GLDS(pa + ko * 512, sb + wave * 1024);
    if (wave < 7) GLDS(pb + ko * 512, sb + 8192 + wave * 1024);
  };
  const int wm = wave & 3, wn = wave >> 2;
  f4 acc[2][4];
#pragma unroll
  for (int i = 0; i < 2; ++i)
#pragma unroll
    for (int j = 0; j < 4; ++j) acc[i][j] = (f4){0.f, 0.f, 0.f, 0.f};
  const int g = lane >> 4;
  const int aoff = wm * 2048 + lane * 16;
  const int boff = 8192 + wn * 4096 + lane * 16;
  s8 fa[2][2], fb[2][4];
  auto rdfrag = [&](int kt) {
    const char* sb = shm + (kt % ND) * STG;
#pragma unroll
    for (int i = 0; i < 2; ++i) fa[kt & 1][i] = *(const s8*)(sb + aoff + i * 1024);
#pragma unroll
    for (int j = 0; j < 4; ++j)
      if (j < 3 || wn == 0) fb[kt & 1][j] = *(const s8*)(sb + boff + j * 1024);
  };
#pragma unroll
  for (int s = 0; s < ND; ++s) stage(s);
  STAMP(1);
  if (wave < 7) WAIT_V((ND - 1) * 2); else WAIT_V(ND - 1);
  RAW_BARRIER();
  STAMP(2);
  rdfrag(0);
  constexpr int LSLOT = (NKT - ND) % ND;
#pragma unroll
  for (int kt = 0; kt < NKT; ++kt) {
    if (kt + 1 < NKT) {
      constexpr int dummy = 0; (void)dummy;
      const int nfl = ((kt + ND - 1 < NKT - 1) ? (kt + ND - 1) : (NKT - 1)) - (kt + 1);
      const int aux = (kt > NKT - ND) ? 2 : 0;
      if (wave < 7) { switch (nfl * 2 + aux) { case 0: WAIT_V(0); break; case 2: WAIT_V(2); break; case 4: WAIT_V(4); break; case 6: WAIT_V(6); break; default: WAIT_V(8); break; } }
      else { switch (nfl + aux) { case 0: WAIT_V(0); break; case 1: WAIT_V(1); break; case 2: WAIT_V(2); break; case 3: WAIT_V(3); break; case 4: WAIT_V(4); break; default: WAIT_V(5); break; } }
    } else {
      WAIT_V(0);
    }
    RAW_BARRIER();
    if (kt + ND < NKT) stage(kt + ND);
    else if (kt + ND == NKT) {
      char* sb = shm + LSLOT * STG;
      GLDS(lut + wave * 512 + lane * 8, sb + wave * 1024);
      if (wave < 4) GLDS(lut + (wave + 8) * 512 + lane * 8, sb + (wave + 8) * 1024);
      else { int o = nt0 * 16 + (lane < 14 ? lane : 13) * 8; if (o > FF - 8) o = FF - 8; GLDS(b1 + o, sb + 12 * 1024); }
    }
    if (kt + 1 < NKT) rdfrag(kt + 1);
#pragma unroll
    for (int j = 0; j < 4; ++j) {
      if (j < 3 || wn == 0) {
#pragma unroll
        for (int i = 0; i < 2; ++i)
          acc[i][j] = __builtin_amdgcn_mfma_f32_16x16x32_bf16(fb[kt & 1][j], fa[kt & 1][i], acc[i][j], 0, 0, 0);
      }
    }
  }
  STAMP(7);
  const u16* slut = (const u16*)(shm + LSLOT * STG);
  const u16* sbias = (const u16*)(shm + LSLOT * STG + 12 * 1024);
#pragma unroll
  for (int j = 0; j < 4; ++j) {
    const int nt = nt0 + wn * 4 + j;
    if ((j < 3 || wn == 0) && nt < 108) {
      u32x2 bb = *(const u32x2*)(sbias + wn * 64 + j * 16 + 4 * g);
      const float bv[4] = {lo_f(bb[0]), hi_f(bb[0]), lo_f(bb[1]), hi_f(bb[1])};
      const int kb = nt >> 1, half = nt & 1;
#pragma unroll
      for (int i = 0; i < 2; ++i) {
        u32 t0 = pk_bf(acc[i][j][0], acc[i][j][1]);
        u32 t1 = pk_bf(acc[i][j][2], acc[i][j][3]);
        u32 h0 = pk_bf(lo_f(t0) + bv[0], hi_f(t0) + bv[1]);
        u32 h1 = pk_bf(lo_f(t1) + bv[2], hi_f(t1) + bv[3]);
        u32 hb[4] = {h0 & 0xFFFFu, h0 >> 16, h1 & 0xFFFFu, h1 >> 16};
        u32 res[4];
#pragma unroll
        for (int r = 0; r < 4; ++r) {
          u32 a = hb[r] & 0x7FFFu;
          u32 li = a - (LUT_E0 << 7);
          bool in = li < (u32)LUT_HALF;
          u32 v = slut[(in ? li : 0u) + (hb[r] >> 15) * LUT_HALF];
          float h = __uint_as_float(hb[r] << 16);
          float fbv = (a < (LUT_E0 << 7)) ? h * 0.59765625f : (h > 0.f ? h : h * 0.0f);
          u32 fbb = pk_bf(fbv, fbv) & 0xFFFFu;
          res[r] = in ? v : fbb;
        }
        u32x2 ov = {res[0] | (res[1] << 16), res[2] | (res[3] << 16)};
        const int mt = mt0 + wm * 2 + i;
        *(u32x2*)(actP + (((size_t)mt * 54 + kb) * 64 + lane) * 8 + half * 4) = ov;
      }
    }
  }
  STAMP(8);
}

__global__ __launch_bounds__(512) void k3_kernel(const u16* __restrict__ actP, const u16* __restrict__ w2P,
                                                 const u16* __restrict__ xin, const float2* __restrict__ stats,
                                                 const u16* __restrict__ yT, const u16* __restrict__ dskip,
                                                 u16* __restrict__ out) {
  constexpr int ND = 7;
  constexpr int STG = 18 * 1024;
  constexpr int AUX = ND * STG;
  constexpr int NKT = FF / 64;
  __shared__ __attribute__((aligned(1024))) char shm[ND * STG + 21 * 1024];
  const int t = threadIdx.x, lane = t & 63;
  const int wave = __builtin_amdgcn_readfirstlane(t >> 6);
  const int xcd = blockIdx.x & 7, idx = blockIdx.x >> 3;
  const int mi = idx & 7, ni = idx >> 3;
  const int bm = (xcd >> 1) * 8 + mi, bn = (xcd & 1) * 4 + ni;
  const int phase = ((27 * mi + 54 * ni) >> 3) % NKT;
  const int mt0 = bm * 4, m0 = bm * 64, n0 = bn * 80, nt0 = bn * 5;
  STAMPB(3, (char*)actP - (16u << 20));
  STAMP(0);
  const u16* pa = actP + ((size_t)(mt0 + (wave >> 1)) * 54 + (wave & 1)) * 512 + lane * 8;
  const u16* pb0 = w2P + ((size_t)(nt0 + (wave >> 1)) * 54 + (wave & 1)) * 512 + lane * 8;
  const u16* pb1 = w2P + ((size_t)(nt0 + 4) * 54 + (wave & 1)) * 512 + lane * 8;
  auto stage = [&](int kt) {
    int ko = kt + phase;
    if (ko >= NKT) ko -= NKT;
    char* sb = shm + (kt % ND) * STG;
    GLDS(pa + ko * 1024, sb + wave * 1024);
    GLDS(pb0 + ko * 1024, sb + 8192 + wave * 1024);
    if (wave < 2) GLDS(pb1 + ko * 1024, sb + 8192 + (wave + 8) * 1024);
  };
  const int wm = wave & 3, wn = wave >> 2;
  f4 acc[3];
#pragma unroll
  for (int j = 0; j < 3; ++j) acc[j] = (f4){0.f, 0.f, 0.f, 0.f};
  const int fr = lane & 15, g = lane >> 4, tq = (lane & 15) >> 2, tp = lane & 3;
  const int aoff = wm * 2048 + lane * 16;
  const int boff = 8192 + wn * 6144 + lane * 16;
  s8 fa[2][2], fb[2][3][2];
  auto rdfrag = [&](int kt) {
    const char* sb = shm + (kt % ND) * STG;
#pragma unroll
    for (int ks = 0; ks < 2; ++ks) {
      fa[kt & 1][ks] = *(const s8*)(sb + aoff + ks * 1024);
#pragma unroll
      for (int j = 0; j < 3; ++j)
        if (j < 2 || wn == 0) fb[kt & 1][j][ks] = *(const s8*)(sb + boff + j * 2048 + ks * 1024);
    }
  };
#pragma unroll
  for (int s = 0; s < ND; ++s) stage(s);
  STAMP(1);
  if (wave < 2) WAIT_V((ND - 1) * 3); else WAIT_V((ND - 1) * 2);
  RAW_BARRIER();
  STAMP(2);
  rdfrag(0);
#pragma unroll
  for (int kt = 0; kt < NKT; ++kt) {
    if (kt + 1 < NKT) {
      const int nfl = ((kt + ND - 1 < NKT - 1) ? (kt + ND - 1) : (NKT - 1)) - (kt + 1);
      const int aux = (kt > NKT - ND) ? 3 : 0;
      if (wave < 2) { switch (nfl * 3 + aux) { case 3: WAIT_V(3); break; case 6: WAIT_V(6); break; case 9: WAIT_V(9); break; case 12: WAIT_V(12); break; case 15: WAIT_V(15); break; default: WAIT_V(18); break; } }
      else { switch (nfl * 2 + aux) { case 3: WAIT_V(3); break; case 5: WAIT_V(5); break; case 7: WAIT_V(7); break; case 9: WAIT_V(9); break; case 10: WAIT_V(10); break; case 11: WAIT_V(11); break; default: WAIT_V(13); break; } }
    } else {
      WAIT_V(0);
    }
    RAW_BARRIER();
    if (kt == 4) STAMP(3);
    if (kt == 8) STAMP(4);
    if (kt == 16) STAMP(5);
    if (kt == 26) STAMP(6);
    if (kt + ND < NKT) stage(kt + ND);
    else if (kt + ND == NKT) {
#pragma unroll
      for (int i = 0; i < 3; ++i) {
        int p = wave + 8 * i;
        if (p > 20) p = 20;
        int c = (p < 10 ? p : p - 10) * 64 + lane;
        const u16* src;
        if (p < 10) src = xin + (size_t)(m0 + c / 10) * DM + n0 + (c % 10) * 8;
        else if (p < 20) src = yT + (size_t)(n0 + (c >> 3)) * SEQ + m0 + (c & 7) * 8;
        else src = (lane < 32) ? (const u16*)(stats + m0) + lane * 8 : dskip + n0 + (lane < 42 ? lane - 32 : 9) * 8;
        GLDS(src, shm + AUX + p * 1024);
      }
    }
    if (kt + 1 < NKT) rdfrag(kt + 1);
#pragma unroll
    for (int ks = 0; ks < 2; ++ks)
#pragma unroll
      for (int j = 0; j < 3; ++j)
        if (j < 2 || wn == 0)
          acc[j] = __builtin_amdgcn_mfma_f32_16x16x32_bf16(fb[kt & 1][j][ks], fa[kt & 1][ks], acc[j], 0, 0, 0);
  }
  STAMP(7);
  const char* sx = shm + AUX;
  const char* sy = shm + AUX + 10 * 1024;
  const char* sm = shm + AUX + 20 * 1024;
  const int ml = wm * 16 + fr;
  const float2 st = *(const float2*)(sm + ml * 8);
#pragma unroll
  for (int j = 0; j < 3; ++j) {
    if (j < 2 || wn == 0) {
      const int nl = wn * 48 + j * 16;
      u32x2 xx = *(const u32x2*)(sx + ml * 160 + (nl + 4 * g) * 2);
      u32x2 dd = *(const u32x2*)(sm + 512 + (nl + 4 * g) * 2);
      s4 yy = TRREAD(sy + (nl + 4 * g + tq) * 128 + (wm * 16 + 4 * tp) * 2);
      const float xv[4] = {lo_f(xx[0]), hi_f(xx[0]), lo_f(xx[1]), hi_f(xx[1])};
      const float dv[4] = {lo_f(dd[0]), hi_f(dd[0]), lo_f(dd[1]), hi_f(dd[1])};
      float o[4];
#pragma unroll
      for (int r = 0; r < 4; ++r) {
        float xn = bfr(bfr(xv[r] - st.x) * st.y);
        float y = bf2f((u16)yy[r]);
        float a = bfr(y + bfr(xn * dv[r]));
        float s1 = bfr(xv[r] + a);
        o[r] = s1 + bfr(acc[j][r]);
      }
      u32x2 ov = {pk_bf(o[0], o[1]), pk_bf(o[2], o[3])};
      *(u32x2*)(out + (size_t)(m0 + ml) * DM + n0 + nl + 4 * g) = ov;
    }
  }
  STAMP(8);
}

extern "C" void kernel_launch(void* const* d_in, const int* in_sizes, int n_in,
                              void* d_out, int out_size, void* d_ws, size_t ws_size,
                              hipStream_t stream) {
  const u16* u = (const u16*)d_in[0];
  const u16* lar = (const u16*)d_in[1];
  const u16* aim = (const u16*)d_in[2];
  const u16* cre = (const u16*)d_in[3];
  const u16* cim = (const u16*)d_in[4];
  const u16* logdt = (const u16*)d_in[5];
  const u16* dskip = (const u16*)d_in[6];
  const u16* w1 = (const u16*)d_in[8];
  const u16* b1 = (const u16*)d_in[9];
  const u16* w2 = (const u16*)d_in[10];
  u16* out = (u16*)d_out;
  char* ws = (char*)d_ws;
  if (ws_size < (32u << 20)) return;
  u16* xnP = (u16*)(ws);
  u16* w1P = (u16*)(ws + (4u << 20));
  u16* Kr = (u16*)(ws + (8u << 20));
  float* ksc = (float*)(ws + (12u << 20));
  float2* stats = (float2*)(ws + (12u << 20) + 4096);
  u16* lut = (u16*)(ws + (12u << 20) + 32768);
  u16* yT = (u16*)(ws + (13u << 20));
  u16* actP = (u16*)(ws + (16u << 20));
  u16* w2P = (u16*)(ws + (24u << 20));

  k1_kernel<<<K1_LN + K1_KG + K1_LUT + K1_RP, 256, 0, stream>>>(u, xnP, stats, lut, lar, aim, cre, cim, logdt, Kr, ksc, w1, w1P);
  k2_kernel<<<K2_G1 + K2_CONV + K2_RP, 512, 0, stream>>>(xnP, w1P, b1, lut, actP, Kr, ksc, yT, w2, w2P);
  k3_kernel<<<256, 512, 0, stream>>>(actP, w2P, u, stats, yT, dskip, out);
}
```

```cpp
#include <hip/hip_runtime.h>
#include <hip/hip_bf16.h>

#define SEQ 2048
#define DM 640
#define NS 128
#define FF 1728

typedef unsigned short u16;
typedef short s4 __attribute__((ext_vector_type(4)));
typedef short s8 __attribute__((ext_vector_type(8)));
typedef _Float16 h8 __attribute__((ext_vector_type(8)));
typedef float f4 __attribute__((ext_vector_type(4)));
typedef unsigned int u32;
typedef unsigned int u32x4 __attribute__((ext_vector_type(4)));
typedef unsigned int u32x2 __attribute__((ext_vector_type(2)));

__device__ __forceinline__ float bf2f(u16 v) { return __uint_as_float(((u32)v) << 16); }
__device__ __forceinline__ u16 f2bf(float f) {
  u32 u = __float_as_uint(f);
  u32 r = (u + 0x7FFFu + ((u >> 16) & 1u)) >> 16;
  return (u16)r;
}
__device__ __forceinline__ float bfr(float f) { return bf2f(f2bf(f)); }

#define STAMPB(kid, base)
#define STAMP(k)
typedef __bf16 bf2_t __attribute__((ext_vector_type(2)));
typedef float f2_t __attribute__((ext_vector_type(2)));
__device__ __forceinline__ u32 pk_bf(float a, float b) {
  f2_t v = {a, b};
  bf2_t r = __builtin_convertvector(v, bf2_t);
  return __builtin_bit_cast(u32, r);
}
__device__ __forceinline__ float lo_f(u32 w) { return __uint_as_float(w << 16); }
__device__ __forceinline__ float hi_f(u32 w) { return __uint_as_float(w & 0xFFFF0000u); }

#define WAIT_V(n) asm volatile("s_waitcnt vmcnt(%0)" ::"n"(n) : "memory")
#define RAW_BARRIER() do { asm volatile("s_waitcnt lgkmcnt(0)" ::: "memory"); __builtin_amdgcn_s_barrier(); } while (0)
__device__ __forceinline__ void glds16(const void* gsrc, const void* lp) {
  unsigned keep;
  unsigned lds_dst = (unsigned)(size_t)(const __attribute__((address_space(3))) char*)lp;
  asm volatile("s_mov_b32 %0, m0\n\ts_mov_b32 m0, %2\n\ts_nop 0\n\tglobal_load_lds_dwordx4 %1, off\n\ts_mov_b32 m0, %0"
               : "=&s"(keep) : "v"(gsrc), "s"(lds_dst) : "memory");
}
#define GLDS(gp, lp) glds16((const void*)(gp), (const void*)(lp))

#define LUT_E0 111
#define LUT_E1 134
#define LUT_HALF ((LUT_E1 - LUT_E0) * 128)
#define LUT_N (2 * LUT_HALF)

__device__ __forceinline__ float mish_bf(float h) {
  float e = bfr(expf(-fabsf(h)));
  float l = bfr(log1pf(e));
  float sp = bfr(l + (h > 0.f ? h : 0.f));
  float th = bfr(tanhf(sp));
  return bfr(h * th);
}

#define KRLEN 2080
#define WFENCE() do { asm volatile("" ::: "memory"); __builtin_amdgcn_wave_barrier(); asm volatile("" ::: "memory"); } while (0)
__device__ __forceinline__ void cexp_p(float arl2, float w, float p, float& re, float& im) {
  float mag = __builtin_amdgcn_exp2f(arl2 * p);
  float rev = w * p;
  rev = rev - floorf(rev);
  re = mag * __builtin_amdgcn_cosf(rev);
  im = mag * __builtin_amdgcn_sinf(rev);
}
__device__ __forceinline__ void cmul(float ar, float ai, float br, float bi, float& cr, float& ci) {
  float r = fmaf(ar, br, -(ai * bi));
  float i = fmaf(ar, bi, ai * br);
  cr = r; ci = i;
}
__device__ __forceinline__ u32x2 split2(float a, float b) {
  u32 h = pk_bf(a, b);
  u32 l = pk_bf(a - lo_f(h), b - hi_f(h));
  return (u32x2){h, l};
}

#define K1_LN 256
#define K1_KG 320
#define K1_LUT 24
#define K1_RP 135
#define WPITCH 544
#define TRREAD(p) __builtin_amdgcn_ds_read_tr16_b64_v4i16((__attribute__((address_space(3))) s4*)(p))

__device__ __forceinline__ void repack_load(const u16* __restrict__ w, int N, int ku, int nu, char* lds, int tl) {
#pragma unroll
  for (int i = 0; i < 2; ++i) {
    int idx = tl + 256 * i, r = idx >> 3, c = idx & 7;
    u32x4 v = *(const u32x4*)(w + (size_t)(ku * 64 + r) * N + nu * 64 + c * 8);
    *(u32x4*)(lds + r * 160 + c * 16) = v;
  }
}
__device__ __forceinline__ void repack_store(u16* __restrict__ wP, int KB, int ku, int nu, const char* lds, int tl) {
  const int wl = tl >> 6, lane = tl & 63, g = lane >> 4, tq = (lane & 15) >> 2, tp = lane & 3;
#pragma unroll
  for (int kb2 = 0; kb2 < 2; ++kb2) {
    const char* p0 = lds + (kb2 * 32 + 4 * g + tq) * 160 + wl * 32 + tp * 8;
    s4 b0 = TRREAD(p0);
    s4 b1 = TRREAD(p0 + 16 * 160);
    s8 bf = {b0[0], b0[1], b0[2], b0[3], b1[0], b1[1], b1[2], b1[3]};
    *(s8*)(wP + (((size_t)(nu * 4 + wl) * KB + ku * 2 + kb2) * 64 + lane) * 8) = bf;
  }
}

__global__ __launch_bounds__(256) void k1_kernel(const u16* __restrict__ u, u16* __restrict__ xnP,
                                                 float2* __restrict__ stats,
                                                 u16* __restrict__ lut,
                                                 const u16* __restrict__ lar, const u16* __restrict__ aim,
                                                 const u16* __restrict__ cre, const u16* __restrict__ cim,
                                                 const u16* __restrict__ logdt, u16* __restrict__ Kr,
                                                 float* __restrict__ kscale,
                                                 const u16* __restrict__ w1, u16* __restrict__ w1P) {
  __shared__ __attribute__((aligned(16))) char k1s[4 * 16 * WPITCH];
  const int t = threadIdx.x, lane = t & 63;
  const int wave = __builtin_amdgcn_readfirstlane(t >> 6);
  STAMPB(0, xnP);
  STAMP(0);
  if (blockIdx.x < K1_LN) {
    u16 (*tile)[DM + 8] = (u16 (*)[DM + 8])k1s;
    const int r = wave * 2 + (lane >> 5), q = lane & 31;
    const int row = blockIdx.x * 8 + r;
    float v[20];
    const u32x2* src = (const u32x2*)(u + (size_t)row * DM);
#pragma unroll
    for (int i = 0; i < 5; ++i) {
      u32x2 w = src[q + 32 * i];
      v[i * 4 + 0] = lo_f(w[0]); v[i * 4 + 1] = hi_f(w[0]);
      v[i * 4 + 2] = lo_f(w[1]); v[i * 4 + 3] = hi_f(w[1]);
    }
    float s = 0.f;
#pragma unroll
    for (int i = 0; i < 20; ++i) s += v[i];
    STAMP(1);
#pragma unroll
    for (int o = 1; o < 32; o <<= 1) s += __shfl_xor(s, o);
    const float mean = s * (1.0f / DM);
    float ss = 0.f;
#pragma unroll
    for (int i = 0; i < 20; ++i) { float d = v[i] - mean; ss += d * d; }
#pragma unroll
    for (int o = 1; o < 32; o <<= 1) ss += __shfl_xor(ss, o);
    const float var = ss * (1.0f / DM);
    const float mb = bfr(mean);
    const float vb = bfr(var);
    const float den = bfr(sqrtf(bfr(vb + 1e-5f)));
    const float rden = 1.0f / den;
    if (q == 0) stats[row] = make_float2(mb, rden);
#pragma unroll
    for (int i = 0; i < 5; ++i) {
      u32 d0 = pk_bf(v[i * 4 + 0] - mb, v[i * 4 + 1] - mb);
      u32 d1 = pk_bf(v[i * 4 + 2] - mb, v[i * 4 + 3] - mb);
      u32x2 w = {pk_bf(lo_f(d0) * rden, hi_f(d0) * rden), pk_bf(lo_f(d1) * rden, hi_f(d1) * rden)};
      *(u32x2*)&tile[r][(q + 32 * i) * 4] = w;
    }
    __syncthreads();
    STAMP(2);
    const int mt = blockIdx.x >> 1, half = blockIdx.x & 1;
#pragma unroll
    for (int i = 0; i < 3; ++i) {
      int idx = t + 256 * i;
      if (idx < 640) {
        int kb = idx >> 5, g = (idx >> 3) & 3, fr = idx & 7;
        u32x2 a = *(const u32x2*)&tile[fr][kb * 32 + 4 * g];
        u32x2 b = *(const u32x2*)&tile[fr][kb * 32 + 16 + 4 * g];
        u32x4 w = {a[0], a[1], b[0], b[1]};
        *(u32x4*)(xnP + (((size_t)mt * 20 + kb) * 64 + g * 16 + half * 8 + fr) * 8) = w;
      }
    }
    STAMP(3);
    return;
  }
  if (blockIdx.x >= K1_LN + K1_KG + K1_LUT) {
    const int u0 = (blockIdx.x - (K1_LN + K1_KG + K1_LUT)) * 2;
    repack_load(w1, FF, u0 / 27, u0 % 27, k1s, t);
    repack_load(w1, FF, (u0 + 1) / 27, (u0 + 1) % 27, k1s + 10240, t);
    __syncthreads();
    repack_store(w1P, DM / 32, u0 / 27, u0 % 27, k1s, t);
    repack_store(w1P, DM / 32, (u0 + 1) / 27, (u0 + 1) % 27, k1s + 10240, t);
    STAMP(3);
    return;
  }
  if (blockIdx.x >= K1_LN + K1_KG) {
    int idx = (blockIdx.x - (K1_LN + K1_KG)) * 256 + t;
    if (idx < LUT_N) {
      int sgn = idx >= LUT_HALF;
      int a = idx - sgn * LUT_HALF + (LUT_E0 << 7);
      float h = bf2f((u16)(a | (sgn << 15)));
      lut[idx] = f2bf(mish_bf(h));
    } else if (idx < 6144) {
      lut[idx] = 0;
    }
    return;
  }
  const int ch = (blockIdx.x - K1_LN) * 2 + (wave >> 1);
  const int n = (wave & 1) * 64 + lane;
  char* W = k1s + wave * 16 * WPITCH;
  const float dt = bfr(expf(bf2f(logdt[ch])));
  const float ar = -bfr(expf(bf2f(lar[ch * NS + n]))) * dt;
  const float ai = bf2f(aim[ch * NS + n]) * dt;
  const float c0r = bf2f(cre[ch * NS + lane]) * dt, c0i = bf2f(cim[ch * NS + lane]) * dt;
  const float c1r = bf2f(cre[ch * NS + 64 + lane]) * dt, c1i = bf2f(cim[ch * NS + 64 + lane]) * dt;
  const float cr = (wave & 1) ? c1r : c0r, ci = (wave & 1) ? c1i : c0i;
  float bs = (fabsf(c0r) + fabsf(c0i)) + (fabsf(c1r) + fabsf(c1i));
#pragma unroll
  for (int o = 1; o < 64; o <<= 1) bs += __shfl_xor(bs, o);
  STAMP(1);
  const float arl2 = ar * 1.4426950408889634f;
  const float w = ai * 0.15915494309189535f;
  float e1r, e1i, e8r, e8i, eHr, eHi;
  cexp_p(arl2, w, 1.0f, e1r, e1i);
  cexp_p(arl2, w, 8.0f, e8r, e8i);
  cexp_p(arl2, w, 128.0f, eHr, eHi);
  const int fi = lane & 15, g = lane >> 4;
  char* wr = W + lane * 8;
  const char* rd = W + fi * WPITCH + g * 16;
  {
    float lr = 1.f, li = 0.f;
    *(u32x2*)(wr) = split2(1.f, 0.f);
#pragma unroll
    for (int j = 1; j < 16; ++j) {
      cmul(lr, li, e8r, e8i, lr, li);
      *(u32x2*)(wr + j * WPITCH) = split2(lr, -li);
    }
  }
  WFENCE();
  u32x4 Bf[8], Bs[8];
#pragma unroll
  for (int kb = 0; kb < 8; ++kb) {
    Bf[kb] = *(const u32x4*)(rd + kb * 64);
    Bs[kb] = (u32x4){Bf[kb][1], Bf[kb][0], Bf[kb][3], Bf[kb][2]};
  }
  WFENCE();
  STAMP(2);
  float Rr[8], Ri[8];
  Rr[0] = 1.f; Ri[0] = 0.f; Rr[1] = e1r; Ri[1] = e1i;
#pragma unroll
  for (int r = 2; r < 8; ++r) cmul(Rr[r - 1], Ri[r - 1], e1r, e1i, Rr[r], Ri[r]);
  f4 acc[3];
  float gr = cr, gi = ci;
  {
#pragma unroll
    for (int ii = 0; ii < 2; ++ii) {
      *(u32x2*)(wr + (ii * 8) * WPITCH) = split2(gr, gi);
#pragma unroll
      for (int r = 1; r < 8; ++r) {
        float hr, hi;
        cmul(gr, gi, Rr[r], Ri[r], hr, hi);
        *(u32x2*)(wr + (ii * 8 + r) * WPITCH) = split2(hr, hi);
      }
      cmul(gr, gi, eHr, eHi, gr, gi);
    }
    WFENCE();
    acc[0] = (f4){0.f, 0.f, 0.f, 0.f};
#pragma unroll
    for (int kb = 0; kb < 8; ++kb) {
      u32x4 a = *(const u32x4*)(rd + kb * 64);
      acc[0] = __builtin_amdgcn_mfma_f32_16x16x32_bf16(__builtin_bit_cast(s8, a), __builtin_bit_cast(s8, Bf[kb]), acc[0], 0, 0, 0);
      acc[0] = __builtin_amdgcn_mfma_f32_16x16x32_bf16(__builtin_bit_cast(s8, a), __builtin_bit_cast(s8, Bs[kb]), acc[0], 0, 0, 0);
    }
  }
  WFENCE();
  STAMP(3);
  {
#pragma unroll
    for (int ii = 2; ii < 4; ++ii) {
      *(u32x2*)(wr + ((ii - 2) * 4) * WPITCH) = split2(gr, gi);
#pragma unroll
      for (int r = 1; r < 4; ++r) {
        float hr, hi;
        cmul(gr, gi, Rr[2 * r], Ri[2 * r], hr, hi);
        *(u32x2*)(wr + ((ii - 2) * 4 + r) * WPITCH) = split2(hr, hi);
      }
      cmul(gr, gi, eHr, eHi, gr, gi);
    }
#pragma unroll
    for (int ii = 4; ii < 8; ++ii) {
      *(u32x2*)(wr + (8 + (ii - 4) * 2) * WPITCH) = split2(gr, gi);
      float hr, hi;
      cmul(gr, gi, Rr[4], Ri[4], hr, hi);
      *(u32x2*)(wr + (8 + (ii - 4) * 2 + 1) * WPITCH) = split2(hr, hi);
      cmul(gr, gi, eHr, eHi, gr, gi);
    }
    WFENCE();
    acc[1] = (f4){0.f, 0.f, 0.f, 0.f};
#pragma unroll
    for (int kb = 0; kb < 8; ++kb) {
      u32x4 a = *(const u32x4*)(rd + kb * 64);
      acc[1] = __builtin_amdgcn_mfma_f32_16x16x32_bf16(__builtin_bit_cast(s8, a), __builtin_bit_cast(s8, Bf[kb]), acc[1], 0, 0, 0);
      acc[1] = __builtin_amdgcn_mfma_f32_16x16x32_bf16(__builtin_bit_cast(s8, a), __builtin_bit_cast(s8, Bs[kb]), acc[1], 0, 0, 0);
    }
  }
  WFENCE();
  STAMP(4);
  {
#pragma unroll
    for (int ii = 8; ii < 17; ++ii) {
      *(u32x2*)(wr + (ii - 8) * WPITCH) = split2(gr, gi);
      if (ii < 16) cmul(gr, gi, eHr, eHi, gr, gi);
    }
    WFENCE();
    acc[2] = (f4){0.f, 0.f, 0.f, 0.f};
#pragma unroll
    for (int kb = 0; kb < 8; ++kb) {
      u32x4 a = *(const u32x4*)(rd + kb * 64);
      acc[2] = __builtin_amdgcn_mfma_f32_16x16x32_bf16(__builtin_bit_cast(s8, a), __builtin_bit_cast(s8, Bf[kb]), acc[2], 0, 0, 0);
      acc[2] = __builtin_amdgcn_mfma_f32_16x16x32_bf16(__builtin_bit_cast(s8, a), __builtin_bit_cast(s8, Bs[kb]), acc[2], 0, 0, 0);
    }
  }
  WFENCE();
  STAMP(5);
  {
    float* P = (float*)W;
    const int i0 = (g >> 1) * 128 + (g & 1) * 4 + 8 * fi;
    const int i2 = 512 + 64 * g + fi;
#pragma unroll
    for (int r = 0; r < 4; ++r) {
      P[i0 + r] = acc[0][r];
      int i1 = g < 2 ? 256 + 64 * g + r + 4 * fi : 384 + 32 * (2 * (g - 2) + (r >> 1)) + (r & 1) + 2 * fi;
      P[i1] = acc[1][r];
      P[i2 + 16 * r] = acc[2][r];
    }
  }
  __syncthreads();
  STAMP(6);
  const float bound = 2.0f * bs;
  int ex = 0;
  if (bound > 0.f && bound < 3.0e38f) { (void)frexpf(bound, &ex); }
  int e = 15 - ex;
  e = e > 100 ? 100 : (e < -100 ? -100 : e);
  const float sc2 = ldexpf(2.0f, e);
  if ((t & 127) == 0) kscale[ch] = ldexpf(1.0f, -e);
  const float* P0 = (const float*)(k1s + (wave & 2) * 16 * WPITCH);
  const float* P1 = (const float*)(k1s + ((wave & 2) + 1) * 16 * WPITCH);
  const int tl = t & 127;
#pragma unroll
  for (int i = 0; i < 2; ++i) {
    const int c = tl + 128 * i;
    const int m = 255 - c;
    const int sg = m >= 128 ? 3 : (m >= 64 ? 2 : (m >= 32 ? 1 : 0));
    const int start = sg == 0 ? 0 : (128 << sg);
    const int base = sg == 0 ? 0 : 128 + 128 * sg;
    const int one = sg ? 1 : 0;
    const int hm = ((1 << sg) >> 1) - one;
    u32x4 o;
#pragma unroll
    for (int j = 0; j < 4; ++j) {
      u32 pk = 0;
#pragma unroll
      for (int h = 0; h < 2; ++h) {
        int q = 8 * m + 7 - (2 * j + h) - start;
        int idx = base + ((q + hm + ((q >> sg) & one)) >> sg);
        float val = (P0[idx] + P1[idx]) * sc2;
        _Float16 hv = (_Float16)val;
        pk |= ((u32)__builtin_bit_cast(u16, hv)) << (16 * h);
      }
      o[j] = pk;
    }
    *(u32x4*)(Kr + (size_t)ch * KRLEN + c * 8) = o;
  }
  if (tl < 4) *(u32x4*)(Kr + (size_t)ch * KRLEN + (256 + tl) * 8) = (u32x4){0u, 0u, 0u, 0u};
  STAMP(7);
}
#define K2_G1 256
#define K2_CONV 320
#define K2_RP 135
#define KCPITCH 2080
#define CONV_BYTES (8 * KCPITCH * 2 + (256 + SEQ) * 2)
__global__ __launch_bounds__(512, 4) void k2_kernel(const u16* __restrict__ xnP, const u16* __restrict__ w1P,
                                                    const u16* __restrict__ b1, const u16* __restrict__ lut,
                                                    u16* __restrict__ actP, const u16* __restrict__ Kr,
                                                    const float* __restrict__ kscale, u16* __restrict__ yT,
                                                    const u16* __restrict__ w2, u16* __restrict__ w2P) {
  constexpr int ND = 5;
  constexpr int STG = 15 * 1024;
  constexpr int NKT = DM / 32;
  __shared__ __attribute__((aligned(1024))) char shm[ND * STG];
  const int t = threadIdx.x, lane = t & 63;
  const int wave = __builtin_amdgcn_readfirstlane(t >> 6);
  STAMPB(2, (char*)actP - (16u << 20));
  STAMP(0);
  if (blockIdx.x >= K2_G1 + K2_CONV) {
    const int un = (blockIdx.x - (K2_G1 + K2_CONV)) * 2 + (wave >> 2);
    const int ku = un / 10, nu = un % 10;
    char* lds = shm + (wave >> 2) * 10240;
    repack_load(w2, DM, ku, nu, lds, t & 255);
    __syncthreads();
    repack_store(w2P, FF / 32, ku, nu, lds, t & 255);
    STAMP(8);
    return;
  }
  if (blockIdx.x >= K2_G1) {
    const int cb = blockIdx.x - K2_G1;
    const int pair = (cb & 7) * 40 + (cb >> 3);
    const int hsel = wave >> 2, w4 = wave & 3, tl = t & 255;
    const int grp = pair >> 2, sub = pair & 3;
    const int kb = grp >> 2, gg = grp & 3;
    const int d0 = kb * 32 + (sub >> 1) * 16 + gg * 4 + (sub & 1) * 2;
    const int d = d0 + hsel;
    u16* Kc = (u16*)(shm + hsel * CONV_BYTES);
    u16* xs = Kc + 8 * KCPITCH;
    u16* xsA = (u16*)(shm) + 8 * KCPITCH;
    u16* xsB = (u16*)(shm + CONV_BYTES) + 8 * KCPITCH;
    u32 xv[4];
#pragma unroll
    for (int i = 0; i < 4; ++i) {
      int row = t + 512 * i;
      xv[i] = *(const u32*)(xnP + (((size_t)(row >> 4) * 20 + kb) * 64 + gg * 16 + (row & 15)) * 8 + sub * 2);
    }
    if (tl < 32) *(u32x4*)&xs[tl * 8] = (u32x4){0u, 0u, 0u, 0u};
    for (int c = tl; c < 258; c += 256) {
      u32x4 lo = *(const u32x4*)(Kr + (size_t)d * KRLEN + c * 8);
      u32x4 hi = *(const u32x4*)(Kr + (size_t)d * KRLEN + c * 8 + 8);
      u32 wv[8] = {lo[0], lo[1], lo[2], lo[3], hi[0], hi[1], hi[2], hi[3]};
#pragma unroll
      for (int s = 0; s < 8; ++s) {
        u32x4 o;
#pragma unroll
        for (int j = 0; j < 4; ++j) {
          if ((s & 1) == 0) o[j] = wv[j + s / 2];
          else o[j] = __builtin_amdgcn_alignbit(wv[j + s / 2 + 1], wv[j + s / 2], 16);
        }
        *(u32x4*)&Kc[s * KCPITCH + c * 8] = o;
      }
    }
#pragma unroll
    for (int i = 0; i < 4; ++i) {
      int row = t + 512 * i;
      _Float16 ha = (_Float16)lo_f(xv[i]);
      _Float16 hb = (_Float16)hi_f(xv[i]);
      xsA[256 + row] = __builtin_bit_cast(u16, ha);
      xsB[256 + row] = __builtin_bit_cast(u16, hb);
    }
    __syncthreads();
    STAMP(1);
    const int b = lane & 15, g = lane >> 4;
    const int s = (7 - b) & 7;
    f4 acc[8];
#pragma unroll
    for (int i = 0; i < 8; ++i) acc[i] = (f4){0.f, 0.f, 0.f, 0.f};
    const int abase = s * KCPITCH + (b < 8 ? 2024 : 2016) + 8 * g;
    const int bbase = 256 - 16 + 16 * b + 8 * g;
#pragma unroll
    for (int rr = 0; rr < 2; ++rr) {
      const int rp = w4 * 2 + rr;
      h8 A[8], B[8];
#pragma unroll
      for (int u = 0; u < 8; ++u) A[u] = *(const h8*)&Kc[abase - 32 * rp - 256 * u];
#pragma unroll
      for (int v = 0; v < 8; ++v) B[v] = *(const h8*)&xs[bbase - 32 * rp + 256 * v];
#pragma unroll
      for (int u = 0; u < 8; ++u)
#pragma unroll
        for (int v = 0; v + u < 8; ++v)
          acc[u + v] = __builtin_amdgcn_mfma_f32_16x16x32_f16(A[u], B[v], acc[u + v], 0, 0, 0);
    }
    __syncthreads();
    STAMP(2);
    float* red = (float*)Kc;
#pragma unroll
    for (int tt = 0; tt < 8; ++tt) *(f4*)&red[((w4 * 8 + tt) * 64 + lane) * 4] = acc[tt];
    __syncthreads();
    const float sc = kscale[d];
#pragma unroll
    for (int i = 0; i < 2; ++i) {
      const int tt = w4 * 2 + i;
      f4 s0 = *(const f4*)&red[((0 * 8 + tt) * 64 + lane) * 4];
      f4 s1 = *(const f4*)&red[((1 * 8 + tt) * 64 + lane) * 4];
      f4 s2 = *(const f4*)&red[((2 * 8 + tt) * 64 + lane) * 4];
      f4 s3 = *(const f4*)&red[((3 * 8 + tt) * 64 + lane) * 4];
      f4 sum = ((s0 + s1) + (s2 + s3)) * sc;
      u32x2 o = {pk_bf(sum[0], sum[1]), pk_bf(sum[2], sum[3])};
      *(u32x2*)(yT + (size_t)d * SEQ + 256 * tt + 16 * b + 4 * g) = o;
    }
    STAMP(8);
    return;
  }
  const int xcd = blockIdx.x & 7, idx = blockIdx.x >> 3;
  const int mi = idx & 3, ni = idx >> 2;
  const int bm = (xcd >> 1) * 4 + mi, bn = (xcd & 1) * 8 + ni;
  const int phase = ((5 * ni + 10 * mi) >> 1) % NKT;
  const int mt0 = bm * 8, nt0 = bn * 7;
  const u16* pa = xnP + (size_t)(mt0 + wave) * 20 * 512 + lane * 8;
  int ntl = nt0 + wave; if (ntl > 107) ntl = 107;
  const u16* pb = w1P + (size_t)ntl * 20 * 512 + lane * 8;
  auto stage = [&](int kt) {
    int ko = kt + phase;
    if (ko >= NKT) ko -= NKT;
    char* sb = shm + (kt % ND) * STG;
    GLDS(pa + ko * 512, sb + wave * 1024);
    if (wave < 7) GLDS(pb + ko * 512, sb + 8192 + wave * 1024);
  };
  const int wm = wave & 3, wn = wave >> 2;
  f4 acc[2][4];
#pragma unroll
  for (int i = 0; i < 2; ++i)
#pragma unroll
    for (int j = 0; j < 4; ++j) acc[i][j] = (f4){0.f, 0.f, 0.f, 0.f};
  const int g = lane >> 4;
  const int aoff = wm * 2048 + lane * 16;
  const int boff = 8192 + wn * 4096 + lane * 16;
  s8 fa[2][2], fb[2][4];
  auto rdfrag = [&](int kt) {
    const char* sb = shm + (kt % ND) * STG;
#pragma unroll
    for (int i = 0; i < 2; ++i) fa[kt & 1][i] = *(const s8*)(sb + aoff + i * 1024);
#pragma unroll
    for (int j = 0; j < 4; ++j)
      if (j < 3 || wn == 0) fb[kt & 1][j] = *(const s8*)(sb + boff + j * 1024);
  };
#pragma unroll
  for (int s = 0; s < ND; ++s) stage(s);
  STAMP(1);
  if (wave < 7) WAIT_V((ND - 1) * 2); else WAIT_V(ND - 1);
  RAW_BARRIER();
  STAMP(2);
  rdfrag(0);
  constexpr int LSLOT = (NKT - ND) % ND;
#pragma unroll
  for (int kt = 0; kt < NKT; ++kt) {
    if (kt + 1 < NKT) {
      constexpr int dummy = 0; (void)dummy;
      const int nfl = ((kt + ND - 1 < NKT - 1) ? (kt + ND - 1) : (NKT - 1)) - (kt + 1);
      const int aux = (kt > NKT - ND) ? 2 : 0;
      if (wave < 7) { switch (nfl * 2 + aux) { case 0: WAIT_V(0); break; case 2: WAIT_V(2); break; case 4: WAIT_V(4); break; case 6: WAIT_V(6); break; default: WAIT_V(8); break; } }
      else { switch (nfl + aux) { case 0: WAIT_V(0); break; case 1: WAIT_V(1); break; case 2: WAIT_V(2); break; case 3: WAIT_V(3); break; case 4: WAIT_V(4); break; default: WAIT_V(5); break; } }
    } else {
      WAIT_V(0);
    }
    RAW_BARRIER();
    if (kt + ND < NKT) stage(kt + ND);
    else if (kt + ND == NKT) {
      char* sb = shm + LSLOT * STG;
      GLDS(lut + wave * 512 + lane * 8, sb + wave * 1024);
      if (wave < 4) GLDS(lut + (wave + 8) * 512 + lane * 8, sb + (wave + 8) * 1024);
      else { int o = nt0 * 16 + (lane < 14 ? lane : 13) * 8; if (o > FF - 8) o = FF - 8; GLDS(b1 + o, sb + 12 * 1024); }
    }
    if (kt + 1 < NKT) rdfrag(kt + 1);
#pragma unroll
    for (int j = 0; j < 4; ++j) {
      if (j < 3 || wn == 0) {
#pragma unroll
        for (int i = 0; i < 2; ++i)
          acc[i][j] = __builtin_amdgcn_mfma_f32_16x16x32_bf16(fb[kt & 1][j], fa[kt & 1][i], acc[i][j], 0, 0, 0);
      }
    }
  }
  STAMP(7);
  const u16* slut = (const u16*)(shm + LSLOT * STG);
  const u16* sbias = (const u16*)(shm + LSLOT * STG + 12 * 1024);
#pragma unroll
  for (int j = 0; j < 4; ++j) {
    const int nt = nt0 + wn * 4 + j;
    if ((j < 3 || wn == 0) && nt < 108) {
      u32x2 bb = *(const u32x2*)(sbias + wn * 64 + j * 16 + 4 * g);
      const float bv[4] = {lo_f(bb[0]), hi_f(bb[0]), lo_f(bb[1]), hi_f(bb[1])};
      const int kb = nt >> 1, half = nt & 1;
#pragma unroll
      for (int i = 0; i < 2; ++i) {
        u32 t0 = pk_bf(acc[i][j][0], acc[i][j][1]);
        u32 t1 = pk_bf(acc[i][j][2], acc[i][j][3]);
        u32 h0 = pk_bf(lo_f(t0) + bv[0], hi_f(t0) + bv[1]);
        u32 h1 = pk_bf(lo_f(t1) + bv[2], hi_f(t1) + bv[3]);
        u32 hb[4] = {h0 & 0xFFFFu, h0 >> 16, h1 & 0xFFFFu, h1 >> 16};
        u32 res[4];
#pragma unroll
        for (int r = 0; r < 4; ++r) {
          u32 a = hb[r] & 0x7FFFu;
          u32 li = a - (LUT_E0 << 7);
          bool in = li < (u32)LUT_HALF;
          u32 v = slut[(in ? li : 0u) + (hb[r] >> 15) * LUT_HALF];
          float h = __uint_as_float(hb[r] << 16);
          float fbv = (a < (LUT_E0 << 7)) ? h * 0.59765625f : (h > 0.f ? h : h * 0.0f);
          u32 fbb = pk_bf(fbv, fbv) & 0xFFFFu;
          res[r] = in ? v : fbb;
        }
        u32x2 ov = {res[0] | (res[1] << 16), res[2] | (res[3] << 16)};
        const int mt = mt0 + wm * 2 + i;
        *(u32x2*)(actP + (((size_t)mt * 54 + kb) * 64 + lane) * 8 + half * 4) = ov;
      }
    }
  }
  STAMP(8);
}

#define SB0() __builtin_amdgcn_sched_barrier(0)
__global__ __launch_bounds__(256, 1) void k3_kernel(const u16* __restrict__ actP, const u16* __restrict__ w2P,
                                                    const u16* __restrict__ xin, const float2* __restrict__ stats,
                                                    const u16* __restrict__ yT, const u16* __restrict__ dskip,
                                                    u16* __restrict__ out) {
  constexpr int R = 5;
  constexpr int NK = FF / 32;
  constexpr int NST = 14;
  __shared__ __attribute__((aligned(1024))) char shm[21 * 1024 + 60 * 1024];
  const int t = threadIdx.x, lane = t & 63;
  const int wave = __builtin_amdgcn_readfirstlane(t >> 6);
  const int xcd = blockIdx.x & 7, idx = blockIdx.x >> 3;
  const int mi = idx & 7, ni = idx >> 3;
  const int bm = (xcd >> 1) * 8 + mi, bn = (xcd & 1) * 4 + ni;
  const int phase = ((27 * mi + 54 * ni) >> 2) % NK;
  const int mt0 = bm * 4, m0 = bm * 64, n0 = bn * 80, nt0 = bn * 5;
  STAMPB(3, (char*)actP - (16u << 20));
  STAMP(0);
#pragma unroll
  for (int i = 0; i < 6; ++i) {
    int p = wave + 4 * i;
    if (p > 20) p = 20;
    int c = (p < 10 ? p : p - 10) * 64 + lane;
    const u16* src;
    if (p < 10) src = xin + (size_t)(m0 + c / 10) * DM + n0 + (c % 10) * 8;
    else if (p < 20) src = yT + (size_t)(n0 + (c >> 3)) * SEQ + m0 + (c & 7) * 8;
    else src = (lane < 32) ? (const u16*)(stats + m0) + lane * 8 : dskip + n0 + (lane < 42 ? lane - 32 : 9) * 8;
    GLDS(src, shm + p * 1024);
  }
  const char* abase = (const char*)actP + (size_t)mt0 * NK * 1024;
  const char* bbase = (const char*)w2P + (size_t)nt0 * NK * 1024;
  u32 va[4], vb[5];
#pragma unroll
  for (int i = 0; i < 4; ++i) va[i] = lane * 16 + ((wave + i) & 3) * (NK * 1024);
#pragma unroll
  for (int j = 0; j < 5; ++j) vb[j] = lane * 16 + j * (NK * 1024);
  u32x4 fa[R][4], fb[R][5];
  auto issue = [&](int s) {
    int q = (s < NST - 1 || wave < 2) ? wave + 4 * s : wave + 4 * (s - 1);
    int ks = q + phase;
    if (ks >= NK) ks -= NK;
    const char* ap = abase + ks * 1024;
    const char* bp = bbase + ks * 1024;
#pragma unroll
    for (int i = 0; i < 4; ++i) fa[s % R][i] = *(const u32x4*)(ap + va[i]);
#pragma unroll
    for (int j = 0; j < 5; ++j) fb[s % R][j] = *(const u32x4*)(bp + vb[j]);
  };
  SB0();
#pragma unroll
  for (int s = 0; s < R; ++s) { issue(s); SB0(); }
  STAMP(1);
  WAIT_V(R * 9);
  RAW_BARRIER();
  STAMP(2);
  const int fr = lane & 15, g = lane >> 4, tq = (lane & 15) >> 2, tp = lane & 3;
  const int ml = wave * 16 + fr;
  u32x2 s1p[5];
  {
    const char* sx = shm;
    const char* sy = shm + 10 * 1024;
    const char* sm = shm + 20 * 1024;
    const float2 st = *(const float2*)(sm + ml * 8);
#pragma unroll
    for (int j = 0; j < 5; ++j) {
      const int nl = j * 16;
      u32x2 xx = *(const u32x2*)(sx + ml * 160 + (nl + 4 * g) * 2);
      u32x2 dd = *(const u32x2*)(sm + 512 + (nl + 4 * g) * 2);
      s4 yy = TRREAD(sy + (nl + 4 * g + tq) * 128 + (wave * 16 + 4 * tp) * 2);
      const u32x2 yw = __builtin_bit_cast(u32x2, yy);
#pragma unroll
      for (int h = 0; h < 2; ++h) {
        const float x0 = lo_f(xx[h]), x1 = hi_f(xx[h]);
        u32 d = pk_bf(x0 - st.x, x1 - st.x);
        u32 xn = pk_bf(lo_f(d) * st.y, hi_f(d) * st.y);
        u32 tt = pk_bf(lo_f(xn) * lo_f(dd[h]), hi_f(xn) * hi_f(dd[h]));
        u32 a = pk_bf(lo_f(yw[h]) + lo_f(tt), hi_f(yw[h]) + hi_f(tt));
        s1p[j][h] = pk_bf(x0 + lo_f(a), x1 + hi_f(a));
      }
      asm volatile("" : "+v"(s1p[j]));
    }
  }
  f4 acc[4][5];
#pragma unroll
  for (int i = 0; i < 4; ++i)
#pragma unroll
    for (int j = 0; j < 5; ++j) acc[i][j] = (f4){0.f, 0.f, 0.f, 0.f};
  SB0();
  STAMP(3);
#pragma unroll
  for (int s = 0; s < NST; ++s) {
    if (s == NST - 1) {
#pragma unroll
      for (int i = 0; i < 4; ++i) asm volatile("" : "+v"(fa[s % R][i]));
#pragma unroll
      for (int j = 0; j < 5; ++j) asm volatile("" : "+v"(fb[s % R][j]));
    }
    if (s < NST - 1 || wave < 2) {
#pragma unroll
      for (int i = 0; i < 4; ++i)
#pragma unroll
        for (int j = 0; j < 5; ++j)
          acc[i][j] = __builtin_amdgcn_mfma_f32_16x16x32_bf16(__builtin_bit_cast(s8, fb[s % R][j]),
                                                              __builtin_bit_cast(s8, fa[s % R][i]), acc[i][j], 0, 0, 0);
    }
    SB0();
    if (s + R < NST) issue(s + R);
    SB0();
    if (s == 2) STAMP(4);
    if (s == 6) STAMP(5);
    if (s == 10) STAMP(6);
  }
  STAMP(7);
  char* red = shm + 21 * 1024;
#pragma unroll
  for (int i = 1; i < 4; ++i)
#pragma unroll
    for (int j = 0; j < 5; ++j)
      *(f4*)(red + ((((wave + i) & 3) * 3 + (i - 1)) * 5 + j) * 1024 + lane * 16) = acc[i][j];
  __syncthreads();
#pragma unroll
  for (int j = 0; j < 5; ++j) {
    f4 p0 = *(const f4*)(red + ((wave * 3 + 0) * 5 + j) * 1024 + lane * 16);
    f4 p1 = *(const f4*)(red + ((wave * 3 + 1) * 5 + j) * 1024 + lane * 16);
    f4 p2 = *(const f4*)(red + ((wave * 3 + 2) * 5 + j) * 1024 + lane * 16);
    f4 sum = (acc[0][j] + p0) + (p1 + p2);
    u32 m0p = pk_bf(sum[0], sum[1]), m1p = pk_bf(sum[2], sum[3]);
    u32x2 ov = {pk_bf(lo_f(s1p[j][0]) + lo_f(m0p), hi_f(s1p[j][0]) + hi_f(m0p)),
                pk_bf(lo_f(s1p[j][1]) + lo_f(m1p), hi_f(s1p[j][1]) + hi_f(m1p))};
    *(u32x2*)(out + (size_t)(m0 + ml) * DM + n0 + j * 16 + 4 * g) = ov;
  }
  STAMP(8);
}

extern "C" void kernel_launch(void* const* d_in, const int* in_sizes, int n_in,
                              void* d_out, int out_size, void* d_ws, size_t ws_size,
                              hipStream_t stream) {
  const u16* u = (const u16*)d_in[0];
  const u16* lar = (const u16*)d_in[1];
  const u16* aim = (const u16*)d_in[2];
  const u16* cre = (const u16*)d_in[3];
  const u16* cim = (const u16*)d_in[4];
  const u16* logdt = (const u16*)d_in[5];
  const u16* dskip = (const u16*)d_in[6];
  const u16* w1 = (const u16*)d_in[8];
  const u16* b1 = (const u16*)d_in[9];
  const u16* w2 = (const u16*)d_in[10];
  u16* out = (u16*)d_out;
  char* ws = (char*)d_ws;
  if (ws_size < (32u << 20)) return;
  u16* xnP = (u16*)(ws);
  u16* w1P = (u16*)(ws + (4u << 20));
  u16* Kr = (u16*)(ws + (8u << 20));
  float* ksc = (float*)(ws + (12u << 20));
  float2* stats = (float2*)(ws + (12u << 20) + 4096);
  u16* lut = (u16*)(ws + (12u << 20) + 32768);
  u16* yT = (u16*)(ws + (13u << 20));
  u16* actP = (u16*)(ws + (16u << 20));
  u16* w2P = (u16*)(ws + (24u << 20));

  k1_kernel<<<K1_LN + K1_KG + K1_LUT + K1_RP, 256, 0, stream>>>(u, xnP, stats, lut, lar, aim, cre, cim, logdt, Kr, ksc, w1, w1P);
  k2_kernel<<<K2_G1 + K2_CONV + K2_RP, 512, 0, stream>>>(xnP, w1P, b1, lut, actP, Kr, ksc, yT, w2, w2P);
  k3_kernel<<<256, 256, 0, stream>>>(actP, w2P, u, stats, yT, dskip, out);
}
```

```cpp
#include <hip/hip_runtime.h>
#include <hip/hip_bf16.h>

#define SEQ 2048
#define DM 640
#define NS 128
#define FF 1728

typedef unsigned short u16;
typedef short s4 __attribute__((ext_vector_type(4)));
typedef short s8 __attribute__((ext_vector_type(8)));
typedef _Float16 h8 __attribute__((ext_vector_type(8)));
typedef float f4 __attribute__((ext_vector_type(4)));
typedef unsigned int u32;
typedef unsigned int u32x4 __attribute__((ext_vector_type(4)));
typedef unsigned int u32x2 __attribute__((ext_vector_type(2)));

__device__ __forceinline__ float bf2f(u16 v) { return __uint_as_float(((u32)v) << 16); }
__device__ __forceinline__ u16 f2bf(float f) {
  u32 u = __float_as_uint(f);
  u32 r = (u + 0x7FFFu + ((u >> 16) & 1u)) >> 16;
  return (u16)r;
}
__device__ __forceinline__ float bfr(float f) { return bf2f(f2bf(f)); }

#define STAMPB(kid, base)
#define STAMP(k)
typedef __bf16 bf2_t __attribute__((ext_vector_type(2)));
typedef float f2_t __attribute__((ext_vector_type(2)));
__device__ __forceinline__ u32 pk_bf(float a, float b) {
  f2_t v = {a, b};
  bf2_t r = __builtin_convertvector(v, bf2_t);
  return __builtin_bit_cast(u32, r);
}
__device__ __forceinline__ float lo_f(u32 w) { return __uint_as_float(w << 16); }
__device__ __forceinline__ float hi_f(u32 w) { return __uint_as_float(w & 0xFFFF0000u); }

#define WAIT_V(n) asm volatile("s_waitcnt vmcnt(%0)" ::"n"(n) : "memory")
#define RAW_BARRIER() do { asm volatile("s_waitcnt lgkmcnt(0)" ::: "memory"); __builtin_amdgcn_s_barrier(); } while (0)
__device__ __forceinline__ void glds16(const void* gsrc, const void* lp) {
  unsigned keep;
  unsigned lds_dst = (unsigned)(size_t)(const __attribute__((address_space(3))) char*)lp;
  asm volatile("s_mov_b32 %0, m0\n\ts_mov_b32 m0, %2\n\ts_nop 0\n\tglobal_load_lds_dwordx4 %1, off\n\ts_mov_b32 m0, %0"
               : "=&s"(keep) : "v"(gsrc), "s"(lds_dst) : "memory");
}
#define GLDS(gp, lp) glds16((const void*)(gp), (const void*)(lp))

#define LUT_E0 111
#define LUT_E1 134
#define LUT_HALF ((LUT_E1 - LUT_E0) * 128)
#define LUT_N (2 * LUT_HALF)

__device__ __forceinline__ float mish_bf(float h) {
  float e = bfr(expf(-fabsf(h)));
  float l = bfr(log1pf(e));
  float sp = bfr(l + (h > 0.f ? h : 0.f));
  float th = bfr(tanhf(sp));
  return bfr(h * th);
}

#define KRLEN 2080
#define WFENCE() do { asm volatile("" ::: "memory"); __builtin_amdgcn_wave_barrier(); asm volatile("" ::: "memory"); } while (0)
__device__ __forceinline__ void cexp_p(float arl2, float w, float p, float& re, float& im) {
  float mag = __builtin_amdgcn_exp2f(arl2 * p);
  float rev = w * p;
  rev = rev - floorf(rev);
  re = mag * __builtin_amdgcn_cosf(rev);
  im = mag * __builtin_amdgcn_sinf(rev);
}
__device__ __forceinline__ void cmul(float ar, float ai, float br, float bi, float& cr, float& ci) {
  float r = fmaf(ar, br, -(ai * bi));
  float i = fmaf(ar, bi, ai * br);
  cr = r; ci = i;
}
__device__ __forceinline__ u32x2 split2(float a, float b) {
  u32 h = pk_bf(a, b);
  u32 l = pk_bf(a - lo_f(h), b - hi_f(h));
  return (u32x2){h, l};
}

#define K1_LN 256
#define K1_RP 135
#define K1_LUT 24
#define WPITCH 544
#define TRREAD(p) __builtin_amdgcn_ds_read_tr16_b64_v4i16((__attribute__((address_space(3))) s4*)(p))

__device__ __forceinline__ void repack_load(const u16* __restrict__ w, int N, int ku, int nu, char* lds, int tl) {
#pragma unroll
  for (int i = 0; i < 2; ++i) {
    int idx = tl + 256 * i, r = idx >> 3, c = idx & 7;
    u32x4 v = *(const u32x4*)(w + (size_t)(ku * 64 + r) * N + nu * 64 + c * 8);
    *(u32x4*)(lds + r * 160 + c * 16) = v;
  }
}
__device__ __forceinline__ void repack_store(u16* __restrict__ wP, int KB, int ku, int nu, const char* lds, int tl) {
  const int wl = tl >> 6, lane = tl & 63, g = lane >> 4, tq = (lane & 15) >> 2, tp = lane & 3;
#pragma unroll
  for (int kb2 = 0; kb2 < 2; ++kb2) {
    const char* p0 = lds + (kb2 * 32 + 4 * g + tq) * 160 + wl * 32 + tp * 8;
    s4 b0 = TRREAD(p0);
    s4 b1 = TRREAD(p0 + 16 * 160);
    s8 bf = {b0[0], b0[1], b0[2], b0[3], b1[0], b1[1], b1[2], b1[3]};
    *(s8*)(wP + (((size_t)(nu * 4 + wl) * KB + ku * 2 + kb2) * 64 + lane) * 8) = bf;
  }
}

__global__ __launch_bounds__(256) void k1_kernel(const u16* __restrict__ u, u16* __restrict__ xnP,
                                                 float2* __restrict__ stats, u16* __restrict__ lut,
                                                 const u16* __restrict__ w1, u16* __restrict__ w1P,
                                                 const u16* __restrict__ w2, u16* __restrict__ w2P) {
  __shared__ __attribute__((aligned(16))) char k1s[20480];
  const int t = threadIdx.x, lane = t & 63;
  const int wave = __builtin_amdgcn_readfirstlane(t >> 6);
  STAMPB(0, xnP);
  STAMP(0);
  if (blockIdx.x < K1_LN) {
    u16 (*tile)[DM + 8] = (u16 (*)[DM + 8])k1s;
    const int r = wave * 2 + (lane >> 5), q = lane & 31;
    const int row = blockIdx.x * 8 + r;
    float v[20];
    const u32x2* src = (const u32x2*)(u + (size_t)row * DM);
#pragma unroll
    for (int i = 0; i < 5; ++i) {
      u32x2 w = src[q + 32 * i];
      v[i * 4 + 0] = lo_f(w[0]); v[i * 4 + 1] = hi_f(w[0]);
      v[i * 4 + 2] = lo_f(w[1]); v[i * 4 + 3] = hi_f(w[1]);
    }
    float s = 0.f;
#pragma unroll
    for (int i = 0; i < 20; ++i) s += v[i];
    STAMP(1);
#pragma unroll
    for (int o = 1; o < 32; o <<= 1) s += __shfl_xor(s, o);
    const float mean = s * (1.0f / DM);
    float ss = 0.f;
#pragma unroll
    for (int i = 0; i < 20; ++i) { float d = v[i] - mean; ss += d * d; }
#pragma unroll
    for (int o = 1; o < 32; o <<= 1) ss += __shfl_xor(ss, o);
    const float var = ss * (1.0f / DM);
    const float mb = bfr(mean);
    const float vb = bfr(var);
    const float den = bfr(sqrtf(bfr(vb + 1e-5f)));
    const float rden = 1.0f / den;
    if (q == 0) stats[row] = make_float2(mb, rden);
#pragma unroll
    for (int i = 0; i < 5; ++i) {
      u32 d0 = pk_bf(v[i * 4 + 0] - mb, v[i * 4 + 1] - mb);
      u32 d1 = pk_bf(v[i * 4 + 2] - mb, v[i * 4 + 3] - mb);
      u32x2 w = {pk_bf(lo_f(d0) * rden, hi_f(d0) * rden), pk_bf(lo_f(d1) * rden, hi_f(d1) * rden)};
      *(u32x2*)&tile[r][(q + 32 * i) * 4] = w;
    }
    __syncthreads();
    STAMP(2);
    const int mt = blockIdx.x >> 1, half = blockIdx.x & 1;
#pragma unroll
    for (int i = 0; i < 3; ++i) {
      int idx = t + 256 * i;
      if (idx < 640) {
        int kb = idx >> 5, g = (idx >> 3) & 3, fr = idx & 7;
        u32x2 a = *(const u32x2*)&tile[fr][kb * 32 + 4 * g];
        u32x2 b = *(const u32x2*)&tile[fr][kb * 32 + 16 + 4 * g];
        u32x4 w = {a[0], a[1], b[0], b[1]};
        *(u32x4*)(xnP + (((size_t)mt * 20 + kb) * 64 + g * 16 + half * 8 + fr) * 8) = w;
      }
    }
    STAMP(3);
    return;
  }
  if (blockIdx.x < K1_LN + K1_RP) {
    const int u0 = (blockIdx.x - K1_LN) * 2;
    repack_load(w1, FF, u0 / 27, u0 % 27, k1s, t);
    repack_load(w1, FF, (u0 + 1) / 27, (u0 + 1) % 27, k1s + 10240, t);
    __syncthreads();
    repack_store(w1P, DM / 32, u0 / 27, u0 % 27, k1s, t);
    repack_store(w1P, DM / 32, (u0 + 1) / 27, (u0 + 1) % 27, k1s + 10240, t);
    STAMP(3);
    return;
  }
  if (blockIdx.x < K1_LN + 2 * K1_RP) {
    const int u0 = (blockIdx.x - (K1_LN + K1_RP)) * 2;
    repack_load(w2, DM, u0 / 10, u0 % 10, k1s, t);
    repack_load(w2, DM, (u0 + 1) / 10, (u0 + 1) % 10, k1s + 10240, t);
    __syncthreads();
    repack_store(w2P, FF / 32, u0 / 10, u0 % 10, k1s, t);
    repack_store(w2P, FF / 32, (u0 + 1) / 10, (u0 + 1) % 10, k1s + 10240, t);
    STAMP(3);
    return;
  }
  {
    int idx = (blockIdx.x - (K1_LN + 2 * K1_RP)) * 256 + t;
    if (idx < LUT_N) {
      int sgn = idx >= LUT_HALF;
      int a = idx - sgn * LUT_HALF + (LUT_E0 << 7);
      float h = bf2f((u16)(a | (sgn << 15)));
      lut[idx] = f2bf(mish_bf(h));
    } else if (idx < 6144) {
      lut[idx] = 0;
    }
  }
}

#define K2_G1 256
#define K2_SSM 640
#define KCPITCH 2080
#define SB0() __builtin_amdgcn_sched_barrier(0)
typedef _Float16 h2_t __attribute__((ext_vector_type(2)));
__device__ __forceinline__ u32 pk_h(float a, float b) {
  f2_t v = {a, b};
  h2_t r = __builtin_convertvector(v, h2_t);
  return __builtin_bit_cast(u32, r);
}
__device__ __forceinline__ void glds16s(u32 voff, const void* sbase, const void* lp) {
  unsigned keep;
  unsigned lds_dst = (unsigned)(size_t)(const __attribute__((address_space(3))) char*)lp;
  asm volatile("s_mov_b32 %0, m0\n\ts_mov_b32 m0, %3\n\ts_nop 0\n\tglobal_load_lds_dwordx4 %1, %2\n\ts_mov_b32 m0, %0"
               : "=&s"(keep) : "v"(voff), "s"(sbase), "s"(lds_dst) : "memory");
}
#define MFMA_BF(acc, a, b) \
  asm volatile("v_mfma_f32_16x16x32_bf16 %0, %1, %2, %0" : "+v"(acc) : "v"(a), "v"(b))
#define MFMA_BF0(acc, a, b) \
  asm volatile("v_mfma_f32_16x16x32_bf16 %0, %1, %2, 0" : "=&v"(acc) : "v"(a), "v"(b))
#define GLOAD4(dst, voff, sbase) \
  asm volatile("global_load_dwordx4 %0, %1, %2" : "=v"(dst) : "v"(voff), "s"(sbase) : "memory")

__global__ __launch_bounds__(256, 4) void k2_kernel(const u16* __restrict__ xnP, const u16* __restrict__ w1P,
                                                    const u16* __restrict__ b1, const u16* __restrict__ lut,
                                                    u16* __restrict__ actP,
                                                    const u16* __restrict__ lar, const u16* __restrict__ aim,
                                                    const u16* __restrict__ cre, const u16* __restrict__ cim,
                                                    const u16* __restrict__ logdt, u16* __restrict__ yT) {
  __shared__ __attribute__((aligned(1024))) char shm[40960];
  const int t = threadIdx.x, lane = t & 63;
  const int wave = __builtin_amdgcn_readfirstlane(t >> 6);
  STAMPB(2, (char*)actP - (16u << 20));
  STAMP(0);
  if (blockIdx.x >= K2_G1) {
    const int sb = blockIdx.x - K2_G1;
    const int li = sb >> 3;
    const int grp = (sb & 7) * 10 + (li >> 3), ee = li & 7;
    const int kb = grp >> 2, gg = grp & 3;
    const int d = kb * 32 + (ee >> 2) * 16 + gg * 4 + (ee & 3);
    const int n = t & 127;
    u16* P = (u16*)(shm + 34816);
    u16* xs = (u16*)(shm + 36352);
    u16* Kc = (u16*)shm;
    const u16 r_dt = logdt[d];
    const u16 r_ar = lar[d * NS + n], r_ai = aim[d * NS + n];
    const u16 r_cr = cre[d * NS + n], r_ci = cim[d * NS + n];
    const u16 r_cr2 = cre[d * NS + (n ^ 64)], r_ci2 = cim[d * NS + (n ^ 64)];
    u16 xv[8];
    {
      const u16* xp = xnP + (((size_t)(t >> 3) * 20 + kb) * 64 + gg * 16 + ((2 * t) & 15)) * 8 + ee;
#pragma unroll
      for (int i = 0; i < 4; ++i) {
        xv[2 * i] = xp[(size_t)i * 327680];
        xv[2 * i + 1] = xp[(size_t)i * 327680 + 8];
      }
    }
    const float dt = bfr(expf(bf2f(r_dt)));
    const float ar = -bfr(expf(bf2f(r_ar))) * dt;
    const float ai = bf2f(r_ai) * dt;
    float cr = bf2f(r_cr) * dt, ci = bf2f(r_ci) * dt;
    float bs = (fabsf(cr) + fabsf(ci)) + (fabsf(bf2f(r_cr2) * dt) + fabsf(bf2f(r_ci2) * dt));
#pragma unroll
    for (int o = 1; o < 64; o <<= 1) bs += __shfl_xor(bs, o);
    const float bound = 2.0f * bs;
    int ex = 0;
    if (bound > 0.f && bound < 3.0e38f) { (void)frexpf(bound, &ex); }
    int es = 15 - ex;
    es = es > 100 ? 100 : (es < -100 ? -100 : es);
    const float sc2 = ldexpf(2.0f, es);
    const float kscale = ldexpf(1.0f, -es);
    cr *= sc2; ci *= sc2;
    const float arl2 = ar * 1.4426950408889634f;
    const float w = ai * 0.15915494309189535f;
    STAMP(1);
    char* wr = shm + n * 4;
    float eHr, eHi;
    cexp_p(arl2, w, 128.0f, eHr, eHi);
    if (wave < 2) {
      float e8r, e8i, e4r, e4i, gr, gi;
      cexp_p(arl2, w, 8.0f, e8r, e8i);
      cexp_p(arl2, w, 4.0f, e4r, e4i);
      cexp_p(arl2, w, 512.0f, gr, gi);
      cmul(gr, gi, cr, ci, gr, gi);
      float lr = 1.f, lim = 0.f;
      *(u32*)(wr) = pk_h(1.f, 0.f);
#pragma unroll
      for (int j = 1; j < 16; ++j) {
        cmul(lr, lim, e8r, e8i, lr, lim);
        *(u32*)(wr + j * WPITCH) = pk_h(lr, -lim);
        if (j < 5) {
          float hr, hi;
          *(u32*)(wr + (40 + (j - 1) * 2) * WPITCH) = pk_h(gr, gi);
          cmul(gr, gi, e4r, e4i, hr, hi);
          *(u32*)(wr + (41 + (j - 1) * 2) * WPITCH) = pk_h(hr, hi);
          cmul(gr, gi, eHr, eHi, gr, gi);
        } else if (j < 14) {
          *(u32*)(wr + (48 + j - 5) * WPITCH) = pk_h(gr, gi);
          if (j < 13) cmul(gr, gi, eHr, eHi, gr, gi);
        }
      }
    } else {
      float Rr[8], Ri[8];
      Rr[0] = 1.f; Ri[0] = 0.f;
      cexp_p(arl2, w, 1.0f, Rr[1], Ri[1]);
#pragma unroll
      for (int r = 2; r < 8; ++r) cmul(Rr[r - 1], Ri[r - 1], Rr[1], Ri[1], Rr[r], Ri[r]);
      float gr = cr, gi = ci;
#pragma unroll
      for (int ii = 0; ii < 2; ++ii) {
        *(u32*)(wr + (16 + ii * 8) * WPITCH) = pk_h(gr, gi);
#pragma unroll
        for (int r = 1; r < 8; ++r) {
          float hr, hi;
          cmul(gr, gi, Rr[r], Ri[r], hr, hi);
          *(u32*)(wr + (16 + ii * 8 + r) * WPITCH) = pk_h(hr, hi);
        }
        cmul(gr, gi, eHr, eHi, gr, gi);
      }
#pragma unroll
      for (int ii = 2; ii < 4; ++ii) {
        *(u32*)(wr + (32 + (ii - 2) * 4) * WPITCH) = pk_h(gr, gi);
#pragma unroll
        for (int r = 1; r < 4; ++r) {
          float hr, hi;
          cmul(gr, gi, Rr[2 * r], Ri[2 * r], hr, hi);
          *(u32*)(wr + (32 + (ii - 2) * 4 + r) * WPITCH) = pk_h(hr, hi);
        }
        if (ii < 3) cmul(gr, gi, eHr, eHi, gr, gi);
      }
    }
    if (t < 32) *(u32x4*)&xs[t * 8] = (u32x4){0u, 0u, 0u, 0u};
#pragma unroll
    for (int i = 0; i < 4; ++i)
      *(u32*)&xs[256 + 2 * t + 512 * i] = pk_h(bf2f(xv[2 * i]), bf2f(xv[2 * i + 1]));
    __syncthreads();
    STAMP(2);
    const int b = lane & 15, g = lane >> 4;
    if (wave < 3) {
      const char* ra = shm + (16 + 16 * wave + b) * WPITCH + g * 16;
      const char* rb = shm + b * WPITCH + g * 16;
      f4 ka = (f4){0.f, 0.f, 0.f, 0.f};
#pragma unroll
      for (int k8 = 0; k8 < 8; ++k8) {
        h8 a = *(const h8*)(ra + k8 * 64);
        h8 bb = *(const h8*)(rb + k8 * 64);
        ka = __builtin_amdgcn_mfma_f32_16x16x32_f16(a, bb, ka, 0, 0, 0);
      }
      const u32 k01 = pk_h(ka[0], ka[1]), k23 = pk_h(ka[2], ka[3]);
      if (wave == 0) {
        *(u32x2*)&P[(g >> 1) * 128 + (g & 1) * 4 + 8 * b] = (u32x2){k01, k23};
      } else if (wave == 1) {
        const int i1 = g < 2 ? 256 + 64 * g + 4 * b : 384 + 64 * (g - 2) + 2 * b;
        *(u32*)&P[i1] = k01;
        *(u32*)&P[i1 + (g < 2 ? 2 : 32)] = k23;
      } else {
        const int i2 = 512 + 64 * g + b;
        P[i2] = (u16)k01; P[i2 + 16] = (u16)(k01 >> 16);
        P[i2 + 32] = (u16)k23; P[i2 + 48] = (u16)(k23 >> 16);
      }
    }
    __syncthreads();
    STAMP(3);
    {
      u32 wv[6];
#pragma unroll
      for (int hh = 0; hh < 2; ++hh) {
        const int m = 255 - t - hh;
        const int sg = m >= 128 ? 3 : (m >= 64 ? 2 : (m >= 32 ? 1 : 0));
        const int start = sg == 0 ? 0 : (128 << sg);
        const int base = sg == 0 ? 0 : 128 + 128 * sg;
        const int one = sg ? 1 : 0;
        const int hm = ((1 << sg) >> 1) - one;
#pragma unroll
        for (int j = 0; j < (hh ? 2 : 4); ++j) {
          u32 pk = 0;
#pragma unroll
          for (int h = 0; h < 2; ++h) {
            if (hh == 0 || 2 * j + h < 3) {
              int q = 8 * m + 7 - (2 * j + h) - start;
              q = q < 0 ? 0 : q;
              int idx = base + ((q + hm + ((q >> sg) & one)) >> sg);
              pk |= ((u32)P[idx]) << (16 * h);
            }
          }
          wv[hh * 4 + j] = (hh && m < 0) ? 0u : pk;
        }
      }
      *(u32x4*)&Kc[0 * KCPITCH + t * 8] = (u32x4){wv[0], wv[1], wv[2], wv[3]};
      *(u32x4*)&Kc[1 * KCPITCH + t * 8] = (u32x4){__builtin_amdgcn_alignbit(wv[1], wv[0], 16), __builtin_amdgcn_alignbit(wv[2], wv[1], 16),
                                                  __builtin_amdgcn_alignbit(wv[3], wv[2], 16), __builtin_amdgcn_alignbit(wv[4], wv[3], 16)};
      *(u32x4*)&Kc[2 * KCPITCH + t * 8] = (u32x4){wv[1], wv[2], wv[3], wv[4]};
      *(u32x4*)&Kc[3 * KCPITCH + t * 8] = (u32x4){__builtin_amdgcn_alignbit(wv[2], wv[1], 16), __builtin_amdgcn_alignbit(wv[3], wv[2], 16),
                                                  __builtin_amdgcn_alignbit(wv[4], wv[3], 16), __builtin_amdgcn_alignbit(wv[5], wv[4], 16)};
      if (t < 8) *(u32x4*)&Kc[(t >> 1) * KCPITCH + (256 + (t & 1)) * 8] = (u32x4){0u, 0u, 0u, 0u};
    }
    __syncthreads();
    STAMP(4);
    f4 acc[8];
#pragma unroll
    for (int i = 0; i < 8; ++i) acc[i] = (f4){0.f, 0.f, 0.f, 0.f};
    const int abase = ((3 - b) & 3) * KCPITCH + 2028 - 4 * (b >> 2) + 8 * g;
    const int bbase = 256 - 16 + 16 * b + 8 * g;
    const u32 kc_lds = (u32)(size_t)(const __attribute__((address_space(3))) char*)shm;
#pragma unroll
    for (int rr = 0; rr < 2; ++rr) {
      const int rp = wave * 2 + rr;
      h8 A[8], B[8];
      u32x2 a0[8], a1[8];
      const u32 aaddr = kc_lds + (u32)(abase - 32 * rp - 256 * 7) * 2;
#pragma unroll
      for (int u = 0; u < 8; ++u) {
        asm volatile("ds_read_b64 %0, %1 offset:%2" : "=v"(a0[u]) : "v"(aaddr), "n"((7 - u) * 512) : "memory");
        asm volatile("ds_read_b64 %0, %1 offset:%2" : "=v"(a1[u]) : "v"(aaddr), "n"((7 - u) * 512 + 8) : "memory");
      }
#pragma unroll
      for (int u = 0; u < 8; ++u) B[u] = *(const h8*)&xs[bbase - 32 * rp + 256 * u];
      asm volatile("s_waitcnt lgkmcnt(0)"
                   : "+v"(a0[0]), "+v"(a1[0]), "+v"(a0[1]), "+v"(a1[1]), "+v"(a0[2]), "+v"(a1[2]), "+v"(a0[3]), "+v"(a1[3]),
                     "+v"(a0[4]), "+v"(a1[4]), "+v"(a0[5]), "+v"(a1[5]), "+v"(a0[6]), "+v"(a1[6]), "+v"(a0[7]), "+v"(a1[7])
                   :: "memory");
#pragma unroll
      for (int u = 0; u < 8; ++u) A[u] = __builtin_bit_cast(h8, (u32x4){a0[u][0], a0[u][1], a1[u][0], a1[u][1]});
#pragma unroll
      for (int u = 0; u < 8; ++u)
#pragma unroll
        for (int v = 0; v + u < 8; ++v)
          acc[u + v] = __builtin_amdgcn_mfma_f32_16x16x32_f16(A[u], B[v], acc[u + v], 0, 0, 0);
    }
    __syncthreads();
    STAMP(5);
    float* red = (float*)shm;
#pragma unroll
    for (int tt = 0; tt < 8; ++tt) *(f4*)&red[((wave * 8 + tt) * 64 + lane) * 4] = acc[tt];
    __syncthreads();
#pragma unroll
    for (int i = 0; i < 2; ++i) {
      const int tt = wave * 2 + i;
      f4 s0 = *(const f4*)&red[((0 * 8 + tt) * 64 + lane) * 4];
      f4 s1 = *(const f4*)&red[((1 * 8 + tt) * 64 + lane) * 4];
      f4 s2 = *(const f4*)&red[((2 * 8 + tt) * 64 + lane) * 4];
      f4 s3 = *(const f4*)&red[((3 * 8 + tt) * 64 + lane) * 4];
      f4 sum = ((s0 + s1) + (s2 + s3)) * kscale;
      u32x2 o = {pk_bf(sum[0], sum[1]), pk_bf(sum[2], sum[3])};
      *(u32x2*)(yT + (size_t)d * SEQ + 256 * tt + 16 * b + 4 * g) = o;
    }
    STAMP(8);
    return;
  }
  constexpr int ND = 5;
  constexpr int STG = 7 * 1024;
  constexpr int NKT = DM / 32;
  const int xcd = blockIdx.x & 7, idx = blockIdx.x >> 3;
  const int mi = idx & 3, ni = idx >> 2;
  const int bm = (xcd >> 1) * 4 + mi, bn = (xcd & 1) * 8 + ni;
  const int mt0 = bm * 8, nt0 = bn * 7;
  const char* abase = (const char*)xnP + (size_t)(mt0 + 2 * wave) * (20 * 1024);
  const u32 va0 = lane * 16, va1 = lane * 16 + 20 * 1024;
  const int p0 = 2 * wave, p1 = wave < 3 ? 2 * wave + 1 : 6;
  const u32 vb0 = lane * 16 + (u32)(nt0 + p0 > 107 ? 107 : nt0 + p0) * (20 * 1024);
  const u32 vb1 = lane * 16 + (u32)(nt0 + p1 > 107 ? 107 : nt0 + p1) * (20 * 1024);
  auto stage = [&](int s) {
    char* sb = shm + (s % ND) * STG;
    const char* bp = (const char*)w1P + s * 1024;
    glds16s(vb0, bp, sb + p0 * 1024);
    if (wave < 3) glds16s(vb1, bp, sb + p1 * 1024);
  };
  u32x4 fa[3][2];
  auto loadA = [&](int s) {
    const char* ap = abase + s * 1024;
    GLOAD4(fa[s % 3][0], va0, ap);
    GLOAD4(fa[s % 3][1], va1, ap);
  };
  f4 acc[2][7];
  s8 fb[7];
  stage(0); stage(1); loadA(0); stage(2); loadA(1); stage(3); loadA(2);
  STAMP(1);
  if (wave < 3) WAIT_V(12); else WAIT_V(9);
  __builtin_amdgcn_s_barrier();
  STAMP(2);
#pragma unroll
  for (int j = 0; j < 7; ++j) fb[j] = *(const s8*)(shm + j * 1024 + lane * 16);
#pragma unroll
  for (int kt = 0; kt < NKT; ++kt) {
    {
      constexpr int dummy = 0; (void)dummy;
      const int g2 = ((kt - 2 <= 17) ? 1 : 0) + ((kt - 1 <= 17) ? 1 : 0);
      const int a2 = ((kt - 2 <= 16) ? 2 : 0) + ((kt - 1 <= 16) ? 2 : 0);
      if (wave < 3) { switch (2 * g2 + a2) { case 8: WAIT_V(8); break; case 6: WAIT_V(6); break; default: WAIT_V(2); break; } }
      else { switch (g2 + a2) { case 6: WAIT_V(6); break; case 4: WAIT_V(4); break; default: WAIT_V(1); break; } }
      asm volatile("" : "+v"(fa[kt % 3][0]), "+v"(fa[kt % 3][1]));
    }
    __builtin_amdgcn_s_barrier();
    if (kt + ND - 1 < NKT) stage(kt + ND - 1);
    else if (kt == NKT - ND + 1) {
      glds16s(lane * 16, (const char*)lut + p0 * 1024, shm + p0 * 1024);
      if (wave < 3) glds16s(lane * 16, (const char*)lut + p1 * 1024, shm + p1 * 1024);
    } else if (kt == NKT - ND + 2) {
      int o = nt0 * 16 + (lane < 14 ? lane : 13) * 8; if (o > FF - 8) o = FF - 8;
      if (wave < 3) glds16s(lane * 16, (const char*)lut + (7 + p0) * 1024, shm + (7 + p0) * 1024);
      if (wave < 2) glds16s(lane * 16, (const char*)lut + (7 + p1) * 1024, shm + (7 + p1) * 1024);
      else glds16s(o * 2, (const char*)b1, shm + 12 * 1024);
    }
    SB0();
#pragma unroll
    for (int j = 0; j < 7; ++j) {
      if (kt == 0) {
        MFMA_BF0(acc[0][j], fb[j], fa[0][0]);
        MFMA_BF0(acc[1][j], fb[j], fa[0][1]);
      } else {
        MFMA_BF(acc[0][j], fb[j], fa[kt % 3][0]);
        MFMA_BF(acc[1][j], fb[j], fa[kt % 3][1]);
      }
      if (kt + 1 < NKT) fb[j] = *(const s8*)(shm + ((kt + 1) % ND) * STG + j * 1024 + lane * 16);
      SB0();
    }
    if (kt + 3 < NKT) loadA(kt + 3);
    SB0();
    if (kt == 4) STAMP(3);
    if (kt == 12) STAMP(4);
  }
  asm volatile("s_nop 7\n\ts_nop 7\n\ts_nop 7" ::: "memory");
  WAIT_V(0);
  __builtin_amdgcn_s_barrier();
  STAMP(7);
  const int g = lane >> 4;
  const u16* slut = (const u16*)(shm);
  const u16* sbias = (const u16*)(shm + 12 * 1024);
#pragma unroll
  for (int j = 0; j < 7; ++j) {
    const int nt = nt0 + j;
    if (nt < 108) {
      u32x2 bb = *(const u32x2*)(sbias + j * 16 + 4 * g);
      const float bv[4] = {lo_f(bb[0]), hi_f(bb[0]), lo_f(bb[1]), hi_f(bb[1])};
      const int kb = nt >> 1, half = nt & 1;
#pragma unroll
      for (int i = 0; i < 2; ++i) {
        u32 t0 = pk_bf(acc[i][j][0], acc[i][j][1]);
        u32 t1 = pk_bf(acc[i][j][2], acc[i][j][3]);
        u32 h0 = pk_bf(lo_f(t0) + bv[0], hi_f(t0) + bv[1]);
        u32 h1 = pk_bf(lo_f(t1) + bv[2], hi_f(t1) + bv[3]);
        u32 hb[4] = {h0 & 0xFFFFu, h0 >> 16, h1 & 0xFFFFu, h1 >> 16};
        u32 res[4];
        u32 mx = 0;
#pragma unroll
        for (int r = 0; r < 4; ++r) {
          u32 lx = (hb[r] & 0x7FFFu) - (LUT_E0 << 7);
          mx = mx > lx ? mx : lx;
          res[r] = slut[(lx & 0x1FFFu) + (hb[r] >> 15) * LUT_HALF];
        }
        if (__builtin_amdgcn_ballot_w64(mx >= (u32)LUT_HALF) != 0) {
#pragma unroll
          for (int r = 0; r < 4; ++r) {
            u32 a = hb[r] & 0x7FFFu;
            bool in = (a - (LUT_E0 << 7)) < (u32)LUT_HALF;
            float h = __uint_as_float(hb[r] << 16);
            float fbv = (a < (LUT_E0 << 7)) ? h * 0.59765625f : (h > 0.f ? h : h * 0.0f);
            u32 fbb = pk_bf(fbv, fbv) & 0xFFFFu;
            res[r] = in ? res[r] : fbb;
          }
        }
        u32x2 ov = {res[0] | (res[1] << 16), res[2] | (res[3] << 16)};
        const int mt = mt0 + wave * 2 + i;
        *(u32x2*)(actP + (((size_t)mt * 54 + kb) * 64 + lane) * 8 + half * 4) = ov;
      }
    }
  }
  STAMP(8);
}

__global__ __launch_bounds__(256, 1) void k3_kernel(const u16* __restrict__ actP, const u16* __restrict__ w2P,
                                                    const u16* __restrict__ xin, const float2* __restrict__ stats,
                                                    const u16* __restrict__ yT, const u16* __restrict__ dskip,
                                                    u16* __restrict__ out) {
  constexpr int R = 5;
  constexpr int NK = FF / 32;
  constexpr int NST = 14;
  __shared__ __attribute__((aligned(1024))) char shm[21 * 1024 + 60 * 1024];
  const int t = threadIdx.x, lane = t & 63;
  const int wave = __builtin_amdgcn_readfirstlane(t >> 6);
  const int xcd = blockIdx.x & 7, idx = blockIdx.x >> 3;
  const int mi = idx & 7, ni = idx >> 3;
  const int bm = (xcd >> 1) * 8 + mi, bn = (xcd & 1) * 4 + ni;
#define PH3 0
  const int phase = PH3 == 0 ? 0 : (PH3 == 1 ? ((27 * mi + 54 * ni) >> 2) % NK : (PH3 == 2 ? (mi + 2 * ni) : (xcd * 6)));
  const int mt0 = bm * 4, m0 = bm * 64, n0 = bn * 80, nt0 = bn * 5;
  STAMPB(3, (char*)actP - (16u << 20));
  STAMP(0);
#pragma unroll
  for (int i = 0; i < 6; ++i) {
    int p = wave + 4 * i;
    if (p > 20) p = 20;
    int c = (p < 10 ? p : p - 10) * 64 + lane;
    const u16* src;
    if (p < 10) src = xin + (size_t)(m0 + c / 10) * DM + n0 + (c % 10) * 8;
    else if (p < 20) src = yT + (size_t)(n0 + (c >> 3)) * SEQ + m0 + (c & 7) * 8;
    else src = (lane < 32) ? (const u16*)(stats + m0) + lane * 8 : dskip + n0 + (lane < 42 ? lane - 32 : 9) * 8;
    GLDS(src, shm + p * 1024);
  }
  const char* abase = (const char*)actP + (size_t)mt0 * NK * 1024;
  const char* bbase = (const char*)w2P + (size_t)nt0 * NK * 1024;
  u32 va[4], vb[5];
#pragma unroll
  for (int i = 0; i < 4; ++i) va[i] = lane * 16 + ((wave + i) & 3) * (NK * 1024);
#pragma unroll
  for (int j = 0; j < 5; ++j) vb[j] = lane * 16 + j * (NK * 1024);
  u32x4 fa[R][4], fb[R][5];
  auto issue = [&](int s) {
    int q = (s < NST - 1 || wave < 2) ? wave + 4 * s : wave + 4 * (s - 1);
    int ks = q + phase;
    if (ks >= NK) ks -= NK;
    const char* ap = abase + ks * 1024;
    const char* bp = bbase + ks * 1024;
#pragma unroll
    for (int i = 0; i < 4; ++i) fa[s % R][i] = *(const u32x4*)(ap + va[i]);
#pragma unroll
    for (int j = 0; j < 5; ++j) fb[s % R][j] = *(const u32x4*)(bp + vb[j]);
  };
  SB0();
#pragma unroll
  for (int s = 0; s < R; ++s) { issue(s); SB0(); }
  STAMP(1);
  WAIT_V(R * 9);
  RAW_BARRIER();
  STAMP(2);
  const int fr = lane & 15, g = lane >> 4, tq = (lane & 15) >> 2, tp = lane & 3;
  const int ml = wave * 16 + fr;
  u32x2 s1p[5];
  {
    const char* sx = shm;
    const char* sy = shm + 10 * 1024;
    const char* sm = shm + 20 * 1024;
    const float2 st = *(const float2*)(sm + ml * 8);
#pragma unroll
    for (int j = 0; j < 5; ++j) {
      const int nl = j * 16;
      u32x2 xx = *(const u32x2*)(sx + ml * 160 + (nl + 4 * g) * 2);
      u32x2 dd = *(const u32x2*)(sm + 512 + (nl + 4 * g) * 2);
      s4 yy = TRREAD(sy + (nl + 4 * g + tq) * 128 + (wave * 16 + 4 * tp) * 2);
      const u32x2 yw = __builtin_bit_cast(u32x2, yy);
#pragma unroll
      for (int h = 0; h < 2; ++h) {
        const float x0 = lo_f(xx[h]), x1 = hi_f(xx[h]);
        u32 d = pk_bf(x0 - st.x, x1 - st.x);
        u32 xn = pk_bf(lo_f(d) * st.y, hi_f(d) * st.y);
        u32 tt = pk_bf(lo_f(xn) * lo_f(dd[h]), hi_f(xn) * hi_f(dd[h]));
        u32 a = pk_bf(lo_f(yw[h]) + lo_f(tt), hi_f(yw[h]) + hi_f(tt));
        s1p[j][h] = pk_bf(x0 + lo_f(a), x1 + hi_f(a));
      }
      asm volatile("" : "+v"(s1p[j]));
    }
  }
  f4 acc[4][5];
#pragma unroll
  for (int i = 0; i < 4; ++i)
#pragma unroll
    for (int j = 0; j < 5; ++j) acc[i][j] = (f4){0.f, 0.f, 0.f, 0.f};
  SB0();
  STAMP(3);
#pragma unroll
  for (int s = 0; s < NST; ++s) {
    if (s == NST - 1) {
#pragma unroll
      for (int i = 0; i < 4; ++i) asm volatile("" : "+v"(fa[s % R][i]));
#pragma unroll
      for (int j = 0; j < 5; ++j) asm volatile("" : "+v"(fb[s % R][j]));
    }
    if (s < NST - 1 || wave < 2) {
#pragma unroll
      for (int i = 0; i < 4; ++i)
#pragma unroll
        for (int j = 0; j < 5; ++j)
          acc[i][j] = __builtin_amdgcn_mfma_f32_16x16x32_bf16(__builtin_bit_cast(s8, fb[s % R][j]),
                                                              __builtin_bit_cast(s8, fa[s % R][i]), acc[i][j], 0, 0, 0);
    }
    SB0();
    if (s + R < NST) issue(s + R);
    SB0();
    if (s == 2) STAMP(4);
    if (s == 6) STAMP(5);
    if (s == 10) STAMP(6);
  }
  STAMP(7);
  char* red = shm + 21 * 1024;
#pragma unroll
  for (int i = 1; i < 4; ++i)
#pragma unroll
    for (int j = 0; j < 5; ++j)
      *(f4*)(red + ((((wave + i) & 3) * 3 + (i - 1)) * 5 + j) * 1024 + lane * 16) = acc[i][j];
  __syncthreads();
#pragma unroll
  for (int j = 0; j < 5; ++j) {
    f4 p0 = *(const f4*)(red + ((wave * 3 + 0) * 5 + j) * 1024 + lane * 16);
    f4 p1 = *(const f4*)(red + ((wave * 3 + 1) * 5 + j) * 1024 + lane * 16);
    f4 p2 = *(const f4*)(red + ((wave * 3 + 2) * 5 + j) * 1024 + lane * 16);
    f4 sum = (acc[0][j] + p0) + (p1 + p2);
    u32 m0p = pk_bf(sum[0], sum[1]), m1p = pk_bf(sum[2], sum[3]);
    u32x2 ov = {pk_bf(lo_f(s1p[j][0]) + lo_f(m0p), hi_f(s1p[j][0]) + hi_f(m0p)),
                pk_bf(lo_f(s1p[j][1]) + lo_f(m1p), hi_f(s1p[j][1]) + hi_f(m1p))};
    *(u32x2*)(out + (size_t)(m0 + ml) * DM + n0 + j * 16 + 4 * g) = ov;
  }
  STAMP(8);
}

extern "C" void kernel_launch(void* const* d_in, const int* in_sizes, int n_in,
                              void* d_out, int out_size, void* d_ws, size_t ws_size,
                              hipStream_t stream) {
  const u16* u = (const u16*)d_in[0];
  const u16* lar = (const u16*)d_in[1];
  const u16* aim = (const u16*)d_in[2];
  const u16* cre = (const u16*)d_in[3];
  const u16* cim = (const u16*)d_in[4];
  const u16* logdt = (const u16*)d_in[5];
  const u16* dskip = (const u16*)d_in[6];
  const u16* w1 = (const u16*)d_in[8];
  const u16* b1 = (const u16*)d_in[9];
  const u16* w2 = (const u16*)d_in[10];
  u16* out = (u16*)d_out;
  char* ws = (char*)d_ws;
  if (ws_size < (32u << 20)) return;
  u16* xnP = (u16*)(ws);
  u16* w1P = (u16*)(ws + (4u << 20));
  float2* stats = (float2*)(ws + (12u << 20) + 4096);
  u16* lut = (u16*)(ws + (12u << 20) + 32768);
  u16* yT = (u16*)(ws + (13u << 20));
  u16* actP = (u16*)(ws + (16u << 20));
  u16* w2P = (u16*)(ws + (24u << 20));

  k1_kernel<<<K1_LN + 2 * K1_RP + K1_LUT, 256, 0, stream>>>(u, xnP, stats, lut, w1, w1P, w2, w2P);
  k2_kernel<<<K2_G1 + K2_SSM, 256, 0, stream>>>(xnP, w1P, b1, lut, actP, lar, aim, cre, cim, logdt, yT);
  k3_kernel<<<256, 256, 0, stream>>>(actP, w2P, u, stats, yT, dskip, out);
}
```

```cpp
#include <hip/hip_runtime.h>
#include <hip/hip_bf16.h>

#define SEQ 2048
#define DM 640
#define NS 128
#define FF 1728

typedef unsigned short u16;
typedef short s4 __attribute__((ext_vector_type(4)));
typedef short s8 __attribute__((ext_vector_type(8)));
typedef _Float16 h8 __attribute__((ext_vector_type(8)));
typedef float f4 __attribute__((ext_vector_type(4)));
typedef unsigned int u32;
typedef unsigned int u32x4 __attribute__((ext_vector_type(4)));
typedef unsigned int u32x2 __attribute__((ext_vector_type(2)));

__device__ __forceinline__ float bf2f(u16 v) { return __uint_as_float(((u32)v) << 16); }
__device__ __forceinline__ u16 f2bf(float f) {
  u32 u = __float_as_uint(f);
  u32 r = (u + 0x7FFFu + ((u >> 16) & 1u)) >> 16;
  return (u16)r;
}
__device__ __forceinline__ float bfr(float f) { return bf2f(f2bf(f)); }

#define STAMPB(kid, base)
#define STAMP(k)
typedef __bf16 bf2_t __attribute__((ext_vector_type(2)));
typedef float f2_t __attribute__((ext_vector_type(2)));
__device__ __forceinline__ u32 pk_bf(float a, float b) {
  f2_t v = {a, b};
  bf2_t r = __builtin_convertvector(v, bf2_t);
  return __builtin_bit_cast(u32, r);
}
__device__ __forceinline__ float lo_f(u32 w) { return __uint_as_float(w << 16); }
__device__ __forceinline__ float hi_f(u32 w) { return __uint_as_float(w & 0xFFFF0000u); }

#define WAIT_V(n) asm volatile("s_waitcnt vmcnt(%0)" ::"n"(n) : "memory")
#define RAW_BARRIER() do { asm volatile("s_waitcnt lgkmcnt(0)" ::: "memory"); __builtin_amdgcn_s_barrier(); } while (0)
__device__ __forceinline__ void glds16(const void* gsrc, const void* lp) {
  unsigned keep;
  unsigned lds_dst = (unsigned)(size_t)(const __attribute__((address_space(3))) char*)lp;
  asm volatile("s_mov_b32 %0, m0\n\ts_mov_b32 m0, %2\n\ts_nop 0\n\tglobal_load_lds_dwordx4 %1, off\n\ts_mov_b32 m0, %0"
               : "=&s"(keep) : "v"(gsrc), "s"(lds_dst) : "memory");
}
#define GLDS(gp, lp) glds16((const void*)(gp), (const void*)(lp))

#define LUT_E0 111
#define LUT_E1 134
#define LUT_HALF ((LUT_E1 - LUT_E0) * 128)
#define LUT_N (2 * LUT_HALF)

__device__ __forceinline__ float mish_bf(float h) {
  float e = bfr(expf(-fabsf(h)));
  float l = bfr(log1pf(e));
  float sp = bfr(l + (h > 0.f ? h : 0.f));
  float th = bfr(tanhf(sp));
  return bfr(h * th);
}

#define KRLEN 2080
#define WFENCE() do { asm volatile("" ::: "memory"); __builtin_amdgcn_wave_barrier(); asm volatile("" ::: "memory"); } while (0)
__device__ __forceinline__ void cexp_p(float arl2, float w, float p, float& re, float& im) {
  float mag = __builtin_amdgcn_exp2f(arl2 * p);
  float rev = w * p;
  rev = rev - floorf(rev);
  re = mag * __builtin_amdgcn_cosf(rev);
  im = mag * __builtin_amdgcn_sinf(rev);
}
__device__ __forceinline__ void cmul(float ar, float ai, float br, float bi, float& cr, float& ci) {
  float r = fmaf(ar, br, -(ai * bi));
  float i = fmaf(ar, bi, ai * br);
  cr = r; ci = i;
}
__device__ __forceinline__ u32x2 split2(float a, float b) {
  u32 h = pk_bf(a, b);
  u32 l = pk_bf(a - lo_f(h), b - hi_f(h));
  return (u32x2){h, l};
}

#define K1_LN 256
#define K1_KG 640
#define K1_RP 135
#define PPITCH 768
#define K1_LUT 24
#define WPITCH 544
typedef _Float16 h2_t __attribute__((ext_vector_type(2)));
__device__ __forceinline__ u32 pk_h(float a, float b) {
  f2_t v = {a, b};
  h2_t r = __builtin_convertvector(v, h2_t);
  return __builtin_bit_cast(u32, r);
}
#define TRREAD(p) __builtin_amdgcn_ds_read_tr16_b64_v4i16((__attribute__((address_space(3))) s4*)(p))

__device__ __forceinline__ void repack_load(const u16* __restrict__ w, int N, int ku, int nu, char* lds, int tl) {
#pragma unroll
  for (int i = 0; i < 2; ++i) {
    int idx = tl + 256 * i, r = idx >> 3, c = idx & 7;
    u32x4 v = *(const u32x4*)(w + (size_t)(ku * 64 + r) * N + nu * 64 + c * 8);
    *(u32x4*)(lds + r * 160 + c * 16) = v;
  }
}
__device__ __forceinline__ void repack_store(u16* __restrict__ wP, int KB, int ku, int nu, const char* lds, int tl) {
  const int wl = tl >> 6, lane = tl & 63, g = lane >> 4, tq = (lane & 15) >> 2, tp = lane & 3;
#pragma unroll
  for (int kb2 = 0; kb2 < 2; ++kb2) {
    const char* p0 = lds + (kb2 * 32 + 4 * g + tq) * 160 + wl * 32 + tp * 8;
    s4 b0 = TRREAD(p0);
    s4 b1 = TRREAD(p0 + 16 * 160);
    s8 bf = {b0[0], b0[1], b0[2], b0[3], b1[0], b1[1], b1[2], b1[3]};
    *(s8*)(wP + (((size_t)(nu * 4 + wl) * KB + ku * 2 + kb2) * 64 + lane) * 8) = bf;
  }
}

__global__ __launch_bounds__(256) void k1_kernel(const u16* __restrict__ u, u16* __restrict__ xnP,
                                                 float2* __restrict__ stats, u16* __restrict__ lut,
                                                 const u16* __restrict__ w1, u16* __restrict__ w1P,
                                                 const u16* __restrict__ w2, u16* __restrict__ w2P,
                                                 u16* __restrict__ xnT,
                                                 const u16* __restrict__ lar, const u16* __restrict__ aim,
                                                 const u16* __restrict__ cre, const u16* __restrict__ cim,
                                                 const u16* __restrict__ logdt, u16* __restrict__ Pg) {
  __shared__ __attribute__((aligned(16))) char k1s[64 * WPITCH];
  const int t = threadIdx.x, lane = t & 63;
  const int wave = __builtin_amdgcn_readfirstlane(t >> 6);
  STAMPB(0, xnP);
  STAMP(0);
  if (blockIdx.x < K1_LN) {
    u16 (*tile)[DM + 8] = (u16 (*)[DM + 8])k1s;
    const int r = wave * 2 + (lane >> 5), q = lane & 31;
    const int rg = (blockIdx.x & 7) * 32 + (blockIdx.x >> 3);
    const int row = rg * 8 + r;
    float v[20];
    const u32x2* src = (const u32x2*)(u + (size_t)row * DM);
#pragma unroll
    for (int i = 0; i < 5; ++i) {
      u32x2 w = src[q + 32 * i];
      v[i * 4 + 0] = lo_f(w[0]); v[i * 4 + 1] = hi_f(w[0]);
      v[i * 4 + 2] = lo_f(w[1]); v[i * 4 + 3] = hi_f(w[1]);
    }
    float s = 0.f;
#pragma unroll
    for (int i = 0; i < 20; ++i) s += v[i];
    STAMP(1);
#pragma unroll
    for (int o = 1; o < 32; o <<= 1) s += __shfl_xor(s, o);
    const float mean = s * (1.0f / DM);
    float ss = 0.f;
#pragma unroll
    for (int i = 0; i < 20; ++i) { float d = v[i] - mean; ss += d * d; }
#pragma unroll
    for (int o = 1; o < 32; o <<= 1) ss += __shfl_xor(ss, o);
    const float var = ss * (1.0f / DM);
    const float mb = bfr(mean);
    const float vb = bfr(var);
    const float den = bfr(sqrtf(bfr(vb + 1e-5f)));
    const float rden = 1.0f / den;
    if (q == 0) stats[row] = make_float2(mb, rden);
#pragma unroll
    for (int i = 0; i < 5; ++i) {
      u32 d0 = pk_bf(v[i * 4 + 0] - mb, v[i * 4 + 1] - mb);
      u32 d1 = pk_bf(v[i * 4 + 2] - mb, v[i * 4 + 3] - mb);
      u32x2 w = {pk_bf(lo_f(d0) * rden, hi_f(d0) * rden), pk_bf(lo_f(d1) * rden, hi_f(d1) * rden)};
      *(u32x2*)&tile[r][(q + 32 * i) * 4] = w;
    }
    __syncthreads();
    STAMP(2);
    const int mt = rg >> 1, half = rg & 1;
#pragma unroll
    for (int i = 0; i < 3; ++i) {
      int idx = t + 256 * i;
      if (idx < 640) {
        int kb = idx >> 5, g = (idx >> 3) & 3, fr = idx & 7;
        u32x2 a = *(const u32x2*)&tile[fr][kb * 32 + 4 * g];
        u32x2 b = *(const u32x2*)&tile[fr][kb * 32 + 16 + 4 * g];
        u32x4 w = {a[0], a[1], b[0], b[1]};
        *(u32x4*)(xnP + (((size_t)mt * 20 + kb) * 64 + g * 16 + half * 8 + fr) * 8) = w;
      }
    }
#pragma unroll
    for (int i = 0; i < 2; ++i) {
      int idx = t + 256 * i;
      if (idx < 320) {
        u32 w[8];
#pragma unroll
        for (int rr = 0; rr < 8; ++rr) w[rr] = *(const u32*)&tile[rr][2 * idx];
        u32x4 lo = {pk_h(lo_f(w[0]), lo_f(w[1])), pk_h(lo_f(w[2]), lo_f(w[3])), pk_h(lo_f(w[4]), lo_f(w[5])), pk_h(lo_f(w[6]), lo_f(w[7]))};
        u32x4 hi = {pk_h(hi_f(w[0]), hi_f(w[1])), pk_h(hi_f(w[2]), hi_f(w[3])), pk_h(hi_f(w[4]), hi_f(w[5])), pk_h(hi_f(w[6]), hi_f(w[7]))};
        *(u32x4*)(xnT + (size_t)(2 * idx) * SEQ + rg * 8) = lo;
        *(u32x4*)(xnT + (size_t)(2 * idx + 1) * SEQ + rg * 8) = hi;
      }
    }
    STAMP(3);
    return;
  }
  if (blockIdx.x < K1_LN + K1_KG) {
    const int d = blockIdx.x - K1_LN;
    const int n = t & 127;
    char* shm = k1s;
    const u16 r_dt = logdt[d];
    const u16 r_ar = lar[d * NS + n], r_ai = aim[d * NS + n];
    const u16 r_cr = cre[d * NS + n], r_ci = cim[d * NS + n];
    const u16 r_cr2 = cre[d * NS + (n ^ 64)], r_ci2 = cim[d * NS + (n ^ 64)];
    const float dt = bfr(expf(bf2f(r_dt)));
    const float ar = -bfr(expf(bf2f(r_ar))) * dt;
    const float ai = bf2f(r_ai) * dt;
    float cr = bf2f(r_cr) * dt, ci = bf2f(r_ci) * dt;
    float bs = (fabsf(cr) + fabsf(ci)) + (fabsf(bf2f(r_cr2) * dt) + fabsf(bf2f(r_ci2) * dt));
#pragma unroll
    for (int o = 1; o < 64; o <<= 1) bs += __shfl_xor(bs, o);
    const float bound = 2.0f * bs;
    int ex = 0;
    if (bound > 0.f && bound < 3.0e38f) { (void)frexpf(bound, &ex); }
    int es = 15 - ex;
    es = es > 100 ? 100 : (es < -100 ? -100 : es);
    const float sc2 = ldexpf(2.0f, es);
    const float kscale = ldexpf(1.0f, -es);
    cr *= sc2; ci *= sc2;
    const float arl2 = ar * 1.4426950408889634f;
    const float w = ai * 0.15915494309189535f;
    STAMP(1);
    char* wr = shm + n * 4;
    float eHr, eHi;
    cexp_p(arl2, w, 128.0f, eHr, eHi);
    if (wave < 2) {
      float e8r, e8i, e4r, e4i, gr, gi;
      cexp_p(arl2, w, 8.0f, e8r, e8i);
      cexp_p(arl2, w, 4.0f, e4r, e4i);
      cexp_p(arl2, w, 512.0f, gr, gi);
      cmul(gr, gi, cr, ci, gr, gi);
      float lr = 1.f, lim = 0.f;
      *(u32*)(wr) = pk_h(1.f, 0.f);
#pragma unroll
      for (int j = 1; j < 16; ++j) {
        cmul(lr, lim, e8r, e8i, lr, lim);
        *(u32*)(wr + j * WPITCH) = pk_h(lr, -lim);
        if (j < 5) {
          float hr, hi;
          *(u32*)(wr + (40 + (j - 1) * 2) * WPITCH) = pk_h(gr, gi);
          cmul(gr, gi, e4r, e4i, hr, hi);
          *(u32*)(wr + (41 + (j - 1) * 2) * WPITCH) = pk_h(hr, hi);
          cmul(gr, gi, eHr, eHi, gr, gi);
        } else if (j < 14) {
          *(u32*)(wr + (48 + j - 5) * WPITCH) = pk_h(gr, gi);
          if (j < 13) cmul(gr, gi, eHr, eHi, gr, gi);
        }
      }
    } else {
      float Rr[8], Ri[8];
      Rr[0] = 1.f; Ri[0] = 0.f;
      cexp_p(arl2, w, 1.0f, Rr[1], Ri[1]);
#pragma unroll
      for (int r = 2; r < 8; ++r) cmul(Rr[r - 1], Ri[r - 1], Rr[1], Ri[1], Rr[r], Ri[r]);
      float gr = cr, gi = ci;
#pragma unroll
      for (int ii = 0; ii < 2; ++ii) {
        *(u32*)(wr + (16 + ii * 8) * WPITCH) = pk_h(gr, gi);
#pragma unroll
        for (int r = 1; r < 8; ++r) {
          float hr, hi;
          cmul(gr, gi, Rr[r], Ri[r], hr, hi);
          *(u32*)(wr + (16 + ii * 8 + r) * WPITCH) = pk_h(hr, hi);
        }
        cmul(gr, gi, eHr, eHi, gr, gi);
      }
#pragma unroll
      for (int ii = 2; ii < 4; ++ii) {
        *(u32*)(wr + (32 + (ii - 2) * 4) * WPITCH) = pk_h(gr, gi);
#pragma unroll
        for (int r = 1; r < 4; ++r) {
          float hr, hi;
          cmul(gr, gi, Rr[2 * r], Ri[2 * r], hr, hi);
          *(u32*)(wr + (32 + (ii - 2) * 4 + r) * WPITCH) = pk_h(hr, hi);
        }
        if (ii < 3) cmul(gr, gi, eHr, eHi, gr, gi);
      }
    }
    __syncthreads();
    STAMP(2);
    const int b = lane & 15, g = lane >> 4;
    u16* P = Pg + (size_t)d * PPITCH;
    if (wave < 3) {
      const char* ra = shm + (16 + 16 * wave + b) * WPITCH + g * 16;
      const char* rb = shm + b * WPITCH + g * 16;
      f4 ka = (f4){0.f, 0.f, 0.f, 0.f};
#pragma unroll
      for (int k8 = 0; k8 < 8; ++k8) {
        h8 a = *(const h8*)(ra + k8 * 64);
        h8 bb = *(const h8*)(rb + k8 * 64);
        ka = __builtin_amdgcn_mfma_f32_16x16x32_f16(a, bb, ka, 0, 0, 0);
      }
      const u32 k01 = pk_h(ka[0], ka[1]), k23 = pk_h(ka[2], ka[3]);
      if (wave == 0) {
        *(u32x2*)&P[(g >> 1) * 128 + (g & 1) * 4 + 8 * b] = (u32x2){k01, k23};
      } else if (wave == 1) {
        const int i1 = g < 2 ? 256 + 64 * g + 4 * b : 384 + 64 * (g - 2) + 2 * b;
        *(u32*)&P[i1] = k01;
        *(u32*)&P[i1 + (g < 2 ? 2 : 32)] = k23;
      } else {
        const int i2 = 512 + 64 * g + b;
        if (g < 3) P[i2] = (u16)k01;
        if (g < 2) {
          P[i2 + 16] = (u16)(k01 >> 16);
          P[i2 + 32] = (u16)k23; P[i2 + 48] = (u16)(k23 >> 16);
        }
      }
    } else if (lane == 0) {
      *(float*)&P[704] = kscale;
    }
    STAMP(3);
    return;
  }
  if (blockIdx.x < K1_LN + K1_KG + K1_RP) {
    const int u0 = (blockIdx.x - (K1_LN + K1_KG)) * 2;
    repack_load(w1, FF, u0 / 27, u0 % 27, k1s, t);
    repack_load(w1, FF, (u0 + 1) / 27, (u0 + 1) % 27, k1s + 10240, t);
    __syncthreads();
    repack_store(w1P, DM / 32, u0 / 27, u0 % 27, k1s, t);
    repack_store(w1P, DM / 32, (u0 + 1) / 27, (u0 + 1) % 27, k1s + 10240, t);
    STAMP(3);
    return;
  }
  if (blockIdx.x < K1_LN + K1_KG + 2 * K1_RP) {
    const int u0 = (blockIdx.x - (K1_LN + K1_KG + K1_RP)) * 2;
    repack_load(w2, DM, u0 / 10, u0 % 10, k1s, t);
    repack_load(w2, DM, (u0 + 1) / 10, (u0 + 1) % 10, k1s + 10240, t);
    __syncthreads();
    repack_store(w2P, FF / 32, u0 / 10, u0 % 10, k1s, t);
    repack_store(w2P, FF / 32, (u0 + 1) / 10, (u0 + 1) % 10, k1s + 10240, t);
    STAMP(3);
    return;
  }
  {
    int idx = (blockIdx.x - (K1_LN + K1_KG + 2 * K1_RP)) * 256 + t;
    if (idx >= 2 && idx < LUT_N + 2) {
      int sgn = idx & 1;
      int a = ((idx - 2) >> 1) + (LUT_E0 << 7);
      float h = bf2f((u16)(a | (sgn << 15)));
      lut[idx] = f2bf(mish_bf(h));
    } else if (idx < 6144) {
      lut[idx] = 0;
    }
  }
}

#define K2_G1 256
#define K2_SSM 640
#define KCPITCH 2080
#define SB0() __builtin_amdgcn_sched_barrier(0)
template <int V> struct IC { static constexpr int value = V; };
__device__ __forceinline__ void glds16s(u32 voff, const void* sbase, const void* lp) {
  unsigned keep;
  unsigned lds_dst = (unsigned)(size_t)(const __attribute__((address_space(3))) char*)lp;
  asm volatile("s_mov_b32 %0, m0\n\ts_mov_b32 m0, %3\n\ts_nop 0\n\tglobal_load_lds_dwordx4 %1, %2\n\ts_mov_b32 m0, %0"
               : "=&s"(keep) : "v"(voff), "s"(sbase), "s"(lds_dst) : "memory");
}
#define MFMA_BF(acc, a, b) \
  asm volatile("v_mfma_f32_16x16x32_bf16 %0, %1, %2, %0" : "+v"(acc) : "v"(a), "v"(b))
#define MFMA_BF0(acc, a, b) \
  asm volatile("v_mfma_f32_16x16x32_bf16 %0, %1, %2, 0" : "=&v"(acc) : "v"(a), "v"(b))
#define GLOAD4(dst, voff, sbase) \
  asm volatile("global_load_dwordx4 %0, %1, %2" : "=v"(dst) : "v"(voff), "s"(sbase) : "memory")

__global__ __launch_bounds__(256, 4) void k2_kernel(const u16* __restrict__ xnP, const u16* __restrict__ w1P,
                                                    const u16* __restrict__ b1, const u16* __restrict__ lut,
                                                    u16* __restrict__ actP,
                                                    const u16* __restrict__ xnT, const u16* __restrict__ Pg,
                                                    u16* __restrict__ yT) {
  __shared__ __attribute__((aligned(1024))) char shm[40960];
  const int t = threadIdx.x, lane = t & 63;
  const int wave = __builtin_amdgcn_readfirstlane(t >> 6);
  STAMPB(2, (char*)actP - (16u << 20));
  STAMP(0);
  if (blockIdx.x >= K2_G1) {
    const int sb = blockIdx.x - K2_G1;
    const int d = sb;
    u16* P = (u16*)(shm + 34816);
    u16* xs = (u16*)(shm + 36352);
    u16* Kc = (u16*)shm;
    if (wave == 0) glds16s(lane * 16, (const char*)(Pg + (size_t)d * PPITCH), shm + 34816);
    if (wave == 1 && lane < 32) glds16s(lane * 16, (const char*)(Pg + (size_t)d * PPITCH) + 1024, shm + 34816 + 1024);
    glds16s(lane * 16, (const char*)(xnT + (size_t)d * SEQ) + wave * 1024, shm + 36352 + 512 + wave * 1024);
    u32 tdum = 0;
    {
      const int xcd = sb & 7;
      int L = (sb >> 3) * 256 + t;
      if (L < 17 * 704) {
        const int k = 3 + L / 704, rem = L % 704, panel = rem >> 3, sub = rem & 7;
        int np = (xcd & 1) * 56 + panel - 32; np = np > 107 ? 107 : np;
        const char* ad = panel < 32 ? (const char*)xnP + ((size_t)(((xcd >> 1) * 32 + panel) * 20 + k) * 1024 + sub * 128)
                                    : (const char*)w1P + ((size_t)(np * 20 + k) * 1024 + sub * 128);
        asm volatile("global_load_dword %0, %1, off" : "=v"(tdum) : "v"(ad) : "memory");
      }
    }
    if (t < 32) *(u32x4*)&xs[t * 8] = (u32x4){0u, 0u, 0u, 0u};
    if (t < 8) *(u32x4*)&Kc[(t >> 1) * KCPITCH + (256 + (t & 1)) * 8] = (u32x4){0u, 0u, 0u, 0u};
    {
      if ((sb >> 3) * 256 + wave * 64 < 17 * 704) WAIT_V(1); else WAIT_V(0);
    }
    RAW_BARRIER();
    STAMP(3);
    const int b = lane & 15, g = lane >> 4;
    const float kscale = *(const float*)&P[704];
    {
      u32 wv[6];
      const int m = 255 - t;
      if (wave < 2) {
        const int i = 512 + m - 128;
        const u32 pc = P[i - 1], pa = P[i], pb = P[i + 1];
        const u32 aa = pa | (pa << 16);
        wv[0] = pb | (pb << 16);
        wv[1] = pb | (((m & 1) ? pb : pa) << 16);
        wv[2] = aa; wv[3] = aa; wv[4] = aa;
        wv[5] = m == 128 ? pc : pa;
      } else if (wave == 2) {
        const int i = 384 + 2 * (m - 64);
        const u32 q0 = *(const u32*)&P[i - 2], q1 = *(const u32*)&P[i], q2 = *(const u32*)&P[i + 2];
        wv[0] = __builtin_amdgcn_perm(q2, q2, 0x01000100u);
        wv[1] = __builtin_amdgcn_perm(q1, q1, 0x03020302u);
        wv[2] = __builtin_amdgcn_perm(q1, q1, 0x01000302u);
        wv[3] = __builtin_amdgcn_perm(q1, q1, 0x01000100u);
        wv[4] = m == 64 ? __builtin_amdgcn_perm(q0, q1, 0x07060100u) : wv[3];
        wv[5] = m == 64 ? q0 : (q0 >> 16);
      } else {
#pragma unroll
        for (int hh = 0; hh < 2; ++hh) {
          const int mm = m - hh;
          const int sg = mm >= 32 ? 1 : 0;
#pragma unroll
          for (int j = 0; j < (hh ? 2 : 4); ++j) {
            u32 pk = 0;
#pragma unroll
            for (int h = 0; h < 2; ++h) {
              if (hh == 0 || 2 * j + h < 3) {
                int q = 8 * mm + 7 - (2 * j + h);
                q = q < 0 ? 0 : q;
                int idx = sg ? 128 + ((q + ((q >> 1) & 1)) >> 1) : q;
                pk |= ((u32)P[idx]) << (16 * h);
              }
            }
            wv[hh * 4 + j] = (hh && mm < 0) ? 0u : pk;
          }
        }
      }
      *(u32x4*)&Kc[0 * KCPITCH + t * 8] = (u32x4){wv[0], wv[1], wv[2], wv[3]};
      *(u32x4*)&Kc[1 * KCPITCH + t * 8] = (u32x4){__builtin_amdgcn_alignbit(wv[1], wv[0], 16), __builtin_amdgcn_alignbit(wv[2], wv[1], 16),
                                                  __builtin_amdgcn_alignbit(wv[3], wv[2], 16), __builtin_amdgcn_alignbit(wv[4], wv[3], 16)};
      *(u32x4*)&Kc[2 * KCPITCH + t * 8] = (u32x4){wv[1], wv[2], wv[3], wv[4]};
      *(u32x4*)&Kc[3 * KCPITCH + t * 8] = (u32x4){__builtin_amdgcn_alignbit(wv[2], wv[1], 16), __builtin_amdgcn_alignbit(wv[3], wv[2], 16),
                                                  __builtin_amdgcn_alignbit(wv[4], wv[3], 16), __builtin_amdgcn_alignbit(wv[5], wv[4], 16)};
    }
    __syncthreads();
    STAMP(4);
    f4 acc[8];
#pragma unroll
    for (int i = 0; i < 8; ++i) acc[i] = (f4){0.f, 0.f, 0.f, 0.f};
    const int abase = ((3 - b) & 3) * KCPITCH + 2028 - 4 * (b >> 2) + 8 * g;
    const int bbase = 256 - 16 + 16 * b + 8 * g;
    const u32 kc_lds = (u32)(size_t)(const __attribute__((address_space(3))) char*)shm;
#pragma unroll
    for (int rr = 0; rr < 2; ++rr) {
      const int rp = wave * 2 + rr;
      h8 A[8], B[8];
      u32x2 a0[8], a1[8];
      const u32 aaddr = kc_lds + (u32)(abase - 32 * rp - 256 * 7) * 2;
#pragma unroll
      for (int u = 0; u < 8; ++u) {
        asm volatile("ds_read_b64 %0, %1 offset:%2" : "=v"(a0[u]) : "v"(aaddr), "n"((7 - u) * 512) : "memory");
        asm volatile("ds_read_b64 %0, %1 offset:%2" : "=v"(a1[u]) : "v"(aaddr), "n"((7 - u) * 512 + 8) : "memory");
      }
#pragma unroll
      for (int u = 0; u < 8; ++u) B[u] = *(const h8*)&xs[bbase - 32 * rp + 256 * u];
      asm volatile("s_waitcnt lgkmcnt(0)"
                   : "+v"(a0[0]), "+v"(a1[0]), "+v"(a0[1]), "+v"(a1[1]), "+v"(a0[2]), "+v"(a1[2]), "+v"(a0[3]), "+v"(a1[3]),
                     "+v"(a0[4]), "+v"(a1[4]), "+v"(a0[5]), "+v"(a1[5]), "+v"(a0[6]), "+v"(a1[6]), "+v"(a0[7]), "+v"(a1[7])
                   :: "memory");
#pragma unroll
      for (int u = 0; u < 8; ++u) A[u] = __builtin_bit_cast(h8, (u32x4){a0[u][0], a0[u][1], a1[u][0], a1[u][1]});
#pragma unroll
      for (int u = 0; u < 8; ++u)
#pragma unroll
        for (int v = 0; v + u < 8; ++v)
          acc[u + v] = __builtin_amdgcn_mfma_f32_16x16x32_f16(A[u], B[v], acc[u + v], 0, 0, 0);
    }
    __syncthreads();
    STAMP(5);
    float* red = (float*)shm;
    auto red_phase = [&](auto WC) {
      constexpr int W = decltype(WC)::value;
#pragma unroll
      for (int o = 1; o < 4; ++o) {
        constexpr int dummy = 0; (void)dummy;
        const int ow = (W + o) & 3;
#pragma unroll
        for (int i = 0; i < 2; ++i) *(f4*)&red[(((ow * 3 + (o - 1)) * 2 + i) * 64 + lane) * 4] = acc[ow * 2 + i];
      }
      __syncthreads();
#pragma unroll
      for (int i = 0; i < 2; ++i) {
        f4 s1 = *(const f4*)&red[(((W * 3 + 0) * 2 + i) * 64 + lane) * 4];
        f4 s2 = *(const f4*)&red[(((W * 3 + 1) * 2 + i) * 64 + lane) * 4];
        f4 s3 = *(const f4*)&red[(((W * 3 + 2) * 2 + i) * 64 + lane) * 4];
        f4 sum = ((acc[W * 2 + i] + s1) + (s2 + s3)) * kscale;
        u32x2 o = {pk_bf(sum[0], sum[1]), pk_bf(sum[2], sum[3])};
        *(u32x2*)(yT + (size_t)d * SEQ + 256 * (W * 2 + i) + 16 * b + 4 * g) = o;
      }
    };
    if (wave == 0) red_phase(IC<0>{});
    else if (wave == 1) red_phase(IC<1>{});
    else if (wave == 2) red_phase(IC<2>{});
    else red_phase(IC<3>{});
    asm volatile("s_waitcnt vmcnt(0)" :: "v"(tdum) : "memory");
    STAMP(8);
    return;
  }
#define ROT 0
#define KROT(s) (((s) + ROT) % 20)
  constexpr int ND = 5;
  constexpr int STG = 7 * 1024;
  constexpr int NKT = DM / 32;
  const int xcd = blockIdx.x & 7, idx = blockIdx.x >> 3;
  const int mi = idx & 3, ni = idx >> 2;
  const int bm = (xcd >> 1) * 4 + mi, bn = (xcd & 1) * 8 + ni;
  const int mt0 = bm * 8, nt0 = bn * 7;
  const char* abase = (const char*)xnP + (size_t)(mt0 + 2 * wave) * (20 * 1024);
  const u32 va0 = lane * 16, va1 = lane * 16 + 20 * 1024;
  const int p0 = 2 * wave, p1 = wave < 3 ? 2 * wave + 1 : 6;
  const u32 vb0 = lane * 16 + (u32)(nt0 + p0 > 107 ? 107 : nt0 + p0) * (20 * 1024);
  const u32 vb1 = lane * 16 + (u32)(nt0 + p1 > 107 ? 107 : nt0 + p1) * (20 * 1024);
  auto stage = [&](int s) {
    char* sb = shm + (s % ND) * STG;
    const char* bp = (const char*)w1P + KROT(s) * 1024;
    glds16s(vb0, bp, sb + p0 * 1024);
    if (wave < 3) glds16s(vb1, bp, sb + p1 * 1024);
  };
  u32x4 fa[3][2];
  auto loadA = [&](int s) {
    const char* ap = abase + KROT(s) * 1024;
    GLOAD4(fa[s % 3][0], va0, ap);
    GLOAD4(fa[s % 3][1], va1, ap);
  };
  f4 acc[2][7];
  s8 fb[7];
  stage(0); stage(1); loadA(0); stage(2); loadA(1); stage(3); loadA(2);
  STAMP(1);
  if (wave < 3) WAIT_V(12); else WAIT_V(9);
  __builtin_amdgcn_s_barrier();
  STAMP(2);
#pragma unroll
  for (int j = 0; j < 7; ++j) fb[j] = *(const s8*)(shm + j * 1024 + lane * 16);
#pragma unroll
  for (int kt = 0; kt < NKT; ++kt) {
    {
      constexpr int dummy = 0; (void)dummy;
      const int g2 = ((kt - 2 <= 17) ? 1 : 0) + ((kt - 1 <= 17) ? 1 : 0);
      const int a2 = ((kt - 2 <= 16) ? 2 : 0) + ((kt - 1 <= 16) ? 2 : 0);
      if (wave < 3) { switch (2 * g2 + a2) { case 8: WAIT_V(8); break; case 6: WAIT_V(6); break; default: WAIT_V(2); break; } }
      else { switch (g2 + a2) { case 6: WAIT_V(6); break; case 4: WAIT_V(4); break; default: WAIT_V(1); break; } }
      asm volatile("" : "+v"(fa[kt % 3][0]), "+v"(fa[kt % 3][1]));
    }
    __builtin_amdgcn_s_barrier();
    if (kt + ND - 1 < NKT) stage(kt + ND - 1);
    else if (kt == NKT - ND + 1) {
      glds16s(lane * 16, (const char*)lut + p0 * 1024, shm + p0 * 1024);
      if (wave < 3) glds16s(lane * 16, (const char*)lut + p1 * 1024, shm + p1 * 1024);
    } else if (kt == NKT - ND + 2) {
      int o = nt0 * 16 + (lane < 14 ? lane : 13) * 8; if (o > FF - 8) o = FF - 8;
      if (wave < 3) glds16s(lane * 16, (const char*)lut + (7 + p0) * 1024, shm + (7 + p0) * 1024);
      if (wave < 2) glds16s(lane * 16, (const char*)lut + (7 + p1) * 1024, shm + (7 + p1) * 1024);
      else glds16s(o * 2, (const char*)b1, shm + 12 * 1024);
    }
    SB0();
#pragma unroll
    for (int j = 0; j < 7; ++j) {
      if (kt == 0) {
        MFMA_BF0(acc[0][j], fb[j], fa[0][0]);
        MFMA_BF0(acc[1][j], fb[j], fa[0][1]);
      } else {
        MFMA_BF(acc[0][j], fb[j], fa[kt % 3][0]);
        MFMA_BF(acc[1][j], fb[j], fa[kt % 3][1]);
      }
      if (kt + 1 < NKT) fb[j] = *(const s8*)(shm + ((kt + 1) % ND) * STG + j * 1024 + lane * 16);
      SB0();
    }
    if (kt + 3 < NKT) loadA(kt + 3);
    SB0();
    if (kt == 3) STAMP(3);
    if (kt == 7) STAMP(4);
    if (kt == 11) STAMP(5);
    if (kt == 13) STAMP(6);
    if (kt == 15) STAMP(9);
    if (kt == 17) STAMP(10);
  }
  asm volatile("s_nop 7\n\ts_nop 7\n\ts_nop 7" ::: "memory");
  WAIT_V(0);
  __builtin_amdgcn_s_barrier();
  STAMP(7);
  const int g = lane >> 4;
  const char* slut = shm;
  const u16* sbias = (const u16*)(shm + 12 * 1024);
  typedef unsigned short us2 __attribute__((ext_vector_type(2)));
  u32 hw[28];
  us2 res[28];
  us2 mx = {0, 0}, mn = {0xFFFF, 0xFFFF};
#pragma unroll
  for (int j = 0; j < 7; ++j) {
    u32x2 bb = *(const u32x2*)(sbias + j * 16 + 4 * g);
    const f2_t bv0 = {lo_f(bb[0]), hi_f(bb[0])}, bv1 = {lo_f(bb[1]), hi_f(bb[1])};
#pragma unroll
    for (int i = 0; i < 2; ++i) {
      u32 t0 = pk_bf(acc[i][j][0], acc[i][j][1]);
      u32 t1 = pk_bf(acc[i][j][2], acc[i][j][3]);
      f2_t s0 = (f2_t){lo_f(t0), hi_f(t0)} + bv0;
      f2_t s1 = (f2_t){lo_f(t1), hi_f(t1)} + bv1;
      hw[(j * 2 + i) * 2 + 0] = pk_bf(s0[0], s0[1]);
      hw[(j * 2 + i) * 2 + 1] = pk_bf(s1[0], s1[1]);
#pragma unroll
      for (int h = 0; h < 2; ++h) {
        const u32 w = hw[(j * 2 + i) * 2 + h];
        const u32 r = ((w << 1) & 0xFFFEFFFEu) | ((w >> 15) & 0x00010001u);
        const us2 r2 = __builtin_bit_cast(us2, r);
        mx = __builtin_elementwise_max(mx, r2);
        mn = __builtin_elementwise_min(mn, (us2)(r2 - (us2){2, 2}));
        us2 ix = __builtin_elementwise_sub_sat(r2, (us2){(LUT_E0 << 8) - 2, (LUT_E0 << 8) - 2});
        ix = __builtin_elementwise_min(ix, (us2){6143, 6143});
        res[(j * 2 + i) * 2 + h][0] = *(const u16*)(slut + (u32)ix[0] * 2u);
        res[(j * 2 + i) * 2 + h][1] = *(const u16*)(slut + (u32)ix[1] * 2u);
      }
    }
  }
  const bool oor = (mx[0] >= (LUT_E1 << 8)) | (mx[1] >= (LUT_E1 << 8)) | (mn[0] < (LUT_E0 << 8) - 2) | (mn[1] < (LUT_E0 << 8) - 2);
  if (__builtin_amdgcn_ballot_w64(oor) != 0) {
#pragma unroll 1
    for (int jh = 0; jh < 28; ++jh) {
      u32 hv = 0, rv = 0;
#pragma unroll
      for (int q = 0; q < 28; ++q) if (q == jh) { hv = hw[q]; rv = __builtin_bit_cast(u32, res[q]); }
      u32 nv = 0;
#pragma unroll
      for (int e = 0; e < 2; ++e) {
        const u32 hb = (hv >> (16 * e)) & 0xFFFFu;
        const u32 a = hb & 0x7FFFu;
        const bool in = (a - (LUT_E0 << 7)) < (u32)LUT_HALF;
        const float hf = __uint_as_float(hb << 16);
        const float fbv = (a < (LUT_E0 << 7)) ? hf * 0.59765625f : (hf > 0.f ? hf : hf * 0.0f);
        const u32 fbb = pk_bf(fbv, fbv) & 0xFFFFu;
        nv |= (in ? ((rv >> (16 * e)) & 0xFFFFu) : fbb) << (16 * e);
      }
#pragma unroll
      for (int q = 0; q < 28; ++q) if (q == jh) res[q] = __builtin_bit_cast(us2, nv);
    }
  }
#pragma unroll
  for (int j = 0; j < 7; ++j) {
    const int nt = nt0 + j;
    if (nt < 108) {
#pragma unroll
      for (int i = 0; i < 2; ++i) {
        const int mt = mt0 + wave * 2 + i;
        u32x2 ov = {__builtin_bit_cast(u32, res[(j * 2 + i) * 2]), __builtin_bit_cast(u32, res[(j * 2 + i) * 2 + 1])};
        *(u32x2*)(actP + (((size_t)mt * 54 + (nt >> 1)) * 64 + lane) * 8 + (nt & 1) * 4) = ov;
      }
    }
  }
  STAMP(8);
}
__global__ __launch_bounds__(256, 1) void k3_kernel(const u16* __restrict__ actP, const u16* __restrict__ w2P,
                                                    const u16* __restrict__ xin, const float2* __restrict__ stats,
                                                    const u16* __restrict__ yT, const u16* __restrict__ dskip,
                                                    u16* __restrict__ out) {
  constexpr int R = 5;
  constexpr int NK = FF / 32;
  constexpr int NST = 14;
  __shared__ __attribute__((aligned(1024))) char shm[21 * 1024 + 60 * 1024];
  const int t = threadIdx.x, lane = t & 63;
  const int wave = __builtin_amdgcn_readfirstlane(t >> 6);
  const int xcd = blockIdx.x & 7, idx = blockIdx.x >> 3;
  const int mi = idx & 7, ni = idx >> 3;
  const int bm = (xcd >> 1) * 8 + mi, bn = (xcd & 1) * 4 + ni;
#define PH3 0
  const int phase = PH3 == 0 ? 0 : (PH3 == 1 ? ((27 * mi + 54 * ni) >> 2) % NK : (PH3 == 2 ? (mi + 2 * ni) : (xcd * 6)));
  const int mt0 = bm * 4, m0 = bm * 64, n0 = bn * 80, nt0 = bn * 5;
  STAMPB(3, (char*)actP - (16u << 20));
  STAMP(0);
#pragma unroll
  for (int i = 0; i < 6; ++i) {
    int p = wave + 4 * i;
    if (p > 20) p = 20;
    int c = (p < 10 ? p : p - 10) * 64 + lane;
    const u16* src;
    if (p < 10) src = xin + (size_t)(m0 + c / 10) * DM + n0 + (c % 10) * 8;
    else if (p < 20) src = yT + (size_t)(n0 + (c >> 3)) * SEQ + m0 + (c & 7) * 8;
    else src = (lane < 32) ? (const u16*)(stats + m0) + lane * 8 : dskip + n0 + (lane < 42 ? lane - 32 : 9) * 8;
    GLDS(src, shm + p * 1024);
  }
  const char* abase = (const char*)actP + (size_t)mt0 * NK * 1024;
  const char* bbase = (const char*)w2P + (size_t)nt0 * NK * 1024;
  u32 va[4], vb[5];
#pragma unroll
  for (int i = 0; i < 4; ++i) va[i] = lane * 16 + ((wave + i) & 3) * (NK * 1024);
#pragma unroll
  for (int j = 0; j < 5; ++j) vb[j] = lane * 16 + j * (NK * 1024);
  u32x4 fa[R][4], fb[R][5];
  auto issue = [&](int s) {
    int q = (s < NST - 1 || wave < 2) ? wave + 4 * s : wave + 4 * (s - 1);
    int ks = q + phase;
    if (ks >= NK) ks -= NK;
    const char* ap = abase + ks * 1024;
    const char* bp = bbase + ks * 1024;
#pragma unroll
    for (int i = 0; i < 4; ++i) fa[s % R][i] = *(const u32x4*)(ap + va[i]);
#pragma unroll
    for (int j = 0; j < 5; ++j) fb[s % R][j] = *(const u32x4*)(bp + vb[j]);
  };
  SB0();
#pragma unroll
  for (int s = 0; s < R; ++s) { issue(s); SB0(); }
  STAMP(1);
  WAIT_V(R * 9);
  RAW_BARRIER();
  STAMP(2);
  const int fr = lane & 15, g = lane >> 4, tq = (lane & 15) >> 2, tp = lane & 3;
  const int ml = wave * 16 + fr;
  u32x2 s1p[5];
  {
    const char* sx = shm;
    const char* sy = shm + 10 * 1024;
    const char* sm = shm + 20 * 1024;
    const float2 st = *(const float2*)(sm + ml * 8);
#pragma unroll
    for (int j = 0; j < 5; ++j) {
      const int nl = j * 16;
      u32x2 xx = *(const u32x2*)(sx + ml * 160 + (nl + 4 * g) * 2);
      u32x2 dd = *(const u32x2*)(sm + 512 + (nl + 4 * g) * 2);
      s4 yy = TRREAD(sy + (nl + 4 * g + tq) * 128 + (wave * 16 + 4 * tp) * 2);
      const u32x2 yw = __builtin_bit_cast(u32x2, yy);
#pragma unroll
      for (int h = 0; h < 2; ++h) {
        const float x0 = lo_f(xx[h]), x1 = hi_f(xx[h]);
        u32 d = pk_bf(x0 - st.x, x1 - st.x);
        u32 xn = pk_bf(lo_f(d) * st.y, hi_f(d) * st.y);
        u32 tt = pk_bf(lo_f(xn) * lo_f(dd[h]), hi_f(xn) * hi_f(dd[h]));
        u32 a = pk_bf(lo_f(yw[h]) + lo_f(tt), hi_f(yw[h]) + hi_f(tt));
        s1p[j][h] = pk_bf(x0 + lo_f(a), x1 + hi_f(a));
      }
      asm volatile("" : "+v"(s1p[j]));
    }
  }
  f4 acc[4][5];
#pragma unroll
  for (int i = 0; i < 4; ++i)
#pragma unroll
    for (int j = 0; j < 5; ++j) acc[i][j] = (f4){0.f, 0.f, 0.f, 0.f};
  SB0();
  STAMP(3);
#pragma unroll
  for (int s = 0; s < NST; ++s) {
    if (s == NST - 1) {
#pragma unroll
      for (int i = 0; i < 4; ++i) asm volatile("" : "+v"(fa[s % R][i]));
#pragma unroll
      for (int j = 0; j < 5; ++j) asm volatile("" : "+v"(fb[s % R][j]));
    }
    if (s < NST - 1 || wave < 2) {
#pragma unroll
      for (int i = 0; i < 4; ++i)
#pragma unroll
        for (int j = 0; j < 5; ++j)
          acc[i][j] = __builtin_amdgcn_mfma_f32_16x16x32_bf16(__builtin_bit_cast(s8, fb[s % R][j]),
                                                              __builtin_bit_cast(s8, fa[s % R][i]), acc[i][j], 0, 0, 0);
    }
    SB0();
    if (s + R < NST) issue(s + R);
    SB0();
    if (s == 2) STAMP(4);
    if (s == 6) STAMP(5);
    if (s == 10) STAMP(6);
  }
  STAMP(7);
  char* red = shm + 21 * 1024;
#pragma unroll
  for (int i = 1; i < 4; ++i)
#pragma unroll
    for (int j = 0; j < 5; ++j)
      *(f4*)(red + ((((wave + i) & 3) * 3 + (i - 1)) * 5 + j) * 1024 + lane * 16) = acc[i][j];
  __syncthreads();
#pragma unroll
  for (int j = 0; j < 5; ++j) {
    f4 p0 = *(const f4*)(red + ((wave * 3 + 0) * 5 + j) * 1024 + lane * 16);
    f4 p1 = *(const f4*)(red + ((wave * 3 + 1) * 5 + j) * 1024 + lane * 16);
    f4 p2 = *(const f4*)(red + ((wave * 3 + 2) * 5 + j) * 1024 + lane * 16);
    f4 sum = (acc[0][j] + p0) + (p1 + p2);
    u32 m0p = pk_bf(sum[0], sum[1]), m1p = pk_bf(sum[2], sum[3]);
    u32x2 ov = {pk_bf(lo_f(s1p[j][0]) + lo_f(m0p), hi_f(s1p[j][0]) + hi_f(m0p)),
                pk_bf(lo_f(s1p[j][1]) + lo_f(m1p), hi_f(s1p[j][1]) + hi_f(m1p))};
    *(u32x2*)(out + (size_t)(m0 + ml) * DM + n0 + j * 16 + 4 * g) = ov;
  }
  STAMP(8);
}

extern "C" void kernel_launch(void* const* d_in, const int* in_sizes, int n_in,
                              void* d_out, int out_size, void* d_ws, size_t ws_size,
                              hipStream_t stream) {
  const u16* u = (const u16*)d_in[0];
  const u16* lar = (const u16*)d_in[1];
  const u16* aim = (const u16*)d_in[2];
  const u16* cre = (const u16*)d_in[3];
  const u16* cim = (const u16*)d_in[4];
  const u16* logdt = (const u16*)d_in[5];
  const u16* dskip = (const u16*)d_in[6];
  const u16* w1 = (const u16*)d_in[8];
  const u16* b1 = (const u16*)d_in[9];
  const u16* w2 = (const u16*)d_in[10];
  u16* out = (u16*)d_out;
  char* ws = (char*)d_ws;
  if (ws_size < (32u << 20)) return;
  u16* xnP = (u16*)(ws);
  u16* w1P = (u16*)(ws + (4u << 20));
  float2* stats = (float2*)(ws + (12u << 20) + 4096);
  u16* lut = (u16*)(ws + (12u << 20) + 32768);
  u16* yT = (u16*)(ws + (13u << 20));
  u16* actP = (u16*)(ws + (16u << 20));
  u16* w2P = (u16*)(ws + (24u << 20));
  u16* xnT = (u16*)(ws + (8u << 20));
  u16* Pg = (u16*)(ws + (28u << 20));

  k1_kernel<<<K1_LN + K1_KG + 2 * K1_RP + K1_LUT, 256, 0, stream>>>(u, xnP, stats, lut, w1, w1P, w2, w2P, xnT, lar, aim, cre, cim, logdt, Pg);
  k2_kernel<<<K2_G1 + K2_SSM, 256, 0, stream>>>(xnP, w1P, b1, lut, actP, xnT, Pg, yT);
  k3_kernel<<<256, 256, 0, stream>>>(actP, w2P, u, stats, yT, dskip, out);
}
```

```cpp
#include <hip/hip_runtime.h>
#include <hip/hip_bf16.h>

#define SEQ 2048
#define DM 640
#define NS 128
#define FF 1728

typedef unsigned short u16;
typedef short s4 __attribute__((ext_vector_type(4)));
typedef short s8 __attribute__((ext_vector_type(8)));
typedef _Float16 h8 __attribute__((ext_vector_type(8)));
typedef float f4 __attribute__((ext_vector_type(4)));
typedef unsigned int u32;
typedef unsigned int u32x4 __attribute__((ext_vector_type(4)));
typedef unsigned int u32x2 __attribute__((ext_vector_type(2)));

__device__ __forceinline__ float bf2f(u16 v) { return __uint_as_float(((u32)v) << 16); }
__device__ __forceinline__ u16 f2bf(float f) {
  u32 u = __float_as_uint(f);
  u32 r = (u + 0x7FFFu + ((u >> 16) & 1u)) >> 16;
  return (u16)r;
}
__device__ __forceinline__ float bfr(float f) { return bf2f(f2bf(f)); }

#define STAMPB(kid, base)
#define STAMP(k)
typedef __bf16 bf2_t __attribute__((ext_vector_type(2)));
typedef float f2_t __attribute__((ext_vector_type(2)));
__device__ __forceinline__ u32 pk_bf(float a, float b) {
  f2_t v = {a, b};
  bf2_t r = __builtin_convertvector(v, bf2_t);
  return __builtin_bit_cast(u32, r);
}
__device__ __forceinline__ float lo_f(u32 w) { return __uint_as_float(w << 16); }
__device__ __forceinline__ float hi_f(u32 w) { return __uint_as_float(w & 0xFFFF0000u); }

#define WAIT_V(n) asm volatile("s_waitcnt vmcnt(%0)" ::"n"(n) : "memory")
#define RAW_BARRIER() do { asm volatile("s_waitcnt lgkmcnt(0)" ::: "memory"); __builtin_amdgcn_s_barrier(); } while (0)
__device__ __forceinline__ void glds16(const void* gsrc, const void* lp) {
  unsigned keep;
  unsigned lds_dst = (unsigned)(size_t)(const __attribute__((address_space(3))) char*)lp;
  asm volatile("s_mov_b32 %0, m0\n\ts_mov_b32 m0, %2\n\ts_nop 0\n\tglobal_load_lds_dwordx4 %1, off\n\ts_mov_b32 m0, %0"
               : "=&s"(keep) : "v"(gsrc), "s"(lds_dst) : "memory");
}
#define GLDS(gp, lp) glds16((const void*)(gp), (const void*)(lp))

#define LUT_E0 111
#define LUT_E1 134
#define LUT_HALF ((LUT_E1 - LUT_E0) * 128)
#define LUT_N (2 * LUT_HALF)

__device__ __forceinline__ float mish_bf(float h) {
  float e = bfr(expf(-fabsf(h)));
  float l = bfr(log1pf(e));
  float sp = bfr(l + (h > 0.f ? h : 0.f));
  float th = bfr(tanhf(sp));
  return bfr(h * th);
}

#define KRLEN 2080
#define WFENCE() do { asm volatile("" ::: "memory"); __builtin_amdgcn_wave_barrier(); asm volatile("" ::: "memory"); } while (0)
__device__ __forceinline__ void cexp_p(float arl2, float w, float p, float& re, float& im) {
  float mag = __builtin_amdgcn_exp2f(arl2 * p);
  float rev = w * p;
  rev = rev - floorf(rev);
  re = mag * __builtin_amdgcn_cosf(rev);
  im = mag * __builtin_amdgcn_sinf(rev);
}
__device__ __forceinline__ void cmul(float ar, float ai, float br, float bi, float& cr, float& ci) {
  float r = fmaf(ar, br, -(ai * bi));
  float i = fmaf(ar, bi, ai * br);
  cr = r; ci = i;
}
__device__ __forceinline__ u32x2 split2(float a, float b) {
  u32 h = pk_bf(a, b);
  u32 l = pk_bf(a - lo_f(h), b - hi_f(h));
  return (u32x2){h, l};
}

#define K1_LN 256
#define K1_KG 640
#define K1_RP 135
#define PPITCH 768
#define K1_LUT 24
#define WPITCH 544
typedef _Float16 h2_t __attribute__((ext_vector_type(2)));
__device__ __forceinline__ u32 pk_h(float a, float b) {
  f2_t v = {a, b};
  h2_t r = __builtin_convertvector(v, h2_t);
  return __builtin_bit_cast(u32, r);
}
#define TRREAD(p) __builtin_amdgcn_ds_read_tr16_b64_v4i16((__attribute__((address_space(3))) s4*)(p))

__device__ __forceinline__ void repack_load(const u16* __restrict__ w, int N, int ku, int nu, char* lds, int tl) {
#pragma unroll
  for (int i = 0; i < 2; ++i) {
    int idx = tl + 256 * i, r = idx >> 3, c = idx & 7;
    u32x4 v = *(const u32x4*)(w + (size_t)(ku * 64 + r) * N + nu * 64 + c * 8);
    *(u32x4*)(lds + r * 160 + c * 16) = v;
  }
}
__device__ __forceinline__ void repack_store(u16* __restrict__ wP, int KB, int ku, int nu, const char* lds, int tl) {
  const int wl = tl >> 6, lane = tl & 63, g = lane >> 4, tq = (lane & 15) >> 2, tp = lane & 3;
#pragma unroll
  for (int kb2 = 0; kb2 < 2; ++kb2) {
    const char* p0 = lds + (kb2 * 32 + 4 * g + tq) * 160 + wl * 32 + tp * 8;
    s4 b0 = TRREAD(p0);
    s4 b1 = TRREAD(p0 + 16 * 160);
    s8 bf = {b0[0], b0[1], b0[2], b0[3], b1[0], b1[1], b1[2], b1[3]};
    *(s8*)(wP + (((size_t)(nu * 4 + wl) * KB + ku * 2 + kb2) * 64 + lane) * 8) = bf;
  }
}

__global__ __launch_bounds__(256) void k1_kernel(const u16* __restrict__ u, u16* __restrict__ xnP,
                                                 float2* __restrict__ stats, u16* __restrict__ lut,
                                                 const u16* __restrict__ w1, u16* __restrict__ w1P,
                                                 const u16* __restrict__ w2, u16* __restrict__ w2P,
                                                 u16* __restrict__ xnT,
                                                 const u16* __restrict__ lar, const u16* __restrict__ aim,
                                                 const u16* __restrict__ cre, const u16* __restrict__ cim,
                                                 const u16* __restrict__ logdt, u16* __restrict__ Pg) {
  __shared__ __attribute__((aligned(16))) char k1s[57 * WPITCH];
  const int t = threadIdx.x, lane = t & 63;
  const int wave = __builtin_amdgcn_readfirstlane(t >> 6);
  STAMPB(0, xnP);
  STAMP(0);
  if (blockIdx.x < K1_LN) {
    u16 (*tile)[DM + 8] = (u16 (*)[DM + 8])k1s;
    const int r = wave * 2 + (lane >> 5), q = lane & 31;
    const int rg = (blockIdx.x & 7) * 32 + (blockIdx.x >> 3);
    const int row = rg * 8 + r;
    float v[20];
    const u32x2* src = (const u32x2*)(u + (size_t)row * DM);
#pragma unroll
    for (int i = 0; i < 5; ++i) {
      u32x2 w = src[q + 32 * i];
      v[i * 4 + 0] = lo_f(w[0]); v[i * 4 + 1] = hi_f(w[0]);
      v[i * 4 + 2] = lo_f(w[1]); v[i * 4 + 3] = hi_f(w[1]);
    }
    float s = 0.f;
#pragma unroll
    for (int i = 0; i < 20; ++i) s += v[i];
    STAMP(1);
#pragma unroll
    for (int o = 1; o < 32; o <<= 1) s += __shfl_xor(s, o);
    const float mean = s * (1.0f / DM);
    float ss = 0.f;
#pragma unroll
    for (int i = 0; i < 20; ++i) { float d = v[i] - mean; ss += d * d; }
#pragma unroll
    for (int o = 1; o < 32; o <<= 1) ss += __shfl_xor(ss, o);
    const float var = ss * (1.0f / DM);
    const float mb = bfr(mean);
    const float vb = bfr(var);
    const float den = bfr(sqrtf(bfr(vb + 1e-5f)));
    const float rden = 1.0f / den;
    if (q == 0) stats[row] = make_float2(mb, rden);
#pragma unroll
    for (int i = 0; i < 5; ++i) {
      u32 d0 = pk_bf(v[i * 4 + 0] - mb, v[i * 4 + 1] - mb);
      u32 d1 = pk_bf(v[i * 4 + 2] - mb, v[i * 4 + 3] - mb);
      u32x2 w = {pk_bf(lo_f(d0) * rden, hi_f(d0) * rden), pk_bf(lo_f(d1) * rden, hi_f(d1) * rden)};
      *(u32x2*)&tile[r][(q + 32 * i) * 4] = w;
    }
    __syncthreads();
    STAMP(2);
    const int mt = rg >> 1, half = rg & 1;
#pragma unroll
    for (int i = 0; i < 3; ++i) {
      int idx = t + 256 * i;
      if (idx < 640) {
        int kb = idx >> 5, g = (idx >> 3) & 3, fr = idx & 7;
        u32x2 a = *(const u32x2*)&tile[fr][kb * 32 + 4 * g];
        u32x2 b = *(const u32x2*)&tile[fr][kb * 32 + 16 + 4 * g];
        u32x4 w = {a[0], a[1], b[0], b[1]};
        *(u32x4*)(xnP + (((size_t)mt * 20 + kb) * 64 + g * 16 + half * 8 + fr) * 8) = w;
      }
    }
#pragma unroll
    for (int i = 0; i < 2; ++i) {
      int idx = t + 256 * i;
      if (idx < 320) {
        u32 w[8];
#pragma unroll
        for (int rr = 0; rr < 8; ++rr) w[rr] = *(const u32*)&tile[rr][2 * idx];
        u32x4 lo = {pk_h(lo_f(w[0]), lo_f(w[1])), pk_h(lo_f(w[2]), lo_f(w[3])), pk_h(lo_f(w[4]), lo_f(w[5])), pk_h(lo_f(w[6]), lo_f(w[7]))};
        u32x4 hi = {pk_h(hi_f(w[0]), hi_f(w[1])), pk_h(hi_f(w[2]), hi_f(w[3])), pk_h(hi_f(w[4]), hi_f(w[5])), pk_h(hi_f(w[6]), hi_f(w[7]))};
        *(u32x4*)(xnT + (size_t)(2 * idx) * SEQ + rg * 8) = lo;
        *(u32x4*)(xnT + (size_t)(2 * idx + 1) * SEQ + rg * 8) = hi;
      }
    }
    STAMP(3);
    return;
  }
  if (blockIdx.x < K1_LN + K1_KG) {
    const int d = blockIdx.x - K1_LN;
    const int n = t & 127;
    char* shm = k1s;
    const u16 r_dt = logdt[d];
    const u16 r_ar = lar[d * NS + n], r_ai = aim[d * NS + n];
    const u16 r_cr = cre[d * NS + n], r_ci = cim[d * NS + n];
    const u16 r_cr2 = cre[d * NS + (n ^ 64)], r_ci2 = cim[d * NS + (n ^ 64)];
    const float dt = bfr(expf(bf2f(r_dt)));
    const float ar = -bfr(expf(bf2f(r_ar))) * dt;
    const float ai = bf2f(r_ai) * dt;
    float cr = bf2f(r_cr) * dt, ci = bf2f(r_ci) * dt;
    float bs = (fabsf(cr) + fabsf(ci)) + (fabsf(bf2f(r_cr2) * dt) + fabsf(bf2f(r_ci2) * dt));
#pragma unroll
    for (int o = 1; o < 64; o <<= 1) bs += __shfl_xor(bs, o);
    const float bound = 2.0f * bs;
    int ex = 0;
    if (bound > 0.f && bound < 3.0e38f) { (void)frexpf(bound, &ex); }
    int es = 15 - ex;
    es = es > 100 ? 100 : (es < -100 ? -100 : es);
    const float sc2 = ldexpf(2.0f, es);
    const float kscale = ldexpf(1.0f, -es);
    cr *= sc2; ci *= sc2;
    const float arl2 = ar * 1.4426950408889634f;
    const float w = ai * 0.15915494309189535f;
    STAMP(1);
    char* wr = shm + n * 4;
    float eHr, eHi;
    cexp_p(arl2, w, 128.0f, eHr, eHi);
    if (wave < 2) {
      float e8r, e8i, e4r, e4i, gr, gi;
      cexp_p(arl2, w, 8.0f, e8r, e8i);
      cexp_p(arl2, w, 4.0f, e4r, e4i);
      cexp_p(arl2, w, 512.0f, gr, gi);
      cmul(gr, gi, cr, ci, gr, gi);
      float lr = 1.f, lim = 0.f;
      *(u32*)(wr) = pk_h(1.f, 0.f);
#pragma unroll
      for (int j = 1; j < 16; ++j) {
        cmul(lr, lim, e8r, e8i, lr, lim);
        *(u32*)(wr + j * WPITCH) = pk_h(lr, -lim);
        if (j < 5) {
          float hr, hi;
          *(u32*)(wr + (40 + (j - 1) * 2) * WPITCH) = pk_h(gr, gi);
          cmul(gr, gi, e4r, e4i, hr, hi);
          *(u32*)(wr + (41 + (j - 1) * 2) * WPITCH) = pk_h(hr, hi);
          cmul(gr, gi, eHr, eHi, gr, gi);
        } else if (j < 14) {
          *(u32*)(wr + (48 + j - 5) * WPITCH) = pk_h(gr, gi);
          if (j < 13) cmul(gr, gi, eHr, eHi, gr, gi);
        }
      }
    } else {
      float Rr[8], Ri[8];
      Rr[0] = 1.f; Ri[0] = 0.f;
      cexp_p(arl2, w, 1.0f, Rr[1], Ri[1]);
#pragma unroll
      for (int r = 2; r < 8; ++r) cmul(Rr[r - 1], Ri[r - 1], Rr[1], Ri[1], Rr[r], Ri[r]);
      float gr = cr, gi = ci;
#pragma unroll
      for (int ii = 0; ii < 2; ++ii) {
        *(u32*)(wr + (16 + ii * 8) * WPITCH) = pk_h(gr, gi);
#pragma unroll
        for (int r = 1; r < 8; ++r) {
          float hr, hi;
          cmul(gr, gi, Rr[r], Ri[r], hr, hi);
          *(u32*)(wr + (16 + ii * 8 + r) * WPITCH) = pk_h(hr, hi);
        }
        cmul(gr, gi, eHr, eHi, gr, gi);
      }
#pragma unroll
      for (int ii = 2; ii < 4; ++ii) {
        *(u32*)(wr + (32 + (ii - 2) * 4) * WPITCH) = pk_h(gr, gi);
#pragma unroll
        for (int r = 1; r < 4; ++r) {
          float hr, hi;
          cmul(gr, gi, Rr[2 * r], Ri[2 * r], hr, hi);
          *(u32*)(wr + (32 + (ii - 2) * 4 + r) * WPITCH) = pk_h(hr, hi);
        }
        if (ii < 3) cmul(gr, gi, eHr, eHi, gr, gi);
      }
    }
    __syncthreads();
    STAMP(2);
    const int b = lane & 15, g = lane >> 4;
    u16* P = Pg + (size_t)d * PPITCH;
    if (wave < 3) {
      const int rowa = 16 + 16 * wave + b;
      const char* ra = shm + (rowa > 56 ? 56 : rowa) * WPITCH + g * 16;
      const char* rb = shm + b * WPITCH + g * 16;
      f4 ka = (f4){0.f, 0.f, 0.f, 0.f};
#pragma unroll
      for (int k8 = 0; k8 < 8; ++k8) {
        h8 a = *(const h8*)(ra + k8 * 64);
        h8 bb = *(const h8*)(rb + k8 * 64);
        ka = __builtin_amdgcn_mfma_f32_16x16x32_f16(a, bb, ka, 0, 0, 0);
      }
      const u32 k01 = pk_h(ka[0], ka[1]), k23 = pk_h(ka[2], ka[3]);
      if (wave == 0) {
        *(u32x2*)&P[(g >> 1) * 128 + (g & 1) * 4 + 8 * b] = (u32x2){k01, k23};
      } else if (wave == 1) {
        const int i1 = g < 2 ? 256 + 64 * g + 4 * b : 384 + 64 * (g - 2) + 2 * b;
        *(u32*)&P[i1] = k01;
        *(u32*)&P[i1 + (g < 2 ? 2 : 32)] = k23;
      } else {
        const int i2 = 512 + 64 * g + b;
        if (g < 3) P[i2] = (u16)k01;
        if (g < 2) {
          P[i2 + 16] = (u16)(k01 >> 16);
          P[i2 + 32] = (u16)k23; P[i2 + 48] = (u16)(k23 >> 16);
        }
      }
    } else if (lane == 0) {
      *(float*)&P[704] = kscale;
    }
    STAMP(3);
    return;
  }
  if (blockIdx.x < K1_LN + K1_KG + K1_RP) {
    const int u0 = (blockIdx.x - (K1_LN + K1_KG)) * 2;
    repack_load(w1, FF, u0 / 27, u0 % 27, k1s, t);
    repack_load(w1, FF, (u0 + 1) / 27, (u0 + 1) % 27, k1s + 10240, t);
    __syncthreads();
    repack_store(w1P, DM / 32, u0 / 27, u0 % 27, k1s, t);
    repack_store(w1P, DM / 32, (u0 + 1) / 27, (u0 + 1) % 27, k1s + 10240, t);
    STAMP(3);
    return;
  }
  if (blockIdx.x < K1_LN + K1_KG + 2 * K1_RP) {
    const int u0 = (blockIdx.x - (K1_LN + K1_KG + K1_RP)) * 2;
    repack_load(w2, DM, u0 / 10, u0 % 10, k1s, t);
    repack_load(w2, DM, (u0 + 1) / 10, (u0 + 1) % 10, k1s + 10240, t);
    __syncthreads();
    repack_store(w2P, FF / 32, u0 / 10, u0 % 10, k1s, t);
    repack_store(w2P, FF / 32, (u0 + 1) / 10, (u0 + 1) % 10, k1s + 10240, t);
    STAMP(3);
    return;
  }
  {
    int idx = (blockIdx.x - (K1_LN + K1_KG + 2 * K1_RP)) * 256 + t;
    if (idx >= 2 && idx < LUT_N + 2) {
      int sgn = idx & 1;
      int a = ((idx - 2) >> 1) + (LUT_E0 << 7);
      float h = bf2f((u16)(a | (sgn << 15)));
      lut[idx] = f2bf(mish_bf(h));
    } else if (idx < 6144) {
      lut[idx] = 0;
    }
  }
}

#define K2_G1 256
#define K2_SSM 256
#define KCPITCH 2080
#define SB0() __builtin_amdgcn_sched_barrier(0)
template <int V> struct IC { static constexpr int value = V; };
__device__ __forceinline__ void glds16s(u32 voff, const void* sbase, const void* lp) {
  unsigned keep;
  unsigned lds_dst = (unsigned)(size_t)(const __attribute__((address_space(3))) char*)lp;
  asm volatile("s_mov_b32 %0, m0\n\ts_mov_b32 m0, %3\n\ts_nop 0\n\tglobal_load_lds_dwordx4 %1, %2\n\ts_mov_b32 m0, %0"
               : "=&s"(keep) : "v"(voff), "s"(sbase), "s"(lds_dst) : "memory");
}
#define MFMA_BF(acc, a, b) \
  asm volatile("v_mfma_f32_16x16x32_bf16 %0, %1, %2, %0" : "+v"(acc) : "v"(a), "v"(b))
#define MFMA_BF0(acc, a, b) \
  asm volatile("v_mfma_f32_16x16x32_bf16 %0, %1, %2, 0" : "=&v"(acc) : "v"(a), "v"(b))
#define GLOAD4(dst, voff, sbase) \
  asm volatile("global_load_dwordx4 %0, %1, %2" : "=v"(dst) : "v"(voff), "s"(sbase) : "memory")
#define DSR64(dst, addr, off) asm volatile("ds_read_b64 %0, %1 offset:%2" : "=v"(dst) : "v"(addr), "n"(off) : "memory")
#define DSR128(dst, addr, off) asm volatile("ds_read_b128 %0, %1 offset:%2" : "=v"(dst) : "v"(addr), "n"(off) : "memory")

#define G1_ND 11
#define G1_RA 8
#define G1_LK 11
constexpr int g1_wait(int kt, int nb, int nl) {
  int pos = 0, need = 0;
  for (int s = 0; s < G1_ND - 1; ++s) {
    pos += nb; if (s <= kt + 1) need = pos;
    if (s < G1_RA) { pos += 2; if (s <= kt) need = pos; }
  }
  for (int i = 0; i < kt; ++i) {
    if (i + G1_ND - 1 < 20) { pos += nb; if (i + G1_ND - 1 <= kt + 1) need = pos; }
    if (i == G1_LK) pos += nl;
    if (i + G1_RA < 20) { pos += 2; if (i + G1_RA <= kt) need = pos; }
  }
  return pos - need;
}

__global__ __launch_bounds__(256, 2) void k2_kernel(const u16* __restrict__ xnP, const u16* __restrict__ w1P,
                                                    const u16* __restrict__ b1, const u16* __restrict__ lut,
                                                    u16* __restrict__ actP,
                                                    const u16* __restrict__ xnT, const u16* __restrict__ Pg,
                                                    u16* __restrict__ yT) {
  __shared__ __attribute__((aligned(1024))) char shm[81920];
  const int t = threadIdx.x, lane = t & 63;
  const int wave = __builtin_amdgcn_readfirstlane(t >> 6);
  STAMPB(2, (char*)actP - (16u << 20));
  STAMP(0);
  if (blockIdx.x >= K2_G1) {
    const int sb = blockIdx.x - K2_G1;
    const int nch = sb < 128 ? 3 : 2;
    u16* Kc = (u16*)shm;
    float* red = (float*)(shm + 16896);
#pragma unroll
    for (int c = 0; c < 3; ++c) {
      if (c < nch) {
        const int d = sb + 256 * c;
        char* buf = shm + 41472 + c * 6144;
        if (wave == 0) glds16s(lane * 16, (const char*)(Pg + (size_t)d * PPITCH), buf);
        if (wave == 1 && lane < 32) glds16s(lane * 16, (const char*)(Pg + (size_t)d * PPITCH) + 1024, buf + 1024);
        glds16s(lane * 16, (const char*)(xnT + (size_t)d * SEQ) + wave * 1024, buf + 2048 + wave * 1024);
      }
    }
    if (t < 96) *(u32x4*)(shm + 41472 + (t >> 5) * 6144 + 1536 + (t & 31) * 16) = (u32x4){0u, 0u, 0u, 0u};
    if (t < 8) *(u32x4*)&Kc[(t >> 1) * KCPITCH + (256 + (t & 1)) * 8] = (u32x4){0u, 0u, 0u, 0u};
    WAIT_V(0);
    RAW_BARRIER();
    STAMP(3);
    const int b = lane & 15, g = lane >> 4;
    const int m = 255 - t;
    const u32 lds0 = (u32)(size_t)(const __attribute__((address_space(3))) char*)shm;
    const int abase = ((3 - b) & 3) * KCPITCH + 2028 - 4 * (b >> 2) + 8 * g;
    const int bbase = 256 - 16 + 16 * b + 8 * g;
#pragma unroll 1
    for (int c = 0; c < nch; ++c) {
      const int d = sb + 256 * c;
      const u16* P = (const u16*)(shm + 41472 + c * 6144);
      const float kscale = *(const float*)&P[704];
      {
        u32 wv[6];
        if (wave < 2) {
          const int i = 512 + m - 128;
          const u32 pc = P[i - 1], pa = P[i], pb = P[i + 1];
          const u32 aa = pa | (pa << 16);
          wv[0] = pb | (pb << 16);
          wv[1] = pb | (((m & 1) ? pb : pa) << 16);
          wv[2] = aa; wv[3] = aa; wv[4] = aa;
          wv[5] = m == 128 ? pc : pa;
        } else if (wave == 2) {
          const int i = 384 + 2 * (m - 64);
          const u32 q0 = *(const u32*)&P[i - 2], q1 = *(const u32*)&P[i], q2 = *(const u32*)&P[i + 2];
          wv[0] = __builtin_amdgcn_perm(q2, q2, 0x01000100u);
          wv[1] = __builtin_amdgcn_perm(q1, q1, 0x03020302u);
          wv[2] = __builtin_amdgcn_perm(q1, q1, 0x01000302u);
          wv[3] = __builtin_amdgcn_perm(q1, q1, 0x01000100u);
          wv[4] = m == 64 ? __builtin_amdgcn_perm(q0, q1, 0x07060100u) : wv[3];
          wv[5] = m == 64 ? q0 : (q0 >> 16);
        } else {
#pragma unroll
          for (int hh = 0; hh < 2; ++hh) {
            const int mm = m - hh;
            const int sg = mm >= 32 ? 1 : 0;
#pragma unroll
            for (int j = 0; j < (hh ? 2 : 4); ++j) {
              u32 pk = 0;
#pragma unroll
              for (int h = 0; h < 2; ++h) {
                if (hh == 0 || 2 * j + h < 3) {
                  int q = 8 * mm + 7 - (2 * j + h);
                  q = q < 0 ? 0 : q;
                  int idx = sg ? 128 + ((q + ((q >> 1) & 1)) >> 1) : q;
                  pk |= ((u32)P[idx]) << (16 * h);
                }
              }
              wv[hh * 4 + j] = (hh && mm < 0) ? 0u : pk;
            }
          }
        }
        *(u32x4*)&Kc[0 * KCPITCH + t * 8] = (u32x4){wv[0], wv[1], wv[2], wv[3]};
        *(u32x4*)&Kc[1 * KCPITCH + t * 8] = (u32x4){__builtin_amdgcn_alignbit(wv[1], wv[0], 16), __builtin_amdgcn_alignbit(wv[2], wv[1], 16),
                                                    __builtin_amdgcn_alignbit(wv[3], wv[2], 16), __builtin_amdgcn_alignbit(wv[4], wv[3], 16)};
        *(u32x4*)&Kc[2 * KCPITCH + t * 8] = (u32x4){wv[1], wv[2], wv[3], wv[4]};
        *(u32x4*)&Kc[3 * KCPITCH + t * 8] = (u32x4){__builtin_amdgcn_alignbit(wv[2], wv[1], 16), __builtin_amdgcn_alignbit(wv[3], wv[2], 16),
                                                    __builtin_amdgcn_alignbit(wv[4], wv[3], 16), __builtin_amdgcn_alignbit(wv[5], wv[4], 16)};
      }
      __syncthreads();
      if (c == 0) STAMP(4);
      f4 acc[8];
#pragma unroll
      for (int i = 0; i < 8; ++i) acc[i] = (f4){0.f, 0.f, 0.f, 0.f};
      {
        u32x2 a0[2][8], a1[2][8];
        u32x4 bq[2][8];
        const u32 aaddr = lds0 + (u32)(abase - 64 * wave - 32 - 256 * 7) * 2;
        const u32 baddr = lds0 + 41472 + c * 6144 + 1536 + (u32)(bbase - 64 * wave - 32) * 2;
#pragma unroll
        for (int rr = 0; rr < 2; ++rr) {
#pragma unroll
          for (int u = 0; u < 8; ++u) {
            DSR64(a0[rr][u], aaddr, (7 - u) * 512 + (1 - rr) * 64);
            DSR64(a1[rr][u], aaddr, (7 - u) * 512 + (1 - rr) * 64 + 8);
            DSR128(bq[rr][u], baddr, u * 512 + (1 - rr) * 64);
            if (rr == 1 && u == 3) {
              asm volatile("s_waitcnt lgkmcnt(12)"
                           : "+v"(a0[0][0]), "+v"(a1[0][0]), "+v"(a0[0][1]), "+v"(a1[0][1]), "+v"(a0[0][2]), "+v"(a1[0][2]),
                             "+v"(a0[0][3]), "+v"(a1[0][3]), "+v"(a0[0][4]), "+v"(a1[0][4]), "+v"(a0[0][5]), "+v"(a1[0][5]),
                             "+v"(a0[0][6]), "+v"(a1[0][6]), "+v"(a0[0][7]), "+v"(a1[0][7]),
                             "+v"(bq[0][0]), "+v"(bq[0][1]), "+v"(bq[0][2]), "+v"(bq[0][3]),
                             "+v"(bq[0][4]), "+v"(bq[0][5]), "+v"(bq[0][6]), "+v"(bq[0][7])
                           :: "memory");
            }
          }
        }
        SB0();
#pragma unroll
        for (int rr = 0; rr < 2; ++rr) {
          if (rr == 1) {
            asm volatile("s_waitcnt lgkmcnt(0)"
                         : "+v"(a0[1][0]), "+v"(a1[1][0]), "+v"(a0[1][1]), "+v"(a1[1][1]), "+v"(a0[1][2]), "+v"(a1[1][2]),
                           "+v"(a0[1][3]), "+v"(a1[1][3]), "+v"(a0[1][4]), "+v"(a1[1][4]), "+v"(a0[1][5]), "+v"(a1[1][5]),
                           "+v"(a0[1][6]), "+v"(a1[1][6]), "+v"(a0[1][7]), "+v"(a1[1][7]),
                           "+v"(bq[1][0]), "+v"(bq[1][1]), "+v"(bq[1][2]), "+v"(bq[1][3]),
                           "+v"(bq[1][4]), "+v"(bq[1][5]), "+v"(bq[1][6]), "+v"(bq[1][7])
                         :: "memory");
          }
#pragma unroll
          for (int u = 0; u < 8; ++u) {
            const h8 A = __builtin_bit_cast(h8, (u32x4){a0[rr][u][0], a0[rr][u][1], a1[rr][u][0], a1[rr][u][1]});
#pragma unroll
            for (int v = 0; v + u < 8; ++v)
              acc[u + v] = __builtin_amdgcn_mfma_f32_16x16x32_f16(A, __builtin_bit_cast(h8, bq[rr][v]), acc[u + v], 0, 0, 0);
          }
          SB0();
        }
      }
      if (c == 0) STAMP(5);
      auto red_phase = [&](auto WC) {
        constexpr int W = decltype(WC)::value;
#pragma unroll
        for (int o = 1; o < 4; ++o) {
          const int ow = (W + o) & 3;
#pragma unroll
          for (int i = 0; i < 2; ++i) *(f4*)&red[(((ow * 3 + (o - 1)) * 2 + i) * 64 + lane) * 4] = acc[ow * 2 + i];
        }
        __syncthreads();
#pragma unroll
        for (int i = 0; i < 2; ++i) {
          f4 s1 = *(const f4*)&red[(((W * 3 + 0) * 2 + i) * 64 + lane) * 4];
          f4 s2 = *(const f4*)&red[(((W * 3 + 1) * 2 + i) * 64 + lane) * 4];
          f4 s3 = *(const f4*)&red[(((W * 3 + 2) * 2 + i) * 64 + lane) * 4];
          f4 sum = ((acc[W * 2 + i] + s1) + (s2 + s3)) * kscale;
          u32x2 o = {pk_bf(sum[0], sum[1]), pk_bf(sum[2], sum[3])};
          *(u32x2*)(yT + (size_t)d * SEQ + 256 * (W * 2 + i) + 16 * b + 4 * g) = o;
        }
      };
      if (wave == 0) red_phase(IC<0>{});
      else if (wave == 1) red_phase(IC<1>{});
      else if (wave == 2) red_phase(IC<2>{});
      else red_phase(IC<3>{});
      if (c == 0) STAMP(6);
      if (c == 1) STAMP(7);
    }
    STAMP(8);
    return;
  }
  constexpr int ND = G1_ND;
  constexpr int STG = 7 * 1024;
  constexpr int NKT = DM / 32;
  constexpr int LUTOFF = 9 * STG;
  const int xcd = blockIdx.x & 7, idx = blockIdx.x >> 3;
  const int mi = idx & 3, ni = idx >> 2;
  const int bm = (xcd >> 1) * 4 + mi, bn = (xcd & 1) * 8 + ni;
  const int mt0 = bm * 8, nt0 = bn * 7;
  const char* abase = (const char*)xnP + (size_t)(mt0 + 2 * wave) * (20 * 1024);
  const u32 va0 = lane * 16, va1 = lane * 16 + 20 * 1024;
  const int p0 = 2 * wave, p1 = wave < 3 ? 2 * wave + 1 : 6;
  const u32 vb0 = lane * 16 + (u32)(nt0 + p0 > 107 ? 107 : nt0 + p0) * (20 * 1024);
  const u32 vb1 = lane * 16 + (u32)(nt0 + p1 > 107 ? 107 : nt0 + p1) * (20 * 1024);
  auto stage = [&](int s) {
    char* sb = shm + (s % ND) * STG;
    const char* bp = (const char*)w1P + s * 1024;
    glds16s(vb0, bp, sb + p0 * 1024);
    if (wave < 3) glds16s(vb1, bp, sb + p1 * 1024);
  };
  u32x4 fa[G1_RA][2];
  auto loadA = [&](int s) {
    const char* ap = abase + s * 1024;
    GLOAD4(fa[s % G1_RA][0], va0, ap);
    GLOAD4(fa[s % G1_RA][1], va1, ap);
  };
  f4 acc[2][7];
  s8 fb[7];
#pragma unroll
  for (int s = 0; s < ND - 1; ++s) { stage(s); if (s < G1_RA) loadA(s); }
  STAMP(1);
  if (wave < 3) WAIT_V(g1_wait(-1, 2, 3)); else WAIT_V(g1_wait(-1, 1, 4));
  __builtin_amdgcn_s_barrier();
  STAMP(2);
#pragma unroll
  for (int j = 0; j < 7; ++j) fb[j] = *(const s8*)(shm + j * 1024 + lane * 16);
#pragma unroll
  for (int kt = 0; kt < NKT; ++kt) {
    if (wave < 3) WAIT_V(g1_wait(kt, 2, 3)); else WAIT_V(g1_wait(kt, 1, 4));
    asm volatile("" : "+v"(fa[kt % G1_RA][0]), "+v"(fa[kt % G1_RA][1]));
    __builtin_amdgcn_s_barrier();
    if (kt + ND - 1 < NKT) stage(kt + ND - 1);
    if (kt == G1_LK) {
#pragma unroll
      for (int q = 0; q < 3; ++q) glds16s(lane * 16, (const char*)lut + (wave * 3 + q) * 1024, shm + LUTOFF + (wave * 3 + q) * 1024);
      int o = nt0 * 16 + (lane < 14 ? lane : 13) * 8; if (o > FF - 8) o = FF - 8;
      if (wave == 3) glds16s(o * 2, (const char*)b1, shm + LUTOFF + 12 * 1024);
    }
    SB0();
#pragma unroll
    for (int j = 0; j < 7; ++j) {
      if (kt == 0) {
        MFMA_BF0(acc[0][j], fb[j], fa[0][0]);
        MFMA_BF0(acc[1][j], fb[j], fa[0][1]);
      } else {
        MFMA_BF(acc[0][j], fb[j], fa[kt % G1_RA][0]);
        MFMA_BF(acc[1][j], fb[j], fa[kt % G1_RA][1]);
      }
      if (kt + 1 < NKT) fb[j] = *(const s8*)(shm + ((kt + 1) % ND) * STG + j * 1024 + lane * 16);
      SB0();
    }
    if (kt + G1_RA < NKT) loadA(kt + G1_RA);
    SB0();
    if (kt == 3) STAMP(3);
    if (kt == 7) STAMP(4);
    if (kt == 11) STAMP(5);
    if (kt == 15) STAMP(6);
  }
  asm volatile("s_nop 7\n\ts_nop 7\n\ts_nop 7" ::: "memory");
  WAIT_V(0);
  __builtin_amdgcn_s_barrier();
  STAMP(7);
  const int g = lane >> 4;
  const char* slut = shm + LUTOFF;
  const u16* sbias = (const u16*)(shm + LUTOFF + 12 * 1024);
  typedef unsigned short us2 __attribute__((ext_vector_type(2)));
  u32 hw[28];
  us2 res[28];
  us2 mx = {0, 0}, mn = {0xFFFF, 0xFFFF};
#pragma unroll
  for (int j = 0; j < 7; ++j) {
    u32x2 bb = *(const u32x2*)(sbias + j * 16 + 4 * g);
    const f2_t bv0 = {lo_f(bb[0]), hi_f(bb[0])}, bv1 = {lo_f(bb[1]), hi_f(bb[1])};
#pragma unroll
    for (int i = 0; i < 2; ++i) {
      u32 t0 = pk_bf(acc[i][j][0], acc[i][j][1]);
      u32 t1 = pk_bf(acc[i][j][2], acc[i][j][3]);
      f2_t s0 = (f2_t){lo_f(t0), hi_f(t0)} + bv0;
      f2_t s1 = (f2_t){lo_f(t1), hi_f(t1)} + bv1;
      hw[(j * 2 + i) * 2 + 0] = pk_bf(s0[0], s0[1]);
      hw[(j * 2 + i) * 2 + 1] = pk_bf(s1[0], s1[1]);
#pragma unroll
      for (int h = 0; h < 2; ++h) {
        const u32 w = hw[(j * 2 + i) * 2 + h];
        const u32 r = ((w << 1) & 0xFFFEFFFEu) | ((w >> 15) & 0x00010001u);
        const us2 r2 = __builtin_bit_cast(us2, r);
        mx = __builtin_elementwise_max(mx, r2);
        mn = __builtin_elementwise_min(mn, (us2)(r2 - (us2){2, 2}));
        us2 ix = __builtin_elementwise_sub_sat(r2, (us2){(LUT_E0 << 8) - 2, (LUT_E0 << 8) - 2});
        ix = __builtin_elementwise_min(ix, (us2){6143, 6143});
        res[(j * 2 + i) * 2 + h][0] = *(const u16*)(slut + (u32)ix[0] * 2u);
        res[(j * 2 + i) * 2 + h][1] = *(const u16*)(slut + (u32)ix[1] * 2u);
      }
    }
  }
  const bool oor = (mx[0] >= (LUT_E1 << 8)) | (mx[1] >= (LUT_E1 << 8)) | (mn[0] < (LUT_E0 << 8) - 2) | (mn[1] < (LUT_E0 << 8) - 2);
  if (__builtin_amdgcn_ballot_w64(oor) != 0) {
#pragma unroll
    for (int q = 0; q < 28; ++q) {
      const u32 z0 = (hw[q] & 0x7FFFu) - 1u, z1 = ((hw[q] >> 16) & 0x7FFFu) - 1u;
      const bool b0 = z0 != 0xFFFFFFFFu && (z0 - ((LUT_E0 << 7) - 1)) >= (u32)LUT_HALF;
      const bool b1f = z1 != 0xFFFFFFFFu && (z1 - ((LUT_E0 << 7) - 1)) >= (u32)LUT_HALF;
      if (__builtin_amdgcn_ballot_w64(b0 | b1f) != 0) {
        const float h0 = lo_f(hw[q]), h1 = hi_f(hw[q]);
        const float f0 = (z0 < (LUT_E0 << 7)) ? h0 * 0.59765625f : (h0 > 0.f ? h0 : h0 * 0.0f);
        const float f1 = (z1 < (LUT_E0 << 7)) ? h1 * 0.59765625f : (h1 > 0.f ? h1 : h1 * 0.0f);
        const u32 fw = pk_bf(f0, f1);
        res[q][0] = b0 ? (u16)(fw & 0xFFFFu) : res[q][0];
        res[q][1] = b1f ? (u16)(fw >> 16) : res[q][1];
      }
    }
  }
#pragma unroll
  for (int j = 0; j < 7; ++j) {
    const int nt = nt0 + j;
    if (nt < 108) {
#pragma unroll
      for (int i = 0; i < 2; ++i) {
        const int mt = mt0 + wave * 2 + i;
        u32x2 ov = {__builtin_bit_cast(u32, res[(j * 2 + i) * 2]), __builtin_bit_cast(u32, res[(j * 2 + i) * 2 + 1])};
        *(u32x2*)(actP + (((size_t)mt * 54 + (nt >> 1)) * 64 + lane) * 8 + (nt & 1) * 4) = ov;
      }
    }
  }
  STAMP(8);
}

__global__ __launch_bounds__(256, 1) void k3_kernel(const u16* __restrict__ actP, const u16* __restrict__ w2P,
                                                    const u16* __restrict__ xin, const float2* __restrict__ stats,
                                                    const u16* __restrict__ yT, const u16* __restrict__ dskip,
                                                    u16* __restrict__ out) {
  constexpr int R = 5;
  constexpr int NK = FF / 32;
  constexpr int NST = 14;
  __shared__ __attribute__((aligned(1024))) char shm[21 * 1024 + 60 * 1024];
  const int t = threadIdx.x, lane = t & 63;
  const int wave = __builtin_amdgcn_readfirstlane(t >> 6);
  const int xcd = blockIdx.x & 7, idx = blockIdx.x >> 3;
  const int mi = idx & 7, ni = idx >> 3;
  const int bm = (xcd >> 1) * 8 + mi, bn = (xcd & 1) * 4 + ni;
#define PH3 0
  const int phase = PH3 == 0 ? 0 : (PH3 == 1 ? ((27 * mi + 54 * ni) >> 2) % NK : (PH3 == 2 ? (mi + 2 * ni) : (xcd * 6)));
  const int mt0 = bm * 4, m0 = bm * 64, n0 = bn * 80, nt0 = bn * 5;
  STAMPB(3, (char*)actP - (16u << 20));
  STAMP(0);
#pragma unroll
  for (int i = 0; i < 6; ++i) {
    int p = wave + 4 * i;
    if (p > 20) p = 20;
    int c = (p < 10 ? p : p - 10) * 64 + lane;
    const u16* src;
    if (p < 10) src = xin + (size_t)(m0 + c / 10) * DM + n0 + (c % 10) * 8;
    else if (p < 20) src = yT + (size_t)(n0 + (c >> 3)) * SEQ + m0 + (c & 7) * 8;
    else src = (lane < 32) ? (const u16*)(stats + m0) + lane * 8 : dskip + n0 + (lane < 42 ? lane - 32 : 9) * 8;
    GLDS(src, shm + p * 1024);
  }
  const char* abase = (const char*)actP + (size_t)mt0 * NK * 1024;
  const char* bbase = (const char*)w2P + (size_t)nt0 * NK * 1024;
  u32 va[4], vb[5];
#pragma unroll
  for (int i = 0; i < 4; ++i) va[i] = lane * 16 + ((wave + i) & 3) * (NK * 1024);
#pragma unroll
  for (int j = 0; j < 5; ++j) vb[j] = lane * 16 + j * (NK * 1024);
  u32x4 fa[R][4], fb[R][5];
  auto issue = [&](int s) {
    int q = (s < NST - 1 || wave < 2) ? wave + 4 * s : wave + 4 * (s - 1);
    int ks = q + phase;
    if (ks >= NK) ks -= NK;
    const char* ap = abase + ks * 1024;
    const char* bp = bbase + ks * 1024;
#pragma unroll
    for (int i = 0; i < 4; ++i) fa[s % R][i] = *(const u32x4*)(ap + va[i]);
#pragma unroll
    for (int j = 0; j < 5; ++j) fb[s % R][j] = *(const u32x4*)(bp + vb[j]);
  };
  SB0();
#pragma unroll
  for (int s = 0; s < R; ++s) { issue(s); SB0(); }
  STAMP(1);
  WAIT_V(R * 9);
  RAW_BARRIER();
  STAMP(2);
  const int fr = lane & 15, g = lane >> 4, tq = (lane & 15) >> 2, tp = lane & 3;
  const int ml = wave * 16 + fr;
  u32x2 s1p[5];
  {
    const char* sx = shm;
    const char* sy = shm + 10 * 1024;
    const char* sm = shm + 20 * 1024;
    const float2 st = *(const float2*)(sm + ml * 8);
#pragma unroll
    for (int j = 0; j < 5; ++j) {
      const int nl = j * 16;
      u32x2 xx = *(const u32x2*)(sx + ml * 160 + (nl + 4 * g) * 2);
      u32x2 dd = *(const u32x2*)(sm + 512 + (nl + 4 * g) * 2);
      s4 yy = TRREAD(sy + (nl + 4 * g + tq) * 128 + (wave * 16 + 4 * tp) * 2);
      const u32x2 yw = __builtin_bit_cast(u32x2, yy);
#pragma unroll
      for (int h = 0; h < 2; ++h) {
        const float x0 = lo_f(xx[h]), x1 = hi_f(xx[h]);
        u32 d = pk_bf(x0 - st.x, x1 - st.x);
        u32 xn = pk_bf(lo_f(d) * st.y, hi_f(d) * st.y);
        u32 tt = pk_bf(lo_f(xn) * lo_f(dd[h]), hi_f(xn) * hi_f(dd[h]));
        u32 a = pk_bf(lo_f(yw[h]) + lo_f(tt), hi_f(yw[h]) + hi_f(tt));
        s1p[j][h] = pk_bf(x0 + lo_f(a), x1 + hi_f(a));
      }
      asm volatile("" : "+v"(s1p[j]));
    }
  }
  f4 acc[4][5];
#pragma unroll
  for (int i = 0; i < 4; ++i)
#pragma unroll
    for (int j = 0; j < 5; ++j) acc[i][j] = (f4){0.f, 0.f, 0.f, 0.f};
  SB0();
  STAMP(3);
#pragma unroll
  for (int s = 0; s < NST; ++s) {
    if (s == NST - 1) {
#pragma unroll
      for (int i = 0; i < 4; ++i) asm volatile("" : "+v"(fa[s % R][i]));
#pragma unroll
      for (int j = 0; j < 5; ++j) asm volatile("" : "+v"(fb[s % R][j]));
    }
    if (s < NST - 1 || wave < 2) {
#pragma unroll
      for (int i = 0; i < 4; ++i)
#pragma unroll
        for (int j = 0; j < 5; ++j)
          acc[i][j] = __builtin_amdgcn_mfma_f32_16x16x32_bf16(__builtin_bit_cast(s8, fb[s % R][j]),
                                                              __builtin_bit_cast(s8, fa[s % R][i]), acc[i][j], 0, 0, 0);
    }
    SB0();
    if (s + R < NST) issue(s + R);
    SB0();
    if (s == 2) STAMP(4);
    if (s == 6) STAMP(5);
    if (s == 10) STAMP(6);
  }
  STAMP(7);
  char* red = shm + 21 * 1024;
#pragma unroll
  for (int i = 1; i < 4; ++i)
#pragma unroll
    for (int j = 0; j < 5; ++j)
      *(f4*)(red + ((((wave + i) & 3) * 3 + (i - 1)) * 5 + j) * 1024 + lane * 16) = acc[i][j];
  __syncthreads();
#pragma unroll
  for (int j = 0; j < 5; ++j) {
    f4 p0 = *(const f4*)(red + ((wave * 3 + 0) * 5 + j) * 1024 + lane * 16);
    f4 p1 = *(const f4*)(red + ((wave * 3 + 1) * 5 + j) * 1024 + lane * 16);
    f4 p2 = *(const f4*)(red + ((wave * 3 + 2) * 5 + j) * 1024 + lane * 16);
    f4 sum = (acc[0][j] + p0) + (p1 + p2);
    u32 m0p = pk_bf(sum[0], sum[1]), m1p = pk_bf(sum[2], sum[3]);
    u32x2 ov = {pk_bf(lo_f(s1p[j][0]) + lo_f(m0p), hi_f(s1p[j][0]) + hi_f(m0p)),
                pk_bf(lo_f(s1p[j][1]) + lo_f(m1p), hi_f(s1p[j][1]) + hi_f(m1p))};
    *(u32x2*)(out + (size_t)(m0 + ml) * DM + n0 + j * 16 + 4 * g) = ov;
  }
  STAMP(8);
}

extern "C" void kernel_launch(void* const* d_in, const int* in_sizes, int n_in,
                              void* d_out, int out_size, void* d_ws, size_t ws_size,
                              hipStream_t stream) {
  const u16* u = (const u16*)d_in[0];
  const u16* lar = (const u16*)d_in[1];
  const u16* aim = (const u16*)d_in[2];
  const u16* cre = (const u16*)d_in[3];
  const u16* cim = (const u16*)d_in[4];
  const u16* logdt = (const u16*)d_in[5];
  const u16* dskip = (const u16*)d_in[6];
  const u16* w1 = (const u16*)d_in[8];
  const u16* b1 = (const u16*)d_in[9];
  const u16* w2 = (const u16*)d_in[10];
  u16* out = (u16*)d_out;
  char* ws = (char*)d_ws;
  if (ws_size < (32u << 20)) return;
  u16* xnP = (u16*)(ws);
  u16* w1P = (u16*)(ws + (4u << 20));
  float2* stats = (float2*)(ws + (12u << 20) + 4096);
  u16* lut = (u16*)(ws + (12u << 20) + 32768);
  u16* yT = (u16*)(ws + (13u << 20));
  u16* actP = (u16*)(ws + (16u << 20));
  u16* w2P = (u16*)(ws + (24u << 20));
  u16* xnT = (u16*)(ws + (8u << 20));
  u16* Pg = (u16*)(ws + (28u << 20));

  k1_kernel<<<K1_LN + K1_KG + 2 * K1_RP + K1_LUT, 256, 0, stream>>>(u, xnP, stats, lut, w1, w1P, w2, w2P, xnT, lar, aim, cre, cim, logdt, Pg);
  k2_kernel<<<K2_G1 + K2_SSM, 256, 0, stream>>>(xnP, w1P, b1, lut, actP, xnT, Pg, yT);
  k3_kernel<<<256, 256, 0, stream>>>(actP, w2P, u, stats, yT, dskip, out);
}
```

```cpp
#include <hip/hip_runtime.h>
#include <hip/hip_bf16.h>

#define SEQ 2048
#define DM 640
#define NS 128
#define FF 1728

typedef unsigned short u16;
typedef short s4 __attribute__((ext_vector_type(4)));
typedef short s8 __attribute__((ext_vector_type(8)));
typedef _Float16 h8 __attribute__((ext_vector_type(8)));
typedef float f4 __attribute__((ext_vector_type(4)));
typedef unsigned int u32;
typedef unsigned int u32x4 __attribute__((ext_vector_type(4)));
typedef unsigned int u32x2 __attribute__((ext_vector_type(2)));

__device__ __forceinline__ float bf2f(u16 v) { return __uint_as_float(((u32)v) << 16); }
__device__ __forceinline__ u16 f2bf(float f) {
  u32 u = __float_as_uint(f);
  u32 r = (u + 0x7FFFu + ((u >> 16) & 1u)) >> 16;
  return (u16)r;
}
__device__ __forceinline__ float bfr(float f) { return bf2f(f2bf(f)); }

#define STAMPB(kid, base)
#define STAMP(k)
typedef __bf16 bf2_t __attribute__((ext_vector_type(2)));
typedef float f2_t __attribute__((ext_vector_type(2)));
__device__ __forceinline__ u32 pk_bf(float a, float b) {
  f2_t v = {a, b};
  bf2_t r = __builtin_convertvector(v, bf2_t);
  return __builtin_bit_cast(u32, r);
}
__device__ __forceinline__ float lo_f(u32 w) { return __uint_as_float(w << 16); }
__device__ __forceinline__ float hi_f(u32 w) { return __uint_as_float(w & 0xFFFF0000u); }

#define WAIT_V(n) asm volatile("s_waitcnt vmcnt(%0)" ::"n"(n) : "memory")
#define RAW_BARRIER() do { asm volatile("s_waitcnt lgkmcnt(0)" ::: "memory"); __builtin_amdgcn_s_barrier(); } while (0)
__device__ __forceinline__ void glds16(const void* gsrc, const void* lp) {
  unsigned keep;
  unsigned lds_dst = (unsigned)(size_t)(const __attribute__((address_space(3))) char*)lp;
  asm volatile("s_mov_b32 %0, m0\n\ts_mov_b32 m0, %2\n\ts_nop 0\n\tglobal_load_lds_dwordx4 %1, off\n\ts_mov_b32 m0, %0"
               : "=&s"(keep) : "v"(gsrc), "s"(lds_dst) : "memory");
}
#define GLDS(gp, lp) glds16((const void*)(gp), (const void*)(lp))

#define STF " sc1"
__device__ __forceinline__ void st16(void* p, u32x4 v) { asm volatile("global_store_dwordx4 %0, %1, off" STF :: "v"(p), "v"(v) : "memory"); }
__device__ __forceinline__ void st8(void* p, u32x2 v) { asm volatile("global_store_dwordx2 %0, %1, off" STF :: "v"(p), "v"(v) : "memory"); }
__device__ __forceinline__ void st2(void* p, u32 v) { asm volatile("global_store_short %0, %1, off" STF :: "v"(p), "v"(v) : "memory"); }

#define LUT_E0 111
#define LUT_E1 134
#define LUT_HALF ((LUT_E1 - LUT_E0) * 128)
#define LUT_N (2 * LUT_HALF)

__device__ __forceinline__ float mish_bf(float h) {
  float e = bfr(expf(-fabsf(h)));
  float l = bfr(log1pf(e));
  float sp = bfr(l + (h > 0.f ? h : 0.f));
  float th = bfr(tanhf(sp));
  return bfr(h * th);
}

#define KRLEN 2080
#define WFENCE() do { asm volatile("" ::: "memory"); __builtin_amdgcn_wave_barrier(); asm volatile("" ::: "memory"); } while (0)
__device__ __forceinline__ void cexp_p(float arl2, float w, float p, float& re, float& im) {
  float mag = __builtin_amdgcn_exp2f(arl2 * p);
  float rev = w * p;
  rev = rev - floorf(rev);
  re = mag * __builtin_amdgcn_cosf(rev);
  im = mag * __builtin_amdgcn_sinf(rev);
}
__device__ __forceinline__ void cmul(float ar, float ai, float br, float bi, float& cr, float& ci) {
  float r = fmaf(ar, br, -(ai * bi));
  float i = fmaf(ar, bi, ai * br);
  cr = r; ci = i;
}
__device__ __forceinline__ u32x2 split2(float a, float b) {
  u32 h = pk_bf(a, b);
  u32 l = pk_bf(a - lo_f(h), b - hi_f(h));
  return (u32x2){h, l};
}

#define K1_LN 256
#define K1_RP 135
#define K1_LUT 24
#define WPITCH 544
#define TRREAD(p) __builtin_amdgcn_ds_read_tr16_b64_v4i16((__attribute__((address_space(3))) s4*)(p))

__device__ __forceinline__ void repack_load(const u16* __restrict__ w, int N, int ku, int nu, char* lds, int tl) {
#pragma unroll
  for (int i = 0; i < 2; ++i) {
    int idx = tl + 256 * i, r = idx >> 3, c = idx & 7;
    u32x4 v = *(const u32x4*)(w + (size_t)(ku * 64 + r) * N + nu * 64 + c * 8);
    *(u32x4*)(lds + r * 160 + c * 16) = v;
  }
}
__device__ __forceinline__ void repack_store(u16* __restrict__ wP, int KB, int ku, int nu, const char* lds, int tl) {
  const int wl = tl >> 6, lane = tl & 63, g = lane >> 4, tq = (lane & 15) >> 2, tp = lane & 3;
#pragma unroll
  for (int kb2 = 0; kb2 < 2; ++kb2) {
    const char* p0 = lds + (kb2 * 32 + 4 * g + tq) * 160 + wl * 32 + tp * 8;
    s4 b0 = TRREAD(p0);
    s4 b1 = TRREAD(p0 + 16 * 160);
    s8 bf = {b0[0], b0[1], b0[2], b0[3], b1[0], b1[1], b1[2], b1[3]};
    st16(wP + (((size_t)(nu * 4 + wl) * KB + ku * 2 + kb2) * 64 + lane) * 8, __builtin_bit_cast(u32x4, bf));
  }
}

__global__ __launch_bounds__(256) void k1_kernel(const u16* __restrict__ u, u16* __restrict__ xnP,
                                                 float2* __restrict__ stats, u16* __restrict__ lut,
                                                 const u16* __restrict__ w1, u16* __restrict__ w1P,
                                                 const u16* __restrict__ w2, u16* __restrict__ w2P) {
  __shared__ __attribute__((aligned(16))) char k1s[20480];
  const int t = threadIdx.x, lane = t & 63;
  const int wave = __builtin_amdgcn_readfirstlane(t >> 6);
  STAMPB(0, xnP);
  STAMP(0);
  if (blockIdx.x < K1_LN) {
    u16 (*tile)[DM + 8] = (u16 (*)[DM + 8])k1s;
    const int r = wave * 2 + (lane >> 5), q = lane & 31;
    const int row = blockIdx.x * 8 + r;
    float v[20];
    const u32x2* src = (const u32x2*)(u + (size_t)row * DM);
#pragma unroll
    for (int i = 0; i < 5; ++i) {
      u32x2 w = src[q + 32 * i];
      v[i * 4 + 0] = lo_f(w[0]); v[i * 4 + 1] = hi_f(w[0]);
      v[i * 4 + 2] = lo_f(w[1]); v[i * 4 + 3] = hi_f(w[1]);
    }
    float s = 0.f;
#pragma unroll
    for (int i = 0; i < 20; ++i) s += v[i];
    STAMP(1);
#pragma unroll
    for (int o = 1; o < 32; o <<= 1) s += __shfl_xor(s, o);
    const float mean = s * (1.0f / DM);
    float ss = 0.f;
#pragma unroll
    for (int i = 0; i < 20; ++i) { float d = v[i] - mean; ss += d * d; }
#pragma unroll
    for (int o = 1; o < 32; o <<= 1) ss += __shfl_xor(ss, o);
    const float var = ss * (1.0f / DM);
    const float mb = bfr(mean);
    const float vb = bfr(var);
    const float den = bfr(sqrtf(bfr(vb + 1e-5f)));
    const float rden = 1.0f / den;
    if (q == 0) st8(stats + row, (u32x2){__float_as_uint(mb), __float_as_uint(rden)});
#pragma unroll
    for (int i = 0; i < 5; ++i) {
      u32 d0 = pk_bf(v[i * 4 + 0] - mb, v[i * 4 + 1] - mb);
      u32 d1 = pk_bf(v[i * 4 + 2] - mb, v[i * 4 + 3] - mb);
      u32x2 w = {pk_bf(lo_f(d0) * rden, hi_f(d0) * rden), pk_bf(lo_f(d1) * rden, hi_f(d1) * rden)};
      *(u32x2*)&tile[r][(q + 32 * i) * 4] = w;
    }
    __syncthreads();
    STAMP(2);
    const int mt = blockIdx.x >> 1, half = blockIdx.x & 1;
#pragma unroll
    for (int i = 0; i < 3; ++i) {
      int idx = t + 256 * i;
      if (idx < 640) {
        int kb = idx >> 5, g = (idx >> 3) & 3, fr = idx & 7;
        u32x2 a = *(const u32x2*)&tile[fr][kb * 32 + 4 * g];
        u32x2 b = *(const u32x2*)&tile[fr][kb * 32 + 16 + 4 * g];
        u32x4 w = {a[0], a[1], b[0], b[1]};
        st16(xnP + (((size_t)mt * 20 + kb) * 64 + g * 16 + half * 8 + fr) * 8, w);
      }
    }
    STAMP(3);
    return;
  }
  if (blockIdx.x < K1_LN + K1_RP) {
    const int u0 = (blockIdx.x - K1_LN) * 2;
    repack_load(w1, FF, u0 / 27, u0 % 27, k1s, t);
    repack_load(w1, FF, (u0 + 1) / 27, (u0 + 1) % 27, k1s + 10240, t);
    __syncthreads();
    repack_store(w1P, DM / 32, u0 / 27, u0 % 27, k1s, t);
    repack_store(w1P, DM / 32, (u0 + 1) / 27, (u0 + 1) % 27, k1s + 10240, t);
    STAMP(3);
    return;
  }
  if (blockIdx.x < K1_LN + 2 * K1_RP) {
    const int u0 = (blockIdx.x - (K1_LN + K1_RP)) * 2;
    repack_load(w2, DM, u0 / 10, u0 % 10, k1s, t);
    repack_load(w2, DM, (u0 + 1) / 10, (u0 + 1) % 10, k1s + 10240, t);
    __syncthreads();
    repack_store(w2P, FF / 32, u0 / 10, u0 % 10, k1s, t);
    repack_store(w2P, FF / 32, (u0 + 1) / 10, (u0 + 1) % 10, k1s + 10240, t);
    STAMP(3);
    return;
  }
  {
    int idx = (blockIdx.x - (K1_LN + 2 * K1_RP)) * 256 + t;
    if (idx < LUT_N) {
      int sgn = idx >= LUT_HALF;
      int a = idx - sgn * LUT_HALF + (LUT_E0 << 7);
      float h = bf2f((u16)(a | (sgn << 15)));
      st2(lut + idx, f2bf(mish_bf(h)));
    } else if (idx < 6144) {
      st2(lut + idx, 0u);
    }
  }
}

#define K2_G1 256
#define K2_SSM 640
#define KCPITCH 2080
#define SB0() __builtin_amdgcn_sched_barrier(0)
typedef _Float16 h2_t __attribute__((ext_vector_type(2)));
__device__ __forceinline__ u32 pk_h(float a, float b) {
  f2_t v = {a, b};
  h2_t r = __builtin_convertvector(v, h2_t);
  return __builtin_bit_cast(u32, r);
}
__device__ __forceinline__ void glds16s(u32 voff, const void* sbase, const void* lp) {
  unsigned keep;
  unsigned lds_dst = (unsigned)(size_t)(const __attribute__((address_space(3))) char*)lp;
  asm volatile("s_mov_b32 %0, m0\n\ts_mov_b32 m0, %3\n\ts_nop 0\n\tglobal_load_lds_dwordx4 %1, %2\n\ts_mov_b32 m0, %0"
               : "=&s"(keep) : "v"(voff), "s"(sbase), "s"(lds_dst) : "memory");
}
#define MFMA_BF(acc, a, b) \
  asm volatile("v_mfma_f32_16x16x32_bf16 %0, %1, %2, %0" : "+v"(acc) : "v"(a), "v"(b))
#define MFMA_BF0(acc, a, b) \
  asm volatile("v_mfma_f32_16x16x32_bf16 %0, %1, %2, 0" : "=&v"(acc) : "v"(a), "v"(b))
#define GLOAD4(dst, voff, sbase) \
  asm volatile("global_load_dwordx4 %0, %1, %2" : "=v"(dst) : "v"(voff), "s"(sbase) : "memory")

__global__ __launch_bounds__(256, 4) void k2_kernel(const u16* __restrict__ xnP, const u16* __restrict__ w1P,
                                                    const u16* __restrict__ b1, const u16* __restrict__ lut,
                                                    u16* __restrict__ actP,
                                                    const u16* __restrict__ lar, const u16* __restrict__ aim,
                                                    const u16* __restrict__ cre, const u16* __restrict__ cim,
                                                    const u16* __restrict__ logdt, u16* __restrict__ yT) {
  __shared__ __attribute__((aligned(1024))) char shm[40960];
  const int t = threadIdx.x, lane = t & 63;
  const int wave = __builtin_amdgcn_readfirstlane(t >> 6);
  STAMPB(2, (char*)actP - (16u << 20));
  STAMP(0);
  if (blockIdx.x >= K2_G1) {
    const int sb = blockIdx.x - K2_G1;
    const int li = sb >> 3;
    const int grp = (sb & 7) * 10 + (li >> 3), ee = li & 7;
    const int kb = grp >> 2, gg = grp & 3;
    const int d = kb * 32 + (ee >> 2) * 16 + gg * 4 + (ee & 3);
    const int n = t & 127;
    u16* P = (u16*)(shm + 34816);
    u16* xs = (u16*)(shm + 36352);
    u16* Kc = (u16*)shm;
    const u16 r_dt = logdt[d];
    const u16 r_ar = lar[d * NS + n], r_ai = aim[d * NS + n];
    const u16 r_cr = cre[d * NS + n], r_ci = cim[d * NS + n];
    const u16 r_cr2 = cre[d * NS + (n ^ 64)], r_ci2 = cim[d * NS + (n ^ 64)];
    u16 xv[8];
    {
      const u16* xp = xnP + (((size_t)(t >> 3) * 20 + kb) * 64 + gg * 16 + ((2 * t) & 15)) * 8 + ee;
#pragma unroll
      for (int i = 0; i < 4; ++i) {
        xv[2 * i] = xp[(size_t)i * 327680];
        xv[2 * i + 1] = xp[(size_t)i * 327680 + 8];
      }
    }
    const float dt = bfr(expf(bf2f(r_dt)));
    const float ar = -bfr(expf(bf2f(r_ar))) * dt;
    const float ai = bf2f(r_ai) * dt;
    float cr = bf2f(r_cr) * dt, ci = bf2f(r_ci) * dt;
    float bs = (fabsf(cr) + fabsf(ci)) + (fabsf(bf2f(r_cr2) * dt) + fabsf(bf2f(r_ci2) * dt));
#pragma unroll
    for (int o = 1; o < 64; o <<= 1) bs += __shfl_xor(bs, o);
    const float bound = 2.0f * bs;
    int ex = 0;
    if (bound > 0.f && bound < 3.0e38f) { (void)frexpf(bound, &ex); }
    int es = 15 - ex;
    es = es > 100 ? 100 : (es < -100 ? -100 : es);
    const float sc2 = ldexpf(2.0f, es);
    const float kscale = ldexpf(1.0f, -es);
    cr *= sc2; ci *= sc2;
    const float arl2 = ar * 1.4426950408889634f;
    const float w = ai * 0.15915494309189535f;
    STAMP(1);
    char* wr = shm + n * 4;
    float eHr, eHi;
    cexp_p(arl2, w, 128.0f, eHr, eHi);
    if (wave < 2) {
      float e8r, e8i, e4r, e4i, gr, gi;
      cexp_p(arl2, w, 8.0f, e8r, e8i);
      cexp_p(arl2, w, 4.0f, e4r, e4i);
      cexp_p(arl2, w, 512.0f, gr, gi);
      cmul(gr, gi, cr, ci, gr, gi);
      float lr = 1.f, lim = 0.f;
      *(u32*)(wr) = pk_h(1.f, 0.f);
#pragma unroll
      for (int j = 1; j < 16; ++j) {
        cmul(lr, lim, e8r, e8i, lr, lim);
        *(u32*)(wr + j * WPITCH) = pk_h(lr, -lim);
        if (j < 5) {
          float hr, hi;
          *(u32*)(wr + (40 + (j - 1) * 2) * WPITCH) = pk_h(gr, gi);
          cmul(gr, gi, e4r, e4i, hr, hi);
          *(u32*)(wr + (41 + (j - 1) * 2) * WPITCH) = pk_h(hr, hi);
          cmul(gr, gi, eHr, eHi, gr, gi);
        } else if (j < 14) {
          *(u32*)(wr + (48 + j - 5) * WPITCH) = pk_h(gr, gi);
          if (j < 13) cmul(gr, gi, eHr, eHi, gr, gi);
        }
      }
    } else {
      float Rr[8], Ri[8];
      Rr[0] = 1.f; Ri[0] = 0.f;
      cexp_p(arl2, w, 1.0f, Rr[1], Ri[1]);
#pragma unroll
      for (int r = 2; r < 8; ++r) cmul(Rr[r - 1], Ri[r - 1], Rr[1], Ri[1], Rr[r], Ri[r]);
      float gr = cr, gi = ci;
#pragma unroll
      for (int ii = 0; ii < 2; ++ii) {
        *(u32*)(wr + (16 + ii * 8) * WPITCH) = pk_h(gr, gi);
#pragma unroll
        for (int r = 1; r < 8; ++r) {
          float hr, hi;
          cmul(gr, gi, Rr[r], Ri[r], hr, hi);
          *(u32*)(wr + (16 + ii * 8 + r) * WPITCH) = pk_h(hr, hi);
        }
        cmul(gr, gi, eHr, eHi, gr, gi);
      }
#pragma unroll
      for (int ii = 2; ii < 4; ++ii) {
        *(u32*)(wr + (32 + (ii - 2) * 4) * WPITCH) = pk_h(gr, gi);
#pragma unroll
        for (int r = 1; r < 4; ++r) {
          float hr, hi;
          cmul(gr, gi, Rr[2 * r], Ri[2 * r], hr, hi);
          *(u32*)(wr + (32 + (ii - 2) * 4 + r) * WPITCH) = pk_h(hr, hi);
        }
        if (ii < 3) cmul(gr, gi, eHr, eHi, gr, gi);
      }
    }
    if (t < 32) *(u32x4*)&xs[t * 8] = (u32x4){0u, 0u, 0u, 0u};
#pragma unroll
    for (int i = 0; i < 4; ++i)
      *(u32*)&xs[256 + 2 * t + 512 * i] = pk_h(bf2f(xv[2 * i]), bf2f(xv[2 * i + 1]));
    __syncthreads();
    STAMP(2);
    const int b = lane & 15, g = lane >> 4;
    if (wave < 3) {
      const char* ra = shm + (16 + 16 * wave + b) * WPITCH + g * 16;
      const char* rb = shm + b * WPITCH + g * 16;
      f4 ka = (f4){0.f, 0.f, 0.f, 0.f};
#pragma unroll
      for (int k8 = 0; k8 < 8; ++k8) {
        h8 a = *(const h8*)(ra + k8 * 64);
        h8 bb = *(const h8*)(rb + k8 * 64);
        ka = __builtin_amdgcn_mfma_f32_16x16x32_f16(a, bb, ka, 0, 0, 0);
      }
      const u32 k01 = pk_h(ka[0], ka[1]), k23 = pk_h(ka[2], ka[3]);
      if (wave == 0) {
        *(u32x2*)&P[(g >> 1) * 128 + (g & 1) * 4 + 8 * b] = (u32x2){k01, k23};
      } else if (wave == 1) {
        const int i1 = g < 2 ? 256 + 64 * g + 4 * b : 384 + 64 * (g - 2) + 2 * b;
        *(u32*)&P[i1] = k01;
        *(u32*)&P[i1 + (g < 2 ? 2 : 32)] = k23;
      } else {
        const int i2 = 512 + 64 * g + b;
        P[i2] = (u16)k01; P[i2 + 16] = (u16)(k01 >> 16);
        P[i2 + 32] = (u16)k23; P[i2 + 48] = (u16)(k23 >> 16);
      }
    }
    __syncthreads();
    STAMP(3);
    {
      u32 wv[6];
#pragma unroll
      for (int hh = 0; hh < 2; ++hh) {
        const int m = 255 - t - hh;
        const int sg = m >= 128 ? 3 : (m >= 64 ? 2 : (m >= 32 ? 1 : 0));
        const int start = sg == 0 ? 0 : (128 << sg);
        const int base = sg == 0 ? 0 : 128 + 128 * sg;
        const int one = sg ? 1 : 0;
        const int hm = ((1 << sg) >> 1) - one;
#pragma unroll
        for (int j = 0; j < (hh ? 2 : 4); ++j) {
          u32 pk = 0;
#pragma unroll
          for (int h = 0; h < 2; ++h) {
            if (hh == 0 || 2 * j + h < 3) {
              int q = 8 * m + 7 - (2 * j + h) - start;
              q = q < 0 ? 0 : q;
              int idx = base + ((q + hm + ((q >> sg) & one)) >> sg);
              pk |= ((u32)P[idx]) << (16 * h);
            }
          }
          wv[hh * 4 + j] = (hh && m < 0) ? 0u : pk;
        }
      }
      *(u32x4*)&Kc[0 * KCPITCH + t * 8] = (u32x4){wv[0], wv[1], wv[2], wv[3]};
      *(u32x4*)&Kc[1 * KCPITCH + t * 8] = (u32x4){__builtin_amdgcn_alignbit(wv[1], wv[0], 16), __builtin_amdgcn_alignbit(wv[2], wv[1], 16),
                                                  __builtin_amdgcn_alignbit(wv[3], wv[2], 16), __builtin_amdgcn_alignbit(wv[4], wv[3], 16)};
      *(u32x4*)&Kc[2 * KCPITCH + t * 8] = (u32x4){wv[1], wv[2], wv[3], wv[4]};
      *(u32x4*)&Kc[3 * KCPITCH + t * 8] = (u32x4){__builtin_amdgcn_alignbit(wv[2], wv[1], 16), __builtin_amdgcn_alignbit(wv[3], wv[2], 16),
                                                  __builtin_amdgcn_alignbit(wv[4], wv[3], 16), __builtin_amdgcn_alignbit(wv[5], wv[4], 16)};
      if (t < 8) *(u32x4*)&Kc[(t >> 1) * KCPITCH + (256 + (t & 1)) * 8] = (u32x4){0u, 0u, 0u, 0u};
    }
    __syncthreads();
    STAMP(4);
    f4 acc[8];
#pragma unroll
    for (int i = 0; i < 8; ++i) acc[i] = (f4){0.f, 0.f, 0.f, 0.f};
    const int abase = ((3 - b) & 3) * KCPITCH + 2028 - 4 * (b >> 2) + 8 * g;
    const int bbase = 256 - 16 + 16 * b + 8 * g;
    const u32 kc_lds = (u32)(size_t)(const __attribute__((address_space(3))) char*)shm;
#pragma unroll
    for (int rr = 0; rr < 2; ++rr) {
      const int rp = wave * 2 + rr;
      h8 A[8], B[8];
      u32x2 a0[8], a1[8];
      const u32 aaddr = kc_lds + (u32)(abase - 32 * rp - 256 * 7) * 2;
#pragma unroll
      for (int u = 0; u < 8; ++u) {
        asm volatile("ds_read_b64 %0, %1 offset:%2" : "=v"(a0[u]) : "v"(aaddr), "n"((7 - u) * 512) : "memory");
        asm volatile("ds_read_b64 %0, %1 offset:%2" : "=v"(a1[u]) : "v"(aaddr), "n"((7 - u) * 512 + 8) : "memory");
      }
#pragma unroll
      for (int u = 0; u < 8; ++u) B[u] = *(const h8*)&xs[bbase - 32 * rp + 256 * u];
      asm volatile("s_waitcnt lgkmcnt(0)"
                   : "+v"(a0[0]), "+v"(a1[0]), "+v"(a0[1]), "+v"(a1[1]), "+v"(a0[2]), "+v"(a1[2]), "+v"(a0[3]), "+v"(a1[3]),
                     "+v"(a0[4]), "+v"(a1[4]), "+v"(a0[5]), "+v"(a1[5]), "+v"(a0[6]), "+v"(a1[6]), "+v"(a0[7]), "+v"(a1[7])
                   :: "memory");
#pragma unroll
      for (int u = 0; u < 8; ++u) A[u] = __builtin_bit_cast(h8, (u32x4){a0[u][0], a0[u][1], a1[u][0], a1[u][1]});
#pragma unroll
      for (int u = 0; u < 8; ++u)
#pragma unroll
        for (int v = 0; v + u < 8; ++v)
          acc[u + v] = __builtin_amdgcn_mfma_f32_16x16x32_f16(A[u], B[v], acc[u + v], 0, 0, 0);
    }
    __syncthreads();
    STAMP(5);
    float* red = (float*)shm;
#pragma unroll
    for (int tt = 0; tt < 8; ++tt) *(f4*)&red[((wave * 8 + tt) * 64 + lane) * 4] = acc[tt];
    __syncthreads();
#pragma unroll
    for (int i = 0; i < 2; ++i) {
      const int tt = wave * 2 + i;
      f4 s0 = *(const f4*)&red[((0 * 8 + tt) * 64 + lane) * 4];
      f4 s1 = *(const f4*)&red[((1 * 8 + tt) * 64 + lane) * 4];
      f4 s2 = *(const f4*)&red[((2 * 8 + tt) * 64 + lane) * 4];
      f4 s3 = *(const f4*)&red[((3 * 8 + tt) * 64 + lane) * 4];
      f4 sum = ((s0 + s1) + (s2 + s3)) * kscale;
      u32x2 o = {pk_bf(sum[0], sum[1]), pk_bf(sum[2], sum[3])};
      st8(yT + (size_t)d * SEQ + 256 * tt + 16 * b + 4 * g, o);
    }
    STAMP(8);
    return;
  }
  constexpr int ND = 5;
  constexpr int STG = 7 * 1024;
  constexpr int NKT = DM / 32;
  const int xcd = blockIdx.x & 7, idx = blockIdx.x >> 3;
  const int mi = idx & 3, ni = idx >> 2;
  const int bm = (xcd >> 1) * 4 + mi, bn = (xcd & 1) * 8 + ni;
  const int mt0 = bm * 8, nt0 = bn * 7;
  const char* abase = (const char*)xnP + (size_t)(mt0 + 2 * wave) * (20 * 1024);
  const u32 va0 = lane * 16, va1 = lane * 16 + 20 * 1024;
  const int p0 = 2 * wave, p1 = wave < 3 ? 2 * wave + 1 : 6;
  const u32 vb0 = lane * 16 + (u32)(nt0 + p0 > 107 ? 107 : nt0 + p0) * (20 * 1024);
  const u32 vb1 = lane * 16 + (u32)(nt0 + p1 > 107 ? 107 : nt0 + p1) * (20 * 1024);
  auto stage = [&](int s) {
    char* sb = shm + (s % ND) * STG;
    const char* bp = (const char*)w1P + s * 1024;
    glds16s(vb0, bp, sb + p0 * 1024);
    if (wave < 3) glds16s(vb1, bp, sb + p1 * 1024);
  };
  u32x4 fa[3][2];
  auto loadA = [&](int s) {
    const char* ap = abase + s * 1024;
    GLOAD4(fa[s % 3][0], va0, ap);
    GLOAD4(fa[s % 3][1], va1, ap);
  };
  f4 acc[2][7];
  s8 fb[7];
  stage(0); stage(1); loadA(0); stage(2); loadA(1); stage(3); loadA(2);
  STAMP(1);
  if (wave < 3) WAIT_V(12); else WAIT_V(9);
  __builtin_amdgcn_s_barrier();
  STAMP(2);
#pragma unroll
  for (int j = 0; j < 7; ++j) fb[j] = *(const s8*)(shm + j * 1024 + lane * 16);
#pragma unroll
  for (int kt = 0; kt < NKT; ++kt) {
    {
      constexpr int dummy = 0; (void)dummy;
      const int g2 = ((kt - 2 <= 17) ? 1 : 0) + ((kt - 1 <= 17) ? 1 : 0);
      const int a2 = ((kt - 2 <= 16) ? 2 : 0) + ((kt - 1 <= 16) ? 2 : 0);
      if (wave < 3) { switch (2 * g2 + a2) { case 8: WAIT_V(8); break; case 6: WAIT_V(6); break; default: WAIT_V(2); break; } }
      else { switch (g2 + a2) { case 6: WAIT_V(6); break; case 4: WAIT_V(4); break; default: WAIT_V(1); break; } }
      asm volatile("" : "+v"(fa[kt % 3][0]), "+v"(fa[kt % 3][1]));
    }
    __builtin_amdgcn_s_barrier();
    if (kt + ND - 1 < NKT) stage(kt + ND - 1);
    else if (kt == NKT - ND + 1) {
      glds16s(lane * 16, (const char*)lut + p0 * 1024, shm + p0 * 1024);
      if (wave < 3) glds16s(lane * 16, (const char*)lut + p1 * 1024, shm + p1 * 1024);
    } else if (kt == NKT - ND + 2) {
      int o = nt0 * 16 + (lane < 14 ? lane : 13) * 8; if (o > FF - 8) o = FF - 8;
      if (wave < 3) glds16s(lane * 16, (const char*)lut + (7 + p0) * 1024, shm + (7 + p0) * 1024);
      if (wave < 2) glds16s(lane * 16, (const char*)lut + (7 + p1) * 1024, shm + (7 + p1) * 1024);
      else glds16s(o * 2, (const char*)b1, shm + 12 * 1024);
    }
    SB0();
#pragma unroll
    for (int j = 0; j < 7; ++j) {
      if (kt == 0) {
        MFMA_BF0(acc[0][j], fb[j], fa[0][0]);
        MFMA_BF0(acc[1][j], fb[j], fa[0][1]);
      } else {
        MFMA_BF(acc[0][j], fb[j], fa[kt % 3][0]);
        MFMA_BF(acc[1][j], fb[j], fa[kt % 3][1]);
      }
      if (kt + 1 < NKT) fb[j] = *(const s8*)(shm + ((kt + 1) % ND) * STG + j * 1024 + lane * 16);
      SB0();
    }
    if (kt + 3 < NKT) loadA(kt + 3);
    SB0();
    if (kt == 4) STAMP(3);
    if (kt == 12) STAMP(4);
  }
  asm volatile("s_nop 7\n\ts_nop 7\n\ts_nop 7" ::: "memory");
  WAIT_V(0);
  __builtin_amdgcn_s_barrier();
  STAMP(7);
  const int g = lane >> 4;
  const u16* slut = (const u16*)(shm);
  const u16* sbias = (const u16*)(shm + 12 * 1024);
#pragma unroll
  for (int j = 0; j < 7; ++j) {
    const int nt = nt0 + j;
    if (nt < 108) {
      u32x2 bb = *(const u32x2*)(sbias + j * 16 + 4 * g);
      const float bv[4] = {lo_f(bb[0]), hi_f(bb[0]), lo_f(bb[1]), hi_f(bb[1])};
      const int kb = nt >> 1, half = nt & 1;
#pragma unroll
      for (int i = 0; i < 2; ++i) {
        u32 t0 = pk_bf(acc[i][j][0], acc[i][j][1]);
        u32 t1 = pk_bf(acc[i][j][2], acc[i][j][3]);
        u32 h0 = pk_bf(lo_f(t0) + bv[0], hi_f(t0) + bv[1]);
        u32 h1 = pk_bf(lo_f(t1) + bv[2], hi_f(t1) + bv[3]);
        u32 hb[4] = {h0 & 0xFFFFu, h0 >> 16, h1 & 0xFFFFu, h1 >> 16};
        u32 res[4];
        u32 mx = 0;
#pragma unroll
        for (int r = 0; r < 4; ++r) {
          u32 lx = (hb[r] & 0x7FFFu) - (LUT_E0 << 7);
          mx = mx > lx ? mx : lx;
          res[r] = slut[(lx & 0x1FFFu) + (hb[r] >> 15) * LUT_HALF];
        }
        if (__builtin_amdgcn_ballot_w64(mx >= (u32)LUT_HALF) != 0) {
#pragma unroll
          for (int r = 0; r < 4; ++r) {
            u32 a = hb[r] & 0x7FFFu;
            bool in = (a - (LUT_E0 << 7)) < (u32)LUT_HALF;
            float h = __uint_as_float(hb[r] << 16);
            float fbv = (a < (LUT_E0 << 7)) ? h * 0.59765625f : (h > 0.f ? h : h * 0.0f);
            u32 fbb = pk_bf(fbv, fbv) & 0xFFFFu;
            res[r] = in ? res[r] : fbb;
          }
        }
        u32x2 ov = {res[0] | (res[1] << 16), res[2] | (res[3] << 16)};
        const int mt = mt0 + wave * 2 + i;
        st8(actP + (((size_t)mt * 54 + kb) * 64 + lane) * 8 + half * 4, ov);
      }
    }
  }
  STAMP(8);
}

__global__ __launch_bounds__(256, 1) void k3_kernel(const u16* __restrict__ actP, const u16* __restrict__ w2P,
                                                    const u16* __restrict__ xin, const float2* __restrict__ stats,
                                                    const u16* __restrict__ yT, const u16* __restrict__ dskip,
                                                    u16* __restrict__ out) {
  constexpr int R = 5;
  constexpr int NK = FF / 32;
  constexpr int NST = 14;
  __shared__ __attribute__((aligned(1024))) char shm[21 * 1024 + 60 * 1024];
  const int t = threadIdx.x, lane = t & 63;
  const int wave = __builtin_amdgcn_readfirstlane(t >> 6);
  const int xcd = blockIdx.x & 7, idx = blockIdx.x >> 3;
  const int mi = idx & 7, ni = idx >> 3;
  const int bm = (xcd >> 1) * 8 + mi, bn = (xcd & 1) * 4 + ni;
#define PH3 0
  const int phase = PH3 == 0 ? 0 : (PH3 == 1 ? ((27 * mi + 54 * ni) >> 2) % NK : (PH3 == 2 ? (mi + 2 * ni) : (xcd * 6)));
  const int mt0 = bm * 4, m0 = bm * 64, n0 = bn * 80, nt0 = bn * 5;
  STAMPB(3, (char*)actP - (16u << 20));
  STAMP(0);
#pragma unroll
  for (int i = 0; i < 6; ++i) {
    int p = wave + 4 * i;
    if (p > 20) p = 20;
    int c = (p < 10 ? p : p - 10) * 64 + lane;
    const u16* src;
    if (p < 10) src = xin + (size_t)(m0 + c / 10) * DM + n0 + (c % 10) * 8;
    else if (p < 20) src = yT + (size_t)(n0 + (c >> 3)) * SEQ + m0 + (c & 7) * 8;
    else src = (lane < 32) ? (const u16*)(stats + m0) + lane * 8 : dskip + n0 + (lane < 42 ? lane - 32 : 9) * 8;
    GLDS(src, shm + p * 1024);
  }
  const char* abase = (const char*)actP + (size_t)mt0 * NK * 1024;
  const char* bbase = (const char*)w2P + (size_t)nt0 * NK * 1024;
  u32 va[4], vb[5];
#pragma unroll
  for (int i = 0; i < 4; ++i) va[i] = lane * 16 + ((wave + i) & 3) * (NK * 1024);
#pragma unroll
  for (int j = 0; j < 5; ++j) vb[j] = lane * 16 + j * (NK * 1024);
  u32x4 fa[R][4], fb[R][5];
  auto issue = [&](int s) {
    int q = (s < NST - 1 || wave < 2) ? wave + 4 * s : wave + 4 * (s - 1);
    int ks = q + phase;
    if (ks >= NK) ks -= NK;
    const char* ap = abase + ks * 1024;
    const char* bp = bbase + ks * 1024;
#pragma unroll
    for (int i = 0; i < 4; ++i) fa[s % R][i] = *(const u32x4*)(ap + va[i]);
#pragma unroll
    for (int j = 0; j < 5; ++j) fb[s % R][j] = *(const u32x4*)(bp + vb[j]);
  };
  SB0();
#pragma unroll
  for (int s = 0; s < R; ++s) { issue(s); SB0(); }
  STAMP(1);
  WAIT_V(R * 9);
  RAW_BARRIER();
  STAMP(2);
  const int fr = lane & 15, g = lane >> 4, tq = (lane & 15) >> 2, tp = lane & 3;
  const int ml = wave * 16 + fr;
  u32x2 s1p[5];
  {
    const char* sx = shm;
    const char* sy = shm + 10 * 1024;
    const char* sm = shm + 20 * 1024;
    const float2 st = *(const float2*)(sm + ml * 8);
#pragma unroll
    for (int j = 0; j < 5; ++j) {
      const int nl = j * 16;
      u32x2 xx = *(const u32x2*)(sx + ml * 160 + (nl + 4 * g) * 2);
      u32x2 dd = *(const u32x2*)(sm + 512 + (nl + 4 * g) * 2);
      s4 yy = TRREAD(sy + (nl + 4 * g + tq) * 128 + (wave * 16 + 4 * tp) * 2);
      const u32x2 yw = __builtin_bit_cast(u32x2, yy);
#pragma unroll
      for (int h = 0; h < 2; ++h) {
        const float x0 = lo_f(xx[h]), x1 = hi_f(xx[h]);
        u32 d = pk_bf(x0 - st.x, x1 - st.x);
        u32 xn = pk_bf(lo_f(d) * st.y, hi_f(d) * st.y);
        u32 tt = pk_bf(lo_f(xn) * lo_f(dd[h]), hi_f(xn) * hi_f(dd[h]));
        u32 a = pk_bf(lo_f(yw[h]) + lo_f(tt), hi_f(yw[h]) + hi_f(tt));
        s1p[j][h] = pk_bf(x0 + lo_f(a), x1 + hi_f(a));
      }
      asm volatile("" : "+v"(s1p[j]));
    }
  }
  f4 acc[4][5];
#pragma unroll
  for (int i = 0; i < 4; ++i)
#pragma unroll
    for (int j = 0; j < 5; ++j) acc[i][j] = (f4){0.f, 0.f, 0.f, 0.f};
  SB0();
  STAMP(3);
#pragma unroll
  for (int s = 0; s < NST; ++s) {
    if (s == NST - 1) {
#pragma unroll
      for (int i = 0; i < 4; ++i) asm volatile("" : "+v"(fa[s % R][i]));
#pragma unroll
      for (int j = 0; j < 5; ++j) asm volatile("" : "+v"(fb[s % R][j]));
    }
    if (s < NST - 1 || wave < 2) {
#pragma unroll
      for (int i = 0; i < 4; ++i)
#pragma unroll
        for (int j = 0; j < 5; ++j)
          acc[i][j] = __builtin_amdgcn_mfma_f32_16x16x32_bf16(__builtin_bit_cast(s8, fb[s % R][j]),
                                                              __builtin_bit_cast(s8, fa[s % R][i]), acc[i][j], 0, 0, 0);
    }
    SB0();
    if (s + R < NST) issue(s + R);
    SB0();
    if (s == 2) STAMP(4);
    if (s == 6) STAMP(5);
    if (s == 10) STAMP(6);
  }
  STAMP(7);
  char* red = shm + 21 * 1024;
#pragma unroll
  for (int i = 1; i < 4; ++i)
#pragma unroll
    for (int j = 0; j < 5; ++j)
      *(f4*)(red + ((((wave + i) & 3) * 3 + (i - 1)) * 5 + j) * 1024 + lane * 16) = acc[i][j];
  __syncthreads();
#pragma unroll
  for (int j = 0; j < 5; ++j) {
    f4 p0 = *(const f4*)(red + ((wave * 3 + 0) * 5 + j) * 1024 + lane * 16);
    f4 p1 = *(const f4*)(red + ((wave * 3 + 1) * 5 + j) * 1024 + lane * 16);
    f4 p2 = *(const f4*)(red + ((wave * 3 + 2) * 5 + j) * 1024 + lane * 16);
    f4 sum = (acc[0][j] + p0) + (p1 + p2);
    u32 m0p = pk_bf(sum[0], sum[1]), m1p = pk_bf(sum[2], sum[3]);
    u32x2 ov = {pk_bf(lo_f(s1p[j][0]) + lo_f(m0p), hi_f(s1p[j][0]) + hi_f(m0p)),
                pk_bf(lo_f(s1p[j][1]) + lo_f(m1p), hi_f(s1p[j][1]) + hi_f(m1p))};
    st8(out + (size_t)(m0 + ml) * DM + n0 + j * 16 + 4 * g, ov);
  }
  STAMP(8);
}

extern "C" void kernel_launch(void* const* d_in, const int* in_sizes, int n_in,
                              void* d_out, int out_size, void* d_ws, size_t ws_size,
                              hipStream_t stream) {
  const u16* u = (const u16*)d_in[0];
  const u16* lar = (const u16*)d_in[1];
  const u16* aim = (const u16*)d_in[2];
  const u16* cre = (const u16*)d_in[3];
  const u16* cim = (const u16*)d_in[4];
  const u16* logdt = (const u16*)d_in[5];
  const u16* dskip = (const u16*)d_in[6];
  const u16* w1 = (const u16*)d_in[8];
  const u16* b1 = (const u16*)d_in[9];
  const u16* w2 = (const u16*)d_in[10];
  u16* out = (u16*)d_out;
  char* ws = (char*)d_ws;
  if (ws_size < (32u << 20)) return;
  u16* xnP = (u16*)(ws);
  u16* w1P = (u16*)(ws + (4u << 20));
  float2* stats = (float2*)(ws + (12u << 20) + 4096);
  u16* lut = (u16*)(ws + (12u << 20) + 32768);
  u16* yT = (u16*)(ws + (13u << 20));
  u16* actP = (u16*)(ws + (16u << 20));
  u16* w2P = (u16*)(ws + (24u << 20));

  k1_kernel<<<K1_LN + 2 * K1_RP + K1_LUT, 256, 0, stream>>>(u, xnP, stats, lut, w1, w1P, w2, w2P);
  k2_kernel<<<K2_G1 + K2_SSM, 256, 0, stream>>>(xnP, w1P, b1, lut, actP, lar, aim, cre, cim, logdt, yT);
  k3_kernel<<<256, 256, 0, stream>>>(actP, w2P, u, stats, yT, dskip, out);
}
```

```cpp
#include <hip/hip_runtime.h>
#include <hip/hip_bf16.h>

#define SEQ 2048
#define DM 640
#define NS 128
#define FF 1728

typedef unsigned short u16;
typedef short s4 __attribute__((ext_vector_type(4)));
typedef short s8 __attribute__((ext_vector_type(8)));
typedef _Float16 h8 __attribute__((ext_vector_type(8)));
typedef float f4 __attribute__((ext_vector_type(4)));
typedef unsigned int u32;
typedef unsigned int u32x4 __attribute__((ext_vector_type(4)));
typedef unsigned int u32x2 __attribute__((ext_vector_type(2)));

__device__ __forceinline__ float bf2f(u16 v) { return __uint_as_float(((u32)v) << 16); }
__device__ __forceinline__ u16 f2bf(float f) {
  u32 u = __float_as_uint(f);
  u32 r = (u + 0x7FFFu + ((u >> 16) & 1u)) >> 16;
  return (u16)r;
}
__device__ __forceinline__ float bfr(float f) { return bf2f(f2bf(f)); }

#define STAMPB(kid, base)
#define STAMP(k)
#define STAMPW()
typedef __bf16 bf2_t __attribute__((ext_vector_type(2)));
typedef float f2_t __attribute__((ext_vector_type(2)));
__device__ __forceinline__ u32 pk_bf(float a, float b) {
  f2_t v = {a, b};
  bf2_t r = __builtin_convertvector(v, bf2_t);
  return __builtin_bit_cast(u32, r);
}
__device__ __forceinline__ float lo_f(u32 w) { return __uint_as_float(w << 16); }
__device__ __forceinline__ float hi_f(u32 w) { return __uint_as_float(w & 0xFFFF0000u); }

#define WAIT_V(n) asm volatile("s_waitcnt vmcnt(%0)" ::"n"(n) : "memory")
#define RAW_BARRIER() do { asm volatile("s_waitcnt lgkmcnt(0)" ::: "memory"); __builtin_amdgcn_s_barrier(); } while (0)
__device__ __forceinline__ void glds16(const void* gsrc, const void* lp) {
  unsigned keep;
  unsigned lds_dst = (unsigned)(size_t)(const __attribute__((address_space(3))) char*)lp;
  asm volatile("s_mov_b32 %0, m0\n\ts_mov_b32 m0, %2\n\ts_nop 0\n\tglobal_load_lds_dwordx4 %1, off\n\ts_mov_b32 m0, %0"
               : "=&s"(keep) : "v"(gsrc), "s"(lds_dst) : "memory");
}
#define GLDS(gp, lp) glds16((const void*)(gp), (const void*)(lp))

#define STF " sc1"
__device__ __forceinline__ void st16(void* p, u32x4 v) { asm volatile("global_store_dwordx4 %0, %1, off" STF :: "v"(p), "v"(v) : "memory"); }
__device__ __forceinline__ void st8(void* p, u32x2 v) { asm volatile("global_store_dwordx2 %0, %1, off" STF :: "v"(p), "v"(v) : "memory"); }
__device__ __forceinline__ void st4(void* p, u32 v) { asm volatile("global_store_dword %0, %1, off" STF :: "v"(p), "v"(v) : "memory"); }
__device__ __forceinline__ void st2(void* p, u32 v) { asm volatile("global_store_short %0, %1, off" STF :: "v"(p), "v"(v) : "memory"); }

#define LUT_E0 111
#define LUT_E1 134
#define LUT_HALF ((LUT_E1 - LUT_E0) * 128)
#define LUT_N (2 * LUT_HALF)

__device__ __forceinline__ float mish_bf(float h) {
  float e = bfr(expf(-fabsf(h)));
  float l = bfr(log1pf(e));
  float sp = bfr(l + (h > 0.f ? h : 0.f));
  float th = bfr(tanhf(sp));
  return bfr(h * th);
}

#define KRLEN 2080
#define WFENCE() do { asm volatile("" ::: "memory"); __builtin_amdgcn_wave_barrier(); asm volatile("" ::: "memory"); } while (0)
__device__ __forceinline__ void cexp_p(float arl2, float w, float p, float& re, float& im) {
  float mag = __builtin_amdgcn_exp2f(arl2 * p);
  float rev = w * p;
  rev = rev - floorf(rev);
  re = mag * __builtin_amdgcn_cosf(rev);
  im = mag * __builtin_amdgcn_sinf(rev);
}
__device__ __forceinline__ void cmul(float ar, float ai, float br, float bi, float& cr, float& ci) {
  float r = fmaf(ar, br, -(ai * bi));
  float i = fmaf(ar, bi, ai * br);
  cr = r; ci = i;
}
__device__ __forceinline__ u32x2 split2(float a, float b) {
  u32 h = pk_bf(a, b);
  u32 l = pk_bf(a - lo_f(h), b - hi_f(h));
  return (u32x2){h, l};
}

#define K1_LN 256
#define K1_KG 640
#define K1_RP 135
#define K1_RQ 160
#define PPITCH 768
#define K1_LUT 24
#define WPITCH 544
typedef _Float16 h2_t __attribute__((ext_vector_type(2)));
__device__ __forceinline__ u32 pk_h(float a, float b) {
  f2_t v = {a, b};
  h2_t r = __builtin_convertvector(v, h2_t);
  return __builtin_bit_cast(u32, r);
}
#define TRREAD(p) __builtin_amdgcn_ds_read_tr16_b64_v4i16((__attribute__((address_space(3))) s4*)(p))

__device__ __forceinline__ void repack_load(const u16* __restrict__ w, int N, int ku, int nu, char* lds, int tl) {
#pragma unroll
  for (int i = 0; i < 2; ++i) {
    int idx = tl + 256 * i, r = idx >> 3, c = idx & 7;
    u32x4 v = *(const u32x4*)(w + (size_t)(ku * 64 + r) * N + nu * 64 + c * 8);
    *(u32x4*)(lds + r * 160 + c * 16) = v;
  }
}
__device__ __forceinline__ void repack_store(u16* __restrict__ wP, int KB, int ku, int nu, const char* lds, int tl) {
  const int wl = tl >> 6, lane = tl & 63, g = lane >> 4, tq = (lane & 15) >> 2, tp = lane & 3;
#pragma unroll
  for (int kb2 = 0; kb2 < 2; ++kb2) {
    const char* p0 = lds + (kb2 * 32 + 4 * g + tq) * 160 + wl * 32 + tp * 8;
    s4 b0 = TRREAD(p0);
    s4 b1 = TRREAD(p0 + 16 * 160);
    s8 bf = {b0[0], b0[1], b0[2], b0[3], b1[0], b1[1], b1[2], b1[3]};
    st16(wP + (((size_t)(nu * 4 + wl) * KB + ku * 2 + kb2) * 64 + lane) * 8, __builtin_bit_cast(u32x4, bf));
  }
}

__global__ __launch_bounds__(256) void k1_kernel(const u16* __restrict__ u, u16* __restrict__ xnP,
                                                 float2* __restrict__ stats, u16* __restrict__ lut,
                                                 const u16* __restrict__ w1, u16* __restrict__ w1P,
                                                 const u16* __restrict__ w2, u16* __restrict__ w2F, u16* __restrict__ w2H,
                                                 const u16* __restrict__ b1, float* __restrict__ b1f,
                                                 const u16* __restrict__ lar, const u16* __restrict__ aim,
                                                 const u16* __restrict__ cre, const u16* __restrict__ cim,
                                                 const u16* __restrict__ logdt, u16* __restrict__ Pg) {
  __shared__ __attribute__((aligned(16))) char k1s[57 * WPITCH];
  const int t = threadIdx.x, lane = t & 63;
  const int wave = __builtin_amdgcn_readfirstlane(t >> 6);
  STAMPB(0, xnP);
  STAMP(0);
  if (blockIdx.x < K1_LN) {
    u16 (*tile)[DM + 8] = (u16 (*)[DM + 8])k1s;
    const int r = wave * 2 + (lane >> 5), q = lane & 31;
    const int rg = blockIdx.x;
    const int row = rg * 8 + r;
    float v[20];
    const u32x2* src = (const u32x2*)(u + (size_t)row * DM);
#pragma unroll
    for (int i = 0; i < 5; ++i) {
      u32x2 w = src[q + 32 * i];
      v[i * 4 + 0] = lo_f(w[0]); v[i * 4 + 1] = hi_f(w[0]);
      v[i * 4 + 2] = lo_f(w[1]); v[i * 4 + 3] = hi_f(w[1]);
    }
    float s = 0.f;
#pragma unroll
    for (int i = 0; i < 20; ++i) s += v[i];
    STAMP(1);
#pragma unroll
    for (int o = 1; o < 32; o <<= 1) s += __shfl_xor(s, o);
    const float mean = s * (1.0f / DM);
    float ss = 0.f;
#pragma unroll
    for (int i = 0; i < 20; ++i) { float d = v[i] - mean; ss += d * d; }
#pragma unroll
    for (int o = 1; o < 32; o <<= 1) ss += __shfl_xor(ss, o);
    const float var = ss * (1.0f / DM);
    const float mb = bfr(mean);
    const float vb = bfr(var);
    const float den = bfr(sqrtf(bfr(vb + 1e-5f)));
    const float rden = 1.0f / den;
    if (q == 0) st8(stats + row, (u32x2){__float_as_uint(mb), __float_as_uint(rden)});
#pragma unroll
    for (int i = 0; i < 5; ++i) {
      u32 d0 = pk_bf(v[i * 4 + 0] - mb, v[i * 4 + 1] - mb);
      u32 d1 = pk_bf(v[i * 4 + 2] - mb, v[i * 4 + 3] - mb);
      u32x2 w = {pk_bf(lo_f(d0) * rden, hi_f(d0) * rden), pk_bf(lo_f(d1) * rden, hi_f(d1) * rden)};
      *(u32x2*)&tile[r][(q + 32 * i) * 4] = w;
    }
    __syncthreads();
    STAMP(2);
    const int mt = rg >> 1, half = rg & 1;
#pragma unroll
    for (int i = 0; i < 3; ++i) {
      int idx = t + 256 * i;
      if (idx < 640) {
        int kb = idx >> 5, g = (idx >> 3) & 3, fr = idx & 7;
        u32x2 a = *(const u32x2*)&tile[fr][kb * 32 + 4 * g];
        u32x2 b = *(const u32x2*)&tile[fr][kb * 32 + 16 + 4 * g];
        u32x4 w = {a[0], a[1], b[0], b[1]};
        st16(xnP + (((size_t)mt * 20 + kb) * 64 + g * 16 + half * 8 + fr) * 8, w);
      }
    }
    STAMP(3);
    STAMPW();
    return;
  }
  if (blockIdx.x < K1_LN + K1_KG) {
    const int d = blockIdx.x - K1_LN;
    const int n = t & 127;
    char* shm = k1s;
    const u16 r_dt = logdt[d];
    const u16 r_ar = lar[d * NS + n], r_ai = aim[d * NS + n];
    const u16 r_cr = cre[d * NS + n], r_ci = cim[d * NS + n];
    const u16 r_cr2 = cre[d * NS + (n ^ 64)], r_ci2 = cim[d * NS + (n ^ 64)];
    const float dt = bfr(expf(bf2f(r_dt)));
    const float ar = -bfr(expf(bf2f(r_ar))) * dt;
    const float ai = bf2f(r_ai) * dt;
    float cr = bf2f(r_cr) * dt, ci = bf2f(r_ci) * dt;
    float bs = (fabsf(cr) + fabsf(ci)) + (fabsf(bf2f(r_cr2) * dt) + fabsf(bf2f(r_ci2) * dt));
#pragma unroll
    for (int o = 1; o < 64; o <<= 1) bs += __shfl_xor(bs, o);
    const float bound = 2.0f * bs;
    int ex = 0;
    if (bound > 0.f && bound < 3.0e38f) { (void)frexpf(bound, &ex); }
    int es = 15 - ex;
    es = es > 100 ? 100 : (es < -100 ? -100 : es);
    const float sc2 = ldexpf(2.0f, es);
    const float kscale = ldexpf(1.0f, -es);
    cr *= sc2; ci *= sc2;
    const float arl2 = ar * 1.4426950408889634f;
    const float w = ai * 0.15915494309189535f;
    STAMP(1);
    char* wr = shm + n * 4;
    float eHr, eHi;
    cexp_p(arl2, w, 128.0f, eHr, eHi);
    if (wave < 2) {
      float e8r, e8i, e4r, e4i, gr, gi;
      cexp_p(arl2, w, 8.0f, e8r, e8i);
      cexp_p(arl2, w, 4.0f, e4r, e4i);
      cexp_p(arl2, w, 512.0f, gr, gi);
      cmul(gr, gi, cr, ci, gr, gi);
      float lr = 1.f, lim = 0.f;
      *(u32*)(wr) = pk_h(1.f, 0.f);
#pragma unroll
      for (int j = 1; j < 16; ++j) {
        cmul(lr, lim, e8r, e8i, lr, lim);
        *(u32*)(wr + j * WPITCH) = pk_h(lr, -lim);
        if (j < 5) {
          float hr, hi;
          *(u32*)(wr + (40 + (j - 1) * 2) * WPITCH) = pk_h(gr, gi);
          cmul(gr, gi, e4r, e4i, hr, hi);
          *(u32*)(wr + (41 + (j - 1) * 2) * WPITCH) = pk_h(hr, hi);
          cmul(gr, gi, eHr, eHi, gr, gi);
        } else if (j < 14) {
          *(u32*)(wr + (48 + j - 5) * WPITCH) = pk_h(gr, gi);
          if (j < 13) cmul(gr, gi, eHr, eHi, gr, gi);
        }
      }
    } else {
      float Rr[8], Ri[8];
      Rr[0] = 1.f; Ri[0] = 0.f;
      cexp_p(arl2, w, 1.0f, Rr[1], Ri[1]);
#pragma unroll
      for (int r = 2; r < 8; ++r) cmul(Rr[r - 1], Ri[r - 1], Rr[1], Ri[1], Rr[r], Ri[r]);
      float gr = cr, gi = ci;
#pragma unroll
      for (int ii = 0; ii < 2; ++ii) {
        *(u32*)(wr + (16 + ii * 8) * WPITCH) = pk_h(gr, gi);
#pragma unroll
        for (int r = 1; r < 8; ++r) {
          float hr, hi;
          cmul(gr, gi, Rr[r], Ri[r], hr, hi);
          *(u32*)(wr + (16 + ii * 8 + r) * WPITCH) = pk_h(hr, hi);
        }
        cmul(gr, gi, eHr, eHi, gr, gi);
      }
#pragma unroll
      for (int ii = 2; ii < 4; ++ii) {
        *(u32*)(wr + (32 + (ii - 2) * 4) * WPITCH) = pk_h(gr, gi);
#pragma unroll
        for (int r = 1; r < 4; ++r) {
          float hr, hi;
          cmul(gr, gi, Rr[2 * r], Ri[2 * r], hr, hi);
          *(u32*)(wr + (32 + (ii - 2) * 4 + r) * WPITCH) = pk_h(hr, hi);
        }
        if (ii < 3) cmul(gr, gi, eHr, eHi, gr, gi);
      }
    }
    __syncthreads();
    STAMP(2);
    const int b = lane & 15, g = lane >> 4;
    u16* P = Pg + (size_t)d * PPITCH;
    if (wave < 3) {
      const int rowa = 16 + 16 * wave + b;
      const char* ra = shm + (rowa > 56 ? 56 : rowa) * WPITCH + g * 16;
      const char* rb = shm + b * WPITCH + g * 16;
      f4 ka = (f4){0.f, 0.f, 0.f, 0.f};
#pragma unroll
      for (int k8 = 0; k8 < 8; ++k8) {
        h8 a = *(const h8*)(ra + k8 * 64);
        h8 bb = *(const h8*)(rb + k8 * 64);
        ka = __builtin_amdgcn_mfma_f32_16x16x32_f16(a, bb, ka, 0, 0, 0);
      }
      const u32 k01 = pk_h(ka[0], ka[1]), k23 = pk_h(ka[2], ka[3]);
      if (wave == 0) {
        st8(&P[(g >> 1) * 128 + (g & 1) * 4 + 8 * b], (u32x2){k01, k23});
      } else if (wave == 1) {
        const int i1 = g < 2 ? 256 + 64 * g + 4 * b : 384 + 64 * (g - 2) + 2 * b;
        st4(&P[i1], k01);
        st4(&P[i1 + (g < 2 ? 2 : 32)], k23);
      } else {
        const int i2 = 512 + 64 * g + b;
        if (g < 3) st2(&P[i2], k01);
        if (g < 2) {
          st2(&P[i2 + 16], k01 >> 16);
          st2(&P[i2 + 32], k23); st2(&P[i2 + 48], k23 >> 16);
        }
      }
    } else if (lane == 0) {
      st4(&P[704], __float_as_uint(kscale));
    }
    STAMP(3);
    STAMPW();
    return;
  }
  if (blockIdx.x < K1_LN + K1_KG + K1_RP) {
    const int u0 = (blockIdx.x - (K1_LN + K1_KG)) * 2;
    repack_load(w1, FF, u0 / 27, u0 % 27, k1s, t);
    repack_load(w1, FF, (u0 + 1) / 27, (u0 + 1) % 27, k1s + 10240, t);
    __syncthreads();
    repack_store(w1P, DM / 32, u0 / 27, u0 % 27, k1s, t);
    repack_store(w1P, DM / 32, (u0 + 1) / 27, (u0 + 1) % 27, k1s + 10240, t);
    STAMP(3);
    STAMPW();
    return;
  }
  if (blockIdx.x < K1_LN + K1_KG + K1_RP + K1_RQ) {
    const int un = blockIdx.x - (K1_LN + K1_KG + K1_RP);
    const int bn = un / 10, nu = un % 10;
    const int f0 = 16 * (27 * (bn >> 2) + 7 * (bn & 3));
    const int rows = (bn & 3) == 3 ? 96 : 112;
    u32x4 v[4];
#pragma unroll
    for (int i = 0; i < 4; ++i) {
      const int r = (t >> 3) + 32 * i;
      v[i] = *(const u32x4*)(w2 + (size_t)(f0 + (r < rows ? r : rows - 1)) * DM + nu * 64 + (t & 7) * 8);
    }
#pragma unroll
    for (int i = 0; i < 4; ++i) *(u32x4*)(k1s + ((t >> 3) + 32 * i) * 160 + (t & 7) * 16) = v[i];
    __syncthreads();
    const int g = lane >> 4, tq = (lane & 15) >> 2, tp = lane & 3;
    const int nt = nu * 4 + wave;
    const char* p0 = k1s + (4 * g + tq) * 160 + wave * 32 + tp * 8;
#pragma unroll
    for (int p = 0; p < 3; ++p) {
      s4 b0 = TRREAD(p0 + (32 * p) * 160);
      s4 b1v = TRREAD(p0 + (32 * p + 16) * 160);
      s8 bf = {b0[0], b0[1], b0[2], b0[3], b1v[0], b1v[1], b1v[2], b1v[3]};
      st16(w2F + (((size_t)nt * 48 + 3 * bn + p) * 64 + lane) * 8, __builtin_bit_cast(u32x4, bf));
    }
    if (rows == 112) {
      s4 b0 = TRREAD(p0 + 96 * 160);
      st8(w2H + (((size_t)nt * 12 + 3 * (bn >> 2) + (bn & 3)) * 64 + lane) * 4, __builtin_bit_cast(u32x2, b0));
    }
    STAMP(3);
    STAMPW();
    return;
  }
  {
    const int idx = (blockIdx.x - (K1_LN + K1_KG + K1_RP + K1_RQ)) * 256 + t;
    const int sgn = idx >= 3072, i = idx - sgn * 3072;
    if (idx < FF) st4(b1f + idx, (u32)b1[idx] << 16);
    u32 r = 0;
    if (i < LUT_HALF) r = f2bf(mish_bf(bf2f((u16)((i + (LUT_E0 << 7)) | (sgn << 15)))));
    st2(lut + idx, r);
  }
}

#define K2_G1 256
#define K2_SSM 240
#define KCPITCH 2080
#define SB0() __builtin_amdgcn_sched_barrier(0)
template <int V> struct IC { static constexpr int value = V; };
template <int I, int N, class F> __device__ __forceinline__ void static_for_i(F&& f) {
  if constexpr (I < N) { f(IC<I>{}); static_for_i<I + 1, N>(f); }
}
template <int N, class F> __device__ __forceinline__ void static_for(F&& f) { static_for_i<0, N>(f); }
__device__ __forceinline__ void glds16s(u32 voff, const void* sbase, const void* lp) {
  unsigned keep;
  unsigned lds_dst = (unsigned)(size_t)(const __attribute__((address_space(3))) char*)lp;
  asm volatile("s_mov_b32 %0, m0\n\ts_mov_b32 m0, %3\n\ts_nop 0\n\tglobal_load_lds_dwordx4 %1, %2\n\ts_mov_b32 m0, %0"
               : "=&s"(keep) : "v"(voff), "s"(sbase), "s"(lds_dst) : "memory");
}
#define MFMA_BF(acc, a, b) \
  asm volatile("v_mfma_f32_16x16x32_bf16 %0, %1, %2, %0" : "+v"(acc) : "v"(a), "v"(b))
#define MFMA_BF0(acc, a, b) \
  asm volatile("v_mfma_f32_16x16x32_bf16 %0, %1, %2, 0" : "=&v"(acc) : "v"(a), "v"(b))
#define GLOAD4(dst, voff, sbase) \
  asm volatile("global_load_dwordx4 %0, %1, %2" : "=v"(dst) : "v"(voff), "s"(sbase) : "memory")
#define DSR64(dst, addr, off) asm volatile("ds_read_b64 %0, %1 offset:%2" : "=v"(dst) : "v"(addr), "n"(off) : "memory")
#define DSR128(dst, addr, off) asm volatile("ds_read_b128 %0, %1 offset:%2" : "=v"(dst) : "v"(addr), "n"(off) : "memory")

#define G1_FIRST
#define NT1 4
#define G1_D 4
#define LUT_NEG 65536
#define BIAS_OFF 76800
constexpr int g1_chunk(int slot, int w) { return (slot == 2 && w == 3) ? 71680 : 6144 + (slot * 4 + w) * 5120; }
constexpr int g1_piece_lds(int p) {
  return p < 20 * NT1 ? g1_chunk((p / 20) % 3, p % 4) + ((p % 20) / 4) * 1024
                      : g1_chunk((p / 20) % 3, (p % 20) / 5) + (p % 5) * 1024;
}
constexpr int g1_tile(int p) { return p < 20 * NT1 ? p % NT1 : p / 20; }
constexpr int g1_kt(int p) { return p < 20 * NT1 ? p / NT1 : p % 20; }
constexpr int g1_fin(int T) { return T < NT1 ? 19 * NT1 + T : 20 * T + 19; }
constexpr int g1_vmB(int p) { return (p & 1) && p >= 17 && p < 86 && (p - 17) % 20 < 10 ? ((p - 17) / 20) * 5 + ((p - 17) % 20) / 2 : -1; }
constexpr int g1_vmL(int p) { return (p == 47 || p == 49 || p == 51) ? (p - 47) / 2 : -1; }
struct G1S { int vA[140], vS[140], lB[140], ws[24], cs[24], sp[3]; };
constexpr G1S g1_sched() {
  G1S S{};
  for (int w = 0; w < 24; ++w) {
    const int e = g1_fin(w >> 2) + 2;
    S.ws[w] = (w && S.ws[w - 1] + 2 > e) ? S.ws[w - 1] + 2 : e;
  }
  for (int w = 0; w < 24; ++w) S.cs[w] = S.ws[w] + G1_D + 2;
  for (int r = 0; r < 3; ++r) S.sp[r] = S.cs[8 * r + 7] + 1;
  int idxA[20] = {}, idxB[7] = {}, idxL = 0, ldB[140] = {};
  int v = 1;
  for (int h = 0; h < 3; ++h) {
    v += 5; idxB[h] = v;
    if (h < 2) for (int k = 0; k < 5; ++k) { v += 2; idxA[5 * h + k] = v; }
  }
  int l = NT1;
  for (int q = 0; q < G1_D; ++q) ldB[q] = ++l;
  for (int q = 0; q < 140; ++q) {
    const int gq = q / 20, lp = q % 20;
    S.vA[q] = -1; S.vS[q] = -1;
    if (lp == 20 - G1_D && gq < 6) {
      int need = idxB[gq + 1];
      if (gq + 1 == NT1 && idxL > need) need = idxL;
      S.vS[q] = v - need;
    }
    for (int r = 0; r < 3; ++r) if (q == S.sp[r]) v += 2;
    if (q < 20 * NT1 && q % NT1 == 0) S.vA[q] = v - idxA[q / NT1];
    if (q < 40 && (q & 1) == 0) { v += 1; idxA[10 + q / 4] = v; }
    if (g1_vmB(q) >= 0) { v += 1; idxB[3 + g1_vmB(q) / 5] = v; }
    if (g1_vmL(q) >= 0) { v += 1; idxL = v; }
    if (q + G1_D < 140) ldB[q + G1_D] = ++l;
    if (lp == 12 && gq + 1 >= NT1 && gq < 6) ++l;
    S.lB[q] = l - ldB[q];
    for (int w = 0; w < 24; ++w) if (S.ws[w] + 1 == q) l += 2;
  }
  return S;
}
constexpr G1S G1 = g1_sched();
constexpr int g1_word(int p) { for (int w = 0; w < 24; ++w) if (p >= G1.ws[w] && p < G1.ws[w] + 2) return w; return -1; }
constexpr int g1_comb(int p) { for (int w = 0; w < 24; ++w) if (p == G1.cs[w]) return w; return -1; }
static_assert(G1.sp[2] < 140 && G1.ws[0] >= 20 * NT1 - G1_D, "schedule");
#define WAIT_L(n) __builtin_amdgcn_s_waitcnt(0xC07F | (((n) > 15 ? 15 : (n)) << 8))
#define MF_ "v_mfma_f32_16x16x32_bf16 "
#define SDWA_(h) " dst_sel:DWORD dst_unused:UNUSED_PAD src0_sel:DWORD src1_sel:WORD_" #h "\n\t"
template <bool F> __device__ __forceinline__ void step_p(f4& c0, f4& c1, const u32x4& b, const u32x4& x0, const u32x4& x1, const f4& bs) {
  if constexpr (F) asm volatile(MF_ "%0, %2, %3, %5\n\t" MF_ "%1, %2, %4, %5" : "=&v"(c0), "=&v"(c1) : "v"(b), "v"(x0), "v"(x1), "v"(bs));
  else asm volatile(MF_ "%0, %2, %3, %0\n\t" MF_ "%1, %2, %4, %1" : "+v"(c0), "+v"(c1) : "v"(b), "v"(x0), "v"(x1));
}
template <bool F> __device__ __forceinline__ void step_x(f4& c0, f4& c1, const u32x4& b, const u32x4& x0, const u32x4& x1, const f4& bs,
                                                         u32& w, u32& z, u32& mx, float e0, float e1, u32 a0, u32 a1, u32 kb) {
#define X_MID "v_cvt_pk_bf16_f32 %2, %8, %9\n\tv_pk_add_u16 %3, %2, %12\n\t"
#define X_END "\n\tv_max3_u16 %4, %4, %10, %11"
  if constexpr (F) asm volatile(MF_ "%0, %5, %6, %13\n\t" X_MID MF_ "%1, %5, %7, %13" X_END
                                : "=&v"(c0), "=&v"(c1), "=&v"(w), "=&v"(z), "+v"(mx)
                                : "v"(b), "v"(x0), "v"(x1), "v"(e0), "v"(e1), "v"(a0), "v"(a1), "s"(kb), "v"(bs));
  else asm volatile(MF_ "%0, %5, %6, %0\n\t" X_MID MF_ "%1, %5, %7, %1" X_END
                    : "+v"(c0), "+v"(c1), "=&v"(w), "=&v"(z), "+v"(mx)
                    : "v"(b), "v"(x0), "v"(x1), "v"(e0), "v"(e1), "v"(a0), "v"(a1), "s"(kb));
}
template <bool F> __device__ __forceinline__ void step_y(f4& c0, f4& c1, const u32x4& b, const u32x4& x0, const u32x4& x1, const f4& bs,
                                                         u32& a0, u32& a1, u32& r0, u32& r1, u32 z) {
#define Y_MID "v_lshlrev_b32_sdwa %2, 1, %9" SDWA_(0) "v_lshlrev_b32_sdwa %3, 1, %9" SDWA_(1)
#define Y_END "\n\tds_read_u16 %4, %2\n\tds_read_u16 %5, %3"
  if constexpr (F) asm volatile(MF_ "%0, %6, %7, %10\n\t" Y_MID MF_ "%1, %6, %8, %10" Y_END
                                : "=&v"(c0), "=&v"(c1), "=&v"(a0), "=&v"(a1), "=&v"(r0), "=&v"(r1)
                                : "v"(b), "v"(x0), "v"(x1), "v"(z), "v"(bs) : "memory");
  else asm volatile(MF_ "%0, %6, %7, %0\n\t" Y_MID MF_ "%1, %6, %8, %1" Y_END
                    : "+v"(c0), "+v"(c1), "=&v"(a0), "=&v"(a1), "=&v"(r0), "=&v"(r1)
                    : "v"(b), "v"(x0), "v"(x1), "v"(z) : "memory");
}
__device__ __forceinline__ void comb(u32& r, u32 hi) { asm volatile("v_lshl_or_b32 %0, %1, 16, %0" : "+v"(r) : "v"(hi)); }
__device__ __forceinline__ void max3u(u32& mx, u32 a0, u32 a1) { asm volatile("v_max3_u16 %0, %0, %1, %2" : "+v"(mx) : "v"(a0), "v"(a1)); }
__device__ __forceinline__ void epi_word(u32& w, u32& r0, u32& r1, u32& mx, float e0, float e1, u32 kb) {
  u32 z;
  asm volatile("v_cvt_pk_bf16_f32 %0, %5, %6\n\tv_pk_add_u16 %4, %0, %7\n\t"
               "v_lshlrev_b32_sdwa %1, 1, %4" SDWA_(0) "v_lshlrev_b32_sdwa %2, 1, %4" SDWA_(1)
               "v_max3_u16 %3, %3, %1, %2\n\tds_read_u16 %1, %1\n\tds_read_u16 %2, %2"
               : "=&v"(w), "=&v"(r0), "=&v"(r1), "+v"(mx), "=&v"(z) : "v"(e0), "v"(e1), "s"(kb) : "memory");
}
__device__ __forceinline__ void mfma_c(f4& acc, const u32x4& a, const u32x4& b, const f4& c) {
  asm volatile("v_mfma_f32_16x16x32_bf16 %0, %1, %2, %3" : "=&v"(acc) : "v"(a), "v"(b), "v"(c));
}
__device__ __forceinline__ void mfma_a(f4& acc, const u32x4& a, const u32x4& b) { MFMA_BF(acc, a, b); }
template <int OFF, class V> __device__ __forceinline__ void dsr128(V& d, u32 addr) { DSR128(d, addr, OFF); }
__device__ __forceinline__ void pin2(u32x4& a, u32x4& b) { asm volatile("" : "+v"(a), "+v"(b)); }
template <int N> __device__ __forceinline__ void wait_res(u32 (&a)[4], u32 (&b)[4]) {
  asm volatile("s_waitcnt lgkmcnt(%8)"
               : "+v"(a[0]), "+v"(a[1]), "+v"(a[2]), "+v"(a[3]), "+v"(b[0]), "+v"(b[1]), "+v"(b[2]), "+v"(b[3])
               : "n"(N > 15 ? 15 : N) : "memory");
}
template <int OFF> __device__ __forceinline__ void glds_o(u32 voff, const void* sbase, u32 m) {
  asm volatile("s_mov_b32 m0, %2\n\ts_nop 0\n\tglobal_load_lds_dwordx4 %0, %1 offset:%3"
               :: "v"(voff), "s"(sbase), "s"(m), "n"(OFF) : "memory", "m0");
}
__device__ __forceinline__ void gload4(u32x4& d, u32 voff, const void* sbase) { GLOAD4(d, voff, sbase); }
__device__ __forceinline__ void st16s(u32 voff, const void* sbase, u32x4 v) {
  asm volatile("global_store_dwordx4 %0, %1, %2" STF :: "v"(voff), "v"(v), "s"(sbase) : "memory");
}

__global__ __launch_bounds__(256, 2) void k2_kernel(const u16* __restrict__ xnP, const u16* __restrict__ w1P,
                                                    const float* __restrict__ b1f, const u16* __restrict__ lut,
                                                    u16* __restrict__ actF, u16* __restrict__ actH,
                                                    const u16* __restrict__ Pg, u16* __restrict__ yT) {
  __shared__ __attribute__((aligned(1024))) char shm[81920];
  const int t = threadIdx.x, lane = t & 63;
  const int wave = __builtin_amdgcn_readfirstlane(t >> 6);
  STAMPB(2, (char*)actF - (16u << 20));
  STAMP(0);
  const int vb = blockIdx.x;
  if (vb >= K2_G1) {
    const int sb = vb - K2_G1;
    const int li = sb >> 3;
    const int grp = (sb & 7) * 10 + li / 3, part = li % 3;
    const int nch = part < 2 ? 3 : 2;
    const int dbase = (grp >> 2) * 32 + (grp & 3) * 4;
    u16* Kc = (u16*)shm;
    float* red = (float*)(shm + 16896);
#pragma unroll
    for (int c = 0; c < 3; ++c) {
      if (c < nch) {
        const int e = 3 * part + c;
        const int d = dbase + (e >> 2) * 16 + (e & 3);
        char* buf = shm + 41472 + c * 6144;
        if (wave == 0) glds16s(lane * 16, (const char*)(Pg + (size_t)d * PPITCH), buf);
        if (wave == 1 && lane < 32) glds16s(lane * 16, (const char*)(Pg + (size_t)d * PPITCH) + 1024, buf + 1024);
      }
    }
    {
      u32x2 xv[8];
      const char* xp = (const char*)xnP + ((((size_t)(t >> 3) * 20 + (grp >> 2)) * 64 + (grp & 3) * 16 + ((2 * t) & 15)) * 16 + part * 4);
#pragma unroll
      for (int i = 0; i < 4; ++i) {
        xv[2 * i] = *(const u32x2*)(xp + (size_t)i * 655360);
        xv[2 * i + 1] = *(const u32x2*)(xp + (size_t)i * 655360 + 16);
      }
#pragma unroll
      for (int c = 0; c < 3; ++c) {
        if (c < nch) {
          const u32 pos2 = 2 * (part + c);
          const u32 sel = 0x00000c0cu | (pos2 << 16) | ((pos2 + 1) << 24);
          u16* xs = (u16*)(shm + 41472 + c * 6144 + 2048);
#pragma unroll
          for (int i = 0; i < 4; ++i) {
            const float x0 = __uint_as_float(__builtin_amdgcn_perm(xv[2 * i][1], xv[2 * i][0], sel));
            const float x1 = __uint_as_float(__builtin_amdgcn_perm(xv[2 * i + 1][1], xv[2 * i + 1][0], sel));
            *(u32*)&xs[2 * t + 512 * i] = pk_h(x0, x1);
          }
        }
      }
    }
    if (t < 96) *(u32x4*)(shm + 41472 + (t >> 5) * 6144 + 1536 + (t & 31) * 16) = (u32x4){0u, 0u, 0u, 0u};
    if (t < 8) *(u32x4*)&Kc[(t >> 1) * KCPITCH + (256 + (t & 1)) * 8] = (u32x4){0u, 0u, 0u, 0u};
    WAIT_V(0);
    RAW_BARRIER();
    STAMP(3);
    const int b = lane & 15, g = lane >> 4;
    const int m = 255 - t;
    const u32 lds0 = (u32)(size_t)(const __attribute__((address_space(3))) char*)shm;
    const int abase = ((3 - b) & 3) * KCPITCH + 2028 - 4 * (b >> 2) + 8 * g;
    const int bbase = 256 - 16 + 16 * b + 8 * g;
#pragma unroll 1
    for (int c = 0; c < nch; ++c) {
      const int e = 3 * part + c;
      const int d = dbase + (e >> 2) * 16 + (e & 3);
      const u16* P = (const u16*)(shm + 41472 + c * 6144);
      const float kscale = *(const float*)&P[704];
      {
        u32 wv[6];
        if (wave < 2) {
          const int i = 512 + m - 128;
          const u32 pc = P[i - 1], pa = P[i], pb = P[i + 1];
          const u32 aa = pa | (pa << 16);
          wv[0] = pb | (pb << 16);
          wv[1] = pb | (((m & 1) ? pb : pa) << 16);
          wv[2] = aa; wv[3] = aa; wv[4] = aa;
          wv[5] = m == 128 ? pc : pa;
        } else if (wave == 2) {
          const int i = 384 + 2 * (m - 64);
          const u32 q0 = *(const u32*)&P[i - 2], q1 = *(const u32*)&P[i], q2 = *(const u32*)&P[i + 2];
          wv[0] = __builtin_amdgcn_perm(q2, q2, 0x01000100u);
          wv[1] = __builtin_amdgcn_perm(q1, q1, 0x03020302u);
          wv[2] = __builtin_amdgcn_perm(q1, q1, 0x01000302u);
          wv[3] = __builtin_amdgcn_perm(q1, q1, 0x01000100u);
          wv[4] = m == 64 ? __builtin_amdgcn_perm(q0, q1, 0x07060100u) : wv[3];
          wv[5] = m == 64 ? q0 : (q0 >> 16);
        } else {
#pragma unroll
          for (int hh = 0; hh < 2; ++hh) {
            const int mm = m - hh;
            const int sg = mm >= 32 ? 1 : 0;
#pragma unroll
            for (int j = 0; j < (hh ? 2 : 4); ++j) {
              u32 pk = 0;
#pragma unroll
              for (int h = 0; h < 2; ++h) {
                if (hh == 0 || 2 * j + h < 3) {
                  int q = 8 * mm + 7 - (2 * j + h);
                  q = q < 0 ? 0 : q;
                  int idx = sg ? 128 + ((q + ((q >> 1) & 1)) >> 1) : q;
                  pk |= ((u32)P[idx]) << (16 * h);
                }
              }
              wv[hh * 4 + j] = (hh && mm < 0) ? 0u : pk;
            }
          }
        }
        *(u32x4*)&Kc[0 * KCPITCH + t * 8] = (u32x4){wv[0], wv[1], wv[2], wv[3]};
        *(u32x4*)&Kc[1 * KCPITCH + t * 8] = (u32x4){__builtin_amdgcn_alignbit(wv[1], wv[0], 16), __builtin_amdgcn_alignbit(wv[2], wv[1], 16),
                                                    __builtin_amdgcn_alignbit(wv[3], wv[2], 16), __builtin_amdgcn_alignbit(wv[4], wv[3], 16)};
        *(u32x4*)&Kc[2 * KCPITCH + t * 8] = (u32x4){wv[1], wv[2], wv[3], wv[4]};
        *(u32x4*)&Kc[3 * KCPITCH + t * 8] = (u32x4){__builtin_amdgcn_alignbit(wv[2], wv[1], 16), __builtin_amdgcn_alignbit(wv[3], wv[2], 16),
                                                    __builtin_amdgcn_alignbit(wv[4], wv[3], 16), __builtin_amdgcn_alignbit(wv[5], wv[4], 16)};
      }
      __syncthreads();
      if (c == 0) { STAMP(4); }
      f4 acc[8];
#pragma unroll
      for (int i = 0; i < 8; ++i) acc[i] = (f4){0.f, 0.f, 0.f, 0.f};
      {
        u32x2 a0[2][8], a1[2][8];
        u32x4 bq[2][8];
        const u32 aaddr = lds0 + (u32)(abase - 64 * wave - 32 - 256 * 7) * 2;
        const u32 baddr = lds0 + 41472 + c * 6144 + 1536 + (u32)(bbase - 64 * wave - 32) * 2;
#pragma unroll
        for (int rr = 0; rr < 2; ++rr) {
#pragma unroll
          for (int u = 0; u < 8; ++u) {
            DSR64(a0[rr][u], aaddr, (7 - u) * 512 + (1 - rr) * 64);
            DSR64(a1[rr][u], aaddr, (7 - u) * 512 + (1 - rr) * 64 + 8);
            DSR128(bq[rr][u], baddr, u * 512 + (1 - rr) * 64);
            if (rr == 1 && u == 3) {
              asm volatile("s_waitcnt lgkmcnt(12)"
                           : "+v"(a0[0][0]), "+v"(a1[0][0]), "+v"(a0[0][1]), "+v"(a1[0][1]), "+v"(a0[0][2]), "+v"(a1[0][2]),
                             "+v"(a0[0][3]), "+v"(a1[0][3]), "+v"(a0[0][4]), "+v"(a1[0][4]), "+v"(a0[0][5]), "+v"(a1[0][5]),
                             "+v"(a0[0][6]), "+v"(a1[0][6]), "+v"(a0[0][7]), "+v"(a1[0][7]),
                             "+v"(bq[0][0]), "+v"(bq[0][1]), "+v"(bq[0][2]), "+v"(bq[0][3]),
                             "+v"(bq[0][4]), "+v"(bq[0][5]), "+v"(bq[0][6]), "+v"(bq[0][7])
                           :: "memory");
            }
          }
        }
        SB0();
#pragma unroll
        for (int rr = 0; rr < 2; ++rr) {
          if (rr == 1) {
            asm volatile("s_waitcnt lgkmcnt(0)"
                         : "+v"(a0[1][0]), "+v"(a1[1][0]), "+v"(a0[1][1]), "+v"(a1[1][1]), "+v"(a0[1][2]), "+v"(a1[1][2]),
                           "+v"(a0[1][3]), "+v"(a1[1][3]), "+v"(a0[1][4]), "+v"(a1[1][4]), "+v"(a0[1][5]), "+v"(a1[1][5]),
                           "+v"(a0[1][6]), "+v"(a1[1][6]), "+v"(a0[1][7]), "+v"(a1[1][7]),
                           "+v"(bq[1][0]), "+v"(bq[1][1]), "+v"(bq[1][2]), "+v"(bq[1][3]),
                           "+v"(bq[1][4]), "+v"(bq[1][5]), "+v"(bq[1][6]), "+v"(bq[1][7])
                         :: "memory");
          }
#pragma unroll
          for (int u = 0; u < 8; ++u) {
            const h8 A = __builtin_bit_cast(h8, (u32x4){a0[rr][u][0], a0[rr][u][1], a1[rr][u][0], a1[rr][u][1]});
#pragma unroll
            for (int v = 0; v + u < 8; ++v)
              acc[u + v] = __builtin_amdgcn_mfma_f32_16x16x32_f16(A, __builtin_bit_cast(h8, bq[rr][v]), acc[u + v], 0, 0, 0);
          }
          SB0();
        }
      }
      if (c == 0) { STAMP(5); }
      auto red_phase = [&](auto WC) {
        constexpr int W = decltype(WC)::value;
#pragma unroll
        for (int o = 1; o < 4; ++o) {
          const int ow = (W + o) & 3;
#pragma unroll
          for (int i = 0; i < 2; ++i) *(f4*)&red[(((ow * 3 + (o - 1)) * 2 + i) * 64 + lane) * 4] = acc[ow * 2 + i];
        }
        __syncthreads();
#pragma unroll
        for (int i = 0; i < 2; ++i) {
          f4 s1 = *(const f4*)&red[(((W * 3 + 0) * 2 + i) * 64 + lane) * 4];
          f4 s2 = *(const f4*)&red[(((W * 3 + 1) * 2 + i) * 64 + lane) * 4];
          f4 s3 = *(const f4*)&red[(((W * 3 + 2) * 2 + i) * 64 + lane) * 4];
          f4 sum = ((acc[W * 2 + i] + s1) + (s2 + s3)) * kscale;
          u32x2 o = {pk_bf(sum[0], sum[1]), pk_bf(sum[2], sum[3])};
          st8(yT + (size_t)d * SEQ + 256 * (W * 2 + i) + 16 * b + 4 * g, o);
        }
      };
      if (wave == 0) red_phase(IC<0>{});
      else if (wave == 1) red_phase(IC<1>{});
      else if (wave == 2) red_phase(IC<2>{});
      else red_phase(IC<3>{});
      if (c == 0) { STAMP(6); }
      if (c == 1) { STAMP(7); }
    }
    STAMP(8);
    STAMPW();
    return;
  }
  const int xcd = vb & 7, idx = vb >> 3;
  const int mi = idx & 3, ni = idx >> 2;
  const int bm = (xcd >> 1) * 4 + mi, bn = (xcd & 1) * 8 + ni;
  const int mtw = bm * 8 + 2 * wave, nt0 = 27 * (bn >> 2) + 7 * (bn & 3);
  const char* abase = (const char*)xnP + (size_t)mtw * (20 * 1024);
  const u32 va0 = lane * 16, va1 = lane * 16 + 20 * 1024;
  const u32 lds0 = (u32)(size_t)(const __attribute__((address_space(3))) char*)shm;
  const u32 va4 = lane * 16 + 4096;
  const char* bb1 = (const char*)w1P + (size_t)((nt0 + wave) * 20) * 1024;
  const char* bb2 = (const char*)w1P + (size_t)(nt0 * 20 + 5 * wave) * 1024;
  const u32 mc0 = lds0 + 6144 + wave * 5120;
  const u32 mc2 = wave == 3 ? lds0 + 71680 : mc0 + 40960;
  auto stageB = [&](auto GC, auto IC_) {
    constexpr int gq = decltype(GC)::value, i = decltype(IC_)::value;
    const char* src = gq < NT1 ? bb1 + gq * 5120 : bb2 + (size_t)((gq == 6 && bn == 15) ? 5 : gq) * 20480;
    const u32 m = gq % 3 == 2 ? mc2 : mc0 + (gq % 3) * 20480;
    if constexpr (i < 4) glds_o<i * 1024>(va0, src, m);
    else glds_o<0>(va4, src, m + 4096);
  };
  u32x4 fa[20][2];
  auto loadA = [&](int s) {
    const char* ap = abase + s * 1024;
    GLOAD4(fa[s][0], va0, ap);
    GLOAD4(fa[s][1], va1, ap);
  };
  glds_o<0>((lane < 28 ? lane : 27) * 16, (const char*)(b1f + nt0 * 16), lds0 + BIAS_OFF);
  static_for<5>([&](auto I) { stageB(IC<0>{}, I); });
#pragma unroll
  for (int s = 0; s < 5; ++s) loadA(s);
  static_for<5>([&](auto I) { stageB(IC<1>{}, I); });
#pragma unroll
  for (int s = 5; s < 10; ++s) loadA(s);
  static_for<5>([&](auto I) { stageB(IC<2>{}, I); });
  STAMP(1);
  WAIT_V(30);
  __builtin_amdgcn_s_barrier();
  STAMP(2);
  typedef unsigned short us2 __attribute__((ext_vector_type(2)));
  const u32 lb = lds0 + lane * 16, lbh = lds0 + lane * 16 + 65536, lbb = lds0 + BIAS_OFF + (lane >> 4) * 16;
  f4 acc[7][2];
  u32x4 fb[G1_D + 1];
  f4 bias[7];
  u32 hw[7][4];
  u32 res[7][4], rhi[7][4];
  u32 mx = 0, zz, a0 = 0, a1 = 0;
  auto readB = [&](auto PC) {
    constexpr int q = decltype(PC)::value, off = g1_piece_lds(q);
    if constexpr (off < 65536) dsr128<off>(fb[q % (G1_D + 1)], lb);
    else dsr128<off - 65536>(fb[q % (G1_D + 1)], lbh);
  };
  const u32 kb = (0x10000u - (LUT_E0 << 7)) * 0x10001u;
  auto epi_fix = [&](int T) __attribute__((always_inline)) {
#pragma unroll
    for (int q = 0; q < 4; ++q) {
      const u32 z0 = hw[T][q] & 0x7FFFu, z1 = (hw[T][q] >> 16) & 0x7FFFu;
      const bool b0 = (z0 - (LUT_E0 << 7)) >= (u32)LUT_HALF, b1v = (z1 - (LUT_E0 << 7)) >= (u32)LUT_HALF;
      const float h0 = lo_f(hw[T][q]), h1 = hi_f(hw[T][q]);
      const float f0 = (z0 < (LUT_E0 << 7)) ? h0 * 0.59765625f : (h0 > 0.f ? h0 : h0 * 0.0f);
      const float f1 = (z1 < (LUT_E0 << 7)) ? h1 * 0.59765625f : (h1 > 0.f ? h1 : h1 * 0.0f);
      const u32 fw = pk_bf(f0, f1);
      res[T][q] = ((b0 ? fw : res[T][q]) & 0xFFFFu) | ((b1v ? fw : res[T][q]) & 0xFFFF0000u);
    }
  };
  const char* obase = (const char*)actF + ((size_t)mtw * 48 + 3 * bn) * 1024;
#pragma unroll
  for (int T = 0; T < NT1; ++T) DSR128(bias[T], lbb, T * 64);
  static_for<G1_D>(readB);
  static_for<140>([&](auto PC) {
    constexpr int p = decltype(PC)::value;
    constexpr int T = g1_tile(p), kt = g1_kt(p), gq = p / 20, lp = p % 20;
    if constexpr (G1.vS[p] >= 0) {
      WAIT_L(0);
      WAIT_V(G1.vS[p]);
      __builtin_amdgcn_s_barrier();
      if (gq == 0) { STAMP(3); }
      if (gq == 1) { STAMP(4); }
      if (gq == 3) { STAMP(5); }
      if (gq == 5) { STAMP(6); }
    }
    static_for<3>([&](auto RC) {
      constexpr int r = decltype(RC)::value;
      if constexpr (p == G1.sp[r]) {
        if constexpr (!(r < 2 && G1.ws[r < 2 ? 8 * r + 8 : 0] < p)) max3u(mx, a0, a1);
        if (__builtin_expect(__builtin_amdgcn_ballot_w64((mx & 0xFFFFu) >= 2u * LUT_HALF) != 0, 0)) {
          epi_fix(2 * r);
          epi_fix(2 * r + 1);
        }
        SB0();
#pragma unroll
        for (int i = 0; i < 2; ++i)
          st16s(va0, obase + (i * 48 + r) * 1024,
                (u32x4){res[2 * r][2 * i], res[2 * r][2 * i + 1], res[2 * r + 1][2 * i], res[2 * r + 1][2 * i + 1]});
      }
    });
    if constexpr (G1.vA[p] >= 0) {
      WAIT_V(G1.vA[p]);
      pin2(fa[kt][0], fa[kt][1]);
    }
    if constexpr (p < 40 && (p & 1) == 0) gload4(fa[10 + p / 4][(p >> 1) & 1], (p & 2) ? va1 : va0, abase + (10 + p / 4) * 1024);
    if constexpr (g1_vmB(p) >= 0) stageB(IC<3 + g1_vmB(p) / 5>{}, IC<g1_vmB(p) % 5>{});
    if constexpr (g1_vmL(p) >= 0) {
      glds_o<g1_vmL(p) * 1024>(va0, (const char*)lut + wave * 3072, lds0 + (wave & 1) * 3072 + (wave >> 1) * LUT_NEG);
    }
    if constexpr (p + G1_D < 140) readB(IC<p + G1_D>{});
    if constexpr (lp == 12 && gq + 1 >= NT1 && gq < 6) dsr128<(gq + 1) * 64>(bias[gq + 1], lbb);
    WAIT_L(G1.lB[p]);
    SB0();
    constexpr int w = g1_word(p), wT = w >= 0 ? w >> 2 : 0, wq = w & 3;
    if constexpr (w < 0) step_p<kt == 0>(acc[T][0], acc[T][1], fb[p % (G1_D + 1)], fa[kt][0], fa[kt][1], bias[T]);
    else if constexpr (p == G1.ws[w])
      step_x<kt == 0>(acc[T][0], acc[T][1], fb[p % (G1_D + 1)], fa[kt][0], fa[kt][1], bias[T], hw[wT][wq], zz, mx,
                      acc[wT][wq >> 1][2 * (wq & 1)], acc[wT][wq >> 1][2 * (wq & 1) + 1], a0, a1, kb);
    else step_y<kt == 0>(acc[T][0], acc[T][1], fb[p % (G1_D + 1)], fa[kt][0], fa[kt][1], bias[T], a0, a1, res[wT][wq], rhi[wT][wq], zz);
    constexpr int cw = g1_comb(p);
    if constexpr (cw >= 0) comb(res[cw >> 2][cw & 3], rhi[cw >> 2][cw & 3]);
    SB0();
  });
  STAMP(7);
  asm volatile("s_nop 7\n\ts_nop 7\n\ts_nop 7" : "+v"(acc[6][0]), "+v"(acc[6][1]) :: "memory");
  if ((bn & 3) != 3) {
#pragma unroll
    for (int q = 0; q < 4; ++q) epi_word(hw[6][q], res[6][q], rhi[6][q], mx, acc[6][q >> 1][2 * (q & 1)], acc[6][q >> 1][2 * (q & 1) + 1], kb);
    asm volatile("s_waitcnt lgkmcnt(0)" : "+v"(res[6][0]), "+v"(res[6][1]), "+v"(res[6][2]), "+v"(res[6][3]),
                 "+v"(rhi[6][0]), "+v"(rhi[6][1]), "+v"(rhi[6][2]), "+v"(rhi[6][3]) :: "memory");
#pragma unroll
    for (int q = 0; q < 4; ++q) res[6][q] |= rhi[6][q] << 16;
    if (__builtin_expect(__builtin_amdgcn_ballot_w64((mx & 0xFFFFu) >= 2u * LUT_HALF) != 0, 0)) epi_fix(6);
    st16s(va0, (const char*)actH + ((size_t)(mtw >> 1) * 12 + 3 * (bn >> 2) + (bn & 3)) * 1024,
          (u32x4){res[6][0], res[6][1], res[6][2], res[6][3]});
  }
  STAMP(8);
  STAMPW();
}
#define K3R 5
#define K3AUX 2
__global__ __launch_bounds__(256, 1) void k3_kernel(const u16* __restrict__ actF, const u16* __restrict__ actH,
                                                    const u16* __restrict__ w2F, const u16* __restrict__ w2H,
                                                    const u16* __restrict__ xin, const float2* __restrict__ stats,
                                                    const u16* __restrict__ yT, const u16* __restrict__ dskip,
                                                    u16* __restrict__ out) {
  constexpr int R = K3R;
  constexpr int NF = 12, NST = 15;
  __shared__ __attribute__((aligned(1024))) char shm[21 * 1024 + 60 * 1024];
  const int t = threadIdx.x, lane = t & 63;
  const int wave = __builtin_amdgcn_readfirstlane(t >> 6);
  const int xcd = blockIdx.x & 7, idx = blockIdx.x >> 3;
  const int mi = idx & 7, ni = idx >> 3;
  const int bm = (xcd >> 1) * 8 + mi, bn = (xcd & 1) * 4 + ni;
  const int mt0 = bm * 4, m0 = bm * 64, n0 = bn * 80, nt0 = bn * 5;
  STAMPB(3, (char*)actF - (16u << 20));
  STAMP(0);
  const char* abase = (const char*)actF + ((size_t)mt0 * 48 + wave) * 1024;
  const char* bbase = (const char*)w2F + ((size_t)nt0 * 48 + wave) * 1024;
  const char* ahb = (const char*)actH + ((size_t)(mt0 >> 1) * 12 + wave) * 1024;
  const char* bhb = (const char*)w2H + ((size_t)nt0 * 12 + wave) * 512;
  u32 va[4], vb[5];
#pragma unroll
  for (int i = 0; i < 4; ++i) va[i] = lane * 16 + ((wave + i) & 3) * (48 * 1024);
#pragma unroll
  for (int j = 0; j < 5; ++j) vb[j] = lane * 16 + j * (48 * 1024);
  u32x4 fa[R][4], fb[R][5];
  u32x4 ha[3][2];
  u32x2 hb[3][5];
  auto issue = [&](int s) {
    if (s < NF) {
      const char* ap = abase + s * 4096;
      const char* bp = bbase + s * 4096;
#pragma unroll
      for (int i = 0; i < 4; ++i) fa[s % R][i] = *(const u32x4*)(ap + va[i]);
#pragma unroll
      for (int j = 0; j < 5; ++j) fb[s % R][j] = *(const u32x4*)(bp + vb[j]);
    } else {
      const int h = s - NF;
#pragma unroll
      for (int i = 0; i < 2; ++i) ha[h][i] = *(const u32x4*)(ahb + h * 4096 + (lane * 16 + (((wave >> 1) + i) & 1) * (12 * 1024)));
#pragma unroll
      for (int j = 0; j < 5; ++j) hb[h][j] = *(const u32x2*)(bhb + h * 2048 + (lane * 8 + j * (12 * 512)));
    }
  };
  auto aux = [&]() {
#pragma unroll
    for (int i = 0; i < 6; ++i) {
      int p = wave + 4 * i;
      if (p > 20) p = 20;
      int c = (p < 10 ? p : p - 10) * 64 + lane;
      const u16* src;
      if (p < 10) src = xin + (size_t)(m0 + c / 10) * DM + n0 + (c % 10) * 8;
      else if (p < 20) src = yT + (size_t)(n0 + (c >> 3)) * SEQ + m0 + (c & 7) * 8;
      else src = (lane < 32) ? (const u16*)(stats + m0) + lane * 8 : dskip + n0 + (lane < 42 ? lane - 32 : 9) * 8;
      GLDS(src, shm + p * 1024);
    }
  };
  SB0();
#pragma unroll
  for (int s = 0; s < R; ++s) {
    if (s == K3AUX) { aux(); SB0(); }
    issue(s);
    SB0();
  }
  STAMP(1);
  const int fr = lane & 15, g = lane >> 4, tq = (lane & 15) >> 2, tp = lane & 3;
  const int ml = wave * 16 + fr;
  u32x2 s1p[5];
  float2 st;
  auto resid = [&](int j) {
    const char* sx = shm;
    const char* sy = shm + 10 * 1024;
    const char* sm = shm + 20 * 1024;
    if (j == 0) st = *(const float2*)(sm + ml * 8);
    const int nl = j * 16;
    u32x2 xx = *(const u32x2*)(sx + ml * 160 + (nl + 4 * g) * 2);
    u32x2 dd = *(const u32x2*)(sm + 512 + (nl + 4 * g) * 2);
    s4 yy = TRREAD(sy + (nl + 4 * g + tq) * 128 + (wave * 16 + 4 * tp) * 2);
    const u32x2 yw = __builtin_bit_cast(u32x2, yy);
#pragma unroll
    for (int h = 0; h < 2; ++h) {
      const float x0 = lo_f(xx[h]), x1 = hi_f(xx[h]);
      u32 d = pk_bf(x0 - st.x, x1 - st.x);
      u32 xn = pk_bf(lo_f(d) * st.y, hi_f(d) * st.y);
      u32 tt = pk_bf(lo_f(xn) * lo_f(dd[h]), hi_f(xn) * hi_f(dd[h]));
      u32 a = pk_bf(lo_f(yw[h]) + lo_f(tt), hi_f(yw[h]) + hi_f(tt));
      s1p[j][h] = pk_bf(x0 + lo_f(a), x1 + hi_f(a));
    }
    asm volatile("" : "+v"(s1p[j]));
  };
  f4 acc[4][5];
#pragma unroll
  for (int i = 0; i < 4; ++i)
#pragma unroll
    for (int j = 0; j < 5; ++j) acc[i][j] = (f4){0.f, 0.f, 0.f, 0.f};
  const bool odd = wave & 1;
  SB0();
#pragma unroll
  for (int s = 0; s < NST; ++s) {
    if (s < NF) {
#pragma unroll
      for (int i = 0; i < 4; ++i)
#pragma unroll
        for (int j = 0; j < 5; ++j)
          acc[i][j] = __builtin_amdgcn_mfma_f32_16x16x32_bf16(__builtin_bit_cast(s8, fb[s % R][j]),
                                                              __builtin_bit_cast(s8, fa[s % R][i]), acc[i][j], 0, 0, 0);
    } else {
      const int h = s - NF;
      u32x2 af[4];
      af[0] = odd ? (u32x2){ha[h][0][2], ha[h][0][3]} : (u32x2){ha[h][0][0], ha[h][0][1]};
      af[1] = odd ? (u32x2){ha[h][1][0], ha[h][1][1]} : (u32x2){ha[h][0][2], ha[h][0][3]};
      af[2] = odd ? (u32x2){ha[h][1][2], ha[h][1][3]} : (u32x2){ha[h][1][0], ha[h][1][1]};
      af[3] = odd ? (u32x2){ha[h][0][0], ha[h][0][1]} : (u32x2){ha[h][1][2], ha[h][1][3]};
#pragma unroll
      for (int i = 0; i < 4; ++i)
#pragma unroll
        for (int j = 0; j < 5; ++j)
          acc[i][j] = __builtin_amdgcn_mfma_f32_16x16x16bf16_1k(__builtin_bit_cast(s4, hb[h][j]),
                                                               __builtin_bit_cast(s4, af[i]), acc[i][j], 0, 0, 0);
    }
    SB0();
    if (s + R < NST) issue(s + R);
    SB0();
    if (s == 4) {
      WAIT_V(R * 9 > 60 ? 60 : R * 9);
      __builtin_amdgcn_s_barrier();
      SB0();
    }
    if (s >= 5 && s < 10) { resid(s - 5); SB0(); }
    if (s == 0) { STAMP(2); }
    if (s == 2) { STAMP(4); }
    if (s == 6) { STAMP(5); }
    if (s == 10) { STAMP(6); }
  }
  STAMP(7);
  char* red = shm + 21 * 1024;
#pragma unroll
  for (int i = 1; i < 4; ++i)
#pragma unroll
    for (int j = 0; j < 5; ++j)
      *(f4*)(red + ((((wave + i) & 3) * 3 + (i - 1)) * 5 + j) * 1024 + lane * 16) = acc[i][j];
  __syncthreads();
  u32x2 ov[5];
#pragma unroll
  for (int j = 0; j < 5; ++j) {
    f4 p0 = *(const f4*)(red + ((wave * 3 + 0) * 5 + j) * 1024 + lane * 16);
    f4 p1 = *(const f4*)(red + ((wave * 3 + 1) * 5 + j) * 1024 + lane * 16);
    f4 p2 = *(const f4*)(red + ((wave * 3 + 2) * 5 + j) * 1024 + lane * 16);
    f4 sum = (acc[0][j] + p0) + (p1 + p2);
    u32 m0p = pk_bf(sum[0], sum[1]), m1p = pk_bf(sum[2], sum[3]);
    ov[j] = (u32x2){pk_bf(lo_f(s1p[j][0]) + lo_f(m0p), hi_f(s1p[j][0]) + hi_f(m0p)),
                    pk_bf(lo_f(s1p[j][1]) + lo_f(m1p), hi_f(s1p[j][1]) + hi_f(m1p))};
  }
  u16* orow = out + (size_t)(m0 + ml) * DM + n0;
#pragma unroll
  for (int jp = 0; jp < 2; ++jp) {
    auto r0 = __builtin_amdgcn_permlane16_swap(ov[2 * jp][0], ov[2 * jp + 1][0], false, false);
    auto r1 = __builtin_amdgcn_permlane16_swap(ov[2 * jp][1], ov[2 * jp + 1][1], false, false);
    st16(orow + (2 * jp + (g & 1)) * 16 + 8 * (g >> 1), (u32x4){r0[0], r1[0], r0[1], r1[1]});
  }
  st8(orow + 64 + 4 * g, ov[4]);
  STAMP(8);
  STAMPW();
}

extern "C" void kernel_launch(void* const* d_in, const int* in_sizes, int n_in,
                              void* d_out, int out_size, void* d_ws, size_t ws_size,
                              hipStream_t stream) {
  const u16* u = (const u16*)d_in[0];
  const u16* lar = (const u16*)d_in[1];
  const u16* aim = (const u16*)d_in[2];
  const u16* cre = (const u16*)d_in[3];
  const u16* cim = (const u16*)d_in[4];
  const u16* logdt = (const u16*)d_in[5];
  const u16* dskip = (const u16*)d_in[6];
  const u16* w1 = (const u16*)d_in[8];
  const u16* b1 = (const u16*)d_in[9];
  const u16* w2 = (const u16*)d_in[10];
  u16* out = (u16*)d_out;
  char* ws = (char*)d_ws;
  if (ws_size < (32u << 20)) return;
  u16* xnP = (u16*)(ws);
  u16* w1P = (u16*)(ws + (4u << 20));
  float2* stats = (float2*)(ws + (12u << 20) + 4096);
  u16* lut = (u16*)(ws + (12u << 20) + 32768);
  float* b1f = (float*)(ws + (12u << 20) + 65536);
  u16* yT = (u16*)(ws + (13u << 20));
  u16* actF = (u16*)(ws + (16u << 20));
  u16* actH = (u16*)(ws + (22u << 20));
  u16* w2F = (u16*)(ws + (24u << 20));
  u16* w2H = (u16*)(ws + (26u << 20));
  u16* Pg = (u16*)(ws + (28u << 20));

  k1_kernel<<<K1_LN + K1_KG + K1_RP + K1_RQ + K1_LUT, 256, 0, stream>>>(u, xnP, stats, lut, w1, w1P, w2, w2F, w2H, b1, b1f,
                                                                        lar, aim, cre, cim, logdt, Pg);
  k2_kernel<<<K2_G1 + K2_SSM, 256, 0, stream>>>(xnP, w1P, b1f, lut, actF, actH, Pg, yT);
  k3_kernel<<<256, 256, 0, stream>>>(actF, actH, w2F, w2H, u, stats, yT, dskip, out);
}
```

```cpp
#include <hip/hip_runtime.h>
#include <hip/hip_bf16.h>

#define SEQ 2048
#define DM 640
#define NS 128
#define FF 1728

typedef unsigned short u16;
typedef short s4 __attribute__((ext_vector_type(4)));
typedef short s8 __attribute__((ext_vector_type(8)));
typedef _Float16 h8 __attribute__((ext_vector_type(8)));
typedef float f4 __attribute__((ext_vector_type(4)));
typedef unsigned int u32;
typedef unsigned int u32x4 __attribute__((ext_vector_type(4)));
typedef unsigned int u32x2 __attribute__((ext_vector_type(2)));

__device__ __forceinline__ float bf2f(u16 v) { return __uint_as_float(((u32)v) << 16); }
__device__ __forceinline__ u16 f2bf(float f) {
  u32 u = __float_as_uint(f);
  u32 r = (u + 0x7FFFu + ((u >> 16) & 1u)) >> 16;
  return (u16)r;
}
__device__ __forceinline__ float bfr(float f) { return bf2f(f2bf(f)); }

#define STAMPB(kid, base)
#define STAMP(k)
#define STAMPW()
typedef __bf16 bf2_t __attribute__((ext_vector_type(2)));
typedef float f2_t __attribute__((ext_vector_type(2)));
__device__ __forceinline__ u32 pk_bf(float a, float b) {
  f2_t v = {a, b};
  bf2_t r = __builtin_convertvector(v, bf2_t);
  return __builtin_bit_cast(u32, r);
}
__device__ __forceinline__ float lo_f(u32 w) { return __uint_as_float(w << 16); }
__device__ __forceinline__ float hi_f(u32 w) { return __uint_as_float(w & 0xFFFF0000u); }

#define WAIT_V(n) asm volatile("s_waitcnt vmcnt(%0)" ::"n"(n) : "memory")
#define RAW_BARRIER() do { asm volatile("s_waitcnt lgkmcnt(0)" ::: "memory"); __builtin_amdgcn_s_barrier(); } while (0)
__device__ __forceinline__ void glds16(const void* gsrc, const void* lp) {
  unsigned keep;
  unsigned lds_dst = (unsigned)(size_t)(const __attribute__((address_space(3))) char*)lp;
  asm volatile("s_mov_b32 %0, m0\n\ts_mov_b32 m0, %2\n\ts_nop 0\n\tglobal_load_lds_dwordx4 %1, off\n\ts_mov_b32 m0, %0"
               : "=&s"(keep) : "v"(gsrc), "s"(lds_dst) : "memory");
}
#define GLDS(gp, lp) glds16((const void*)(gp), (const void*)(lp))

#define STF " sc1"
__device__ __forceinline__ void st16(void* p, u32x4 v) { asm volatile("global_store_dwordx4 %0, %1, off" STF :: "v"(p), "v"(v) : "memory"); }
__device__ __forceinline__ void st8(void* p, u32x2 v) { asm volatile("global_store_dwordx2 %0, %1, off" STF :: "v"(p), "v"(v) : "memory"); }
__device__ __forceinline__ void st4(void* p, u32 v) { asm volatile("global_store_dword %0, %1, off" STF :: "v"(p), "v"(v) : "memory"); }
__device__ __forceinline__ void st2(void* p, u32 v) { asm volatile("global_store_short %0, %1, off" STF :: "v"(p), "v"(v) : "memory"); }

#define LUT_E0 102
#define LUT_E1 134
#define LUT_HALF ((LUT_E1 - LUT_E0) * 128)
#define LUT_N (2 * LUT_HALF)

__device__ __forceinline__ float mish_bf(float h) {
  float e = bfr(expf(-fabsf(h)));
  float l = bfr(log1pf(e));
  float sp = bfr(l + (h > 0.f ? h : 0.f));
  float th = bfr(tanhf(sp));
  return bfr(h * th);
}

#define KRLEN 2080
#define WFENCE() do { asm volatile("" ::: "memory"); __builtin_amdgcn_wave_barrier(); asm volatile("" ::: "memory"); } while (0)
__device__ __forceinline__ void cexp_p(float arl2, float w, float p, float& re, float& im) {
  float mag = __builtin_amdgcn_exp2f(arl2 * p);
  float rev = w * p;
  rev = rev - floorf(rev);
  re = mag * __builtin_amdgcn_cosf(rev);
  im = mag * __builtin_amdgcn_sinf(rev);
}
__device__ __forceinline__ void cmul(float ar, float ai, float br, float bi, float& cr, float& ci) {
  float r = fmaf(ar, br, -(ai * bi));
  float i = fmaf(ar, bi, ai * br);
  cr = r; ci = i;
}
__device__ __forceinline__ u32x2 split2(float a, float b) {
  u32 h = pk_bf(a, b);
  u32 l = pk_bf(a - lo_f(h), b - hi_f(h));
  return (u32x2){h, l};
}

#define CODE_TOUCH(sym0, sym1, line, d0) do { \
    unsigned long long pc_; int o0_, o1_; \
    asm volatile("s_getpc_b64 %0\n.Lpc_here%=:\n\ts_mov_b32 %1, " sym0 "-.Lpc_here%=\n\ts_mov_b32 %2, " sym1 "-.Lpc_here%=" \
                 : "=s"(pc_), "=s"(o0_), "=s"(o1_)); \
    int len_ = o1_ - o0_; \
    if (len_ < 4 || len_ > 65536) { len_ = 4; o0_ = 0; } \
    const char* cb_ = (const char*)(pc_ + (long long)o0_); \
    const int oa_ = (line) * 128 < len_ - 4 ? (line) * 128 : len_ - 4; \
    asm volatile("global_load_dword %0, %1, %2" : "=v"(d0) : "v"(oa_ & ~3), "s"(cb_) : "memory"); \
  } while (0)

#define WS_XNP 0u
#define WS_W1P (4u << 20)
#define WS_STATS ((12u << 20) + 4096)
#define WS_LUT ((12u << 20) + 32768)
#define WS_B1F ((12u << 20) + 65536)
#define WS_FLAGS ((12u << 20) + 98304)
#define WS_YT (13u << 20)
#define WS_ACTF (16u << 20)
#define WS_ACTH (22u << 20)
#define WS_W2F (24u << 20)
#define WS_W2H (26u << 20)
#define WS_PG (28u << 20)
#define K1_LN 256
#define K1_KG 640
#define K1_RP 135
#define K1_RQ 160
#define PPITCH 768
#define K1_LUT 32
#define WPITCH 544
typedef _Float16 h2_t __attribute__((ext_vector_type(2)));
__device__ __forceinline__ u32 pk_h(float a, float b) {
  f2_t v = {a, b};
  h2_t r = __builtin_convertvector(v, h2_t);
  return __builtin_bit_cast(u32, r);
}
#define TRREAD(p) __builtin_amdgcn_ds_read_tr16_b64_v4i16((__attribute__((address_space(3))) s4*)(p))

__device__ __forceinline__ void repack_load(const u16* __restrict__ w, int N, int ku, int nu, char* lds, int tl) {
#pragma unroll
  for (int i = 0; i < 2; ++i) {
    int idx = tl + 256 * i, r = idx >> 3, c = idx & 7;
    u32x4 v = *(const u32x4*)(w + (size_t)(ku * 64 + r) * N + nu * 64 + c * 8);
    *(u32x4*)(lds + r * 160 + c * 16) = v;
  }
}
__device__ __forceinline__ void repack_store(u16* __restrict__ wP, int KB, int ku, int nu, const char* lds, int tl) {
  const int wl = tl >> 6, lane = tl & 63, g = lane >> 4, tq = (lane & 15) >> 2, tp = lane & 3;
#pragma unroll
  for (int kb2 = 0; kb2 < 2; ++kb2) {
    const char* p0 = lds + (kb2 * 32 + 4 * g + tq) * 160 + wl * 32 + tp * 8;
    s4 b0 = TRREAD(p0);
    s4 b1 = TRREAD(p0 + 16 * 160);
    s8 bf = {b0[0], b0[1], b0[2], b0[3], b1[0], b1[1], b1[2], b1[3]};
    st16(wP + (((size_t)(nu * 4 + wl) * KB + ku * 2 + kb2) * 64 + lane) * 8, __builtin_bit_cast(u32x4, bf));
  }
}

extern "C" __global__ __launch_bounds__(256) void k1_kernel(const u16* __restrict__ u, u16* __restrict__ xnP,
                                                 float2* __restrict__ stats, u16* __restrict__ lut,
                                                 const u16* __restrict__ w1, u16* __restrict__ w1P,
                                                 const u16* __restrict__ w2, u16* __restrict__ w2F, u16* __restrict__ w2H,
                                                 const u16* __restrict__ b1, float* __restrict__ b1f,
                                                 const u16* __restrict__ lar, const u16* __restrict__ aim,
                                                 const u16* __restrict__ cre, const u16* __restrict__ cim,
                                                 const u16* __restrict__ logdt, u16* __restrict__ Pg, u32* __restrict__ flags) {
  __shared__ __attribute__((aligned(16))) char k1s[57 * WPITCH];
  const int t = threadIdx.x, lane = t & 63;
  const int wave = __builtin_amdgcn_readfirstlane(t >> 6);
  STAMPB(0, xnP);
  STAMP(0);
  if (blockIdx.x < K1_LN) {
    u16 (*tile)[DM + 8] = (u16 (*)[DM + 8])k1s;
    const int r = wave * 2 + (lane >> 5), q = lane & 31;
    const int rg = blockIdx.x;
    const int row = rg * 8 + r;
    float v[20];
    const u32x2* src = (const u32x2*)(u + (size_t)row * DM);
#pragma unroll
    for (int i = 0; i < 5; ++i) {
      u32x2 w = src[q + 32 * i];
      v[i * 4 + 0] = lo_f(w[0]); v[i * 4 + 1] = hi_f(w[0]);
      v[i * 4 + 2] = lo_f(w[1]); v[i * 4 + 3] = hi_f(w[1]);
    }
    float s = 0.f;
#pragma unroll
    for (int i = 0; i < 20; ++i) s += v[i];
    STAMP(1);
#pragma unroll
    for (int o = 1; o < 32; o <<= 1) s += __shfl_xor(s, o);
    const float mean = s * (1.0f / DM);
    float ss = 0.f;
#pragma unroll
    for (int i = 0; i < 20; ++i) { float d = v[i] - mean; ss += d * d; }
#pragma unroll
    for (int o = 1; o < 32; o <<= 1) ss += __shfl_xor(ss, o);
    const float var = ss * (1.0f / DM);
    const float mb = bfr(mean);
    const float vb = bfr(var);
    const float den = bfr(sqrtf(bfr(vb + 1e-5f)));
    const float rden = 1.0f / den;
    if (q == 0) st8(stats + row, (u32x2){__float_as_uint(mb), __float_as_uint(rden)});
#pragma unroll
    for (int i = 0; i < 5; ++i) {
      u32 d0 = pk_bf(v[i * 4 + 0] - mb, v[i * 4 + 1] - mb);
      u32 d1 = pk_bf(v[i * 4 + 2] - mb, v[i * 4 + 3] - mb);
      u32x2 w = {pk_bf(lo_f(d0) * rden, hi_f(d0) * rden), pk_bf(lo_f(d1) * rden, hi_f(d1) * rden)};
      *(u32x2*)&tile[r][(q + 32 * i) * 4] = w;
    }
    __syncthreads();
    STAMP(2);
    const int mt = rg >> 1, half = rg & 1;
#pragma unroll
    for (int i = 0; i < 3; ++i) {
      int idx = t + 256 * i;
      if (idx < 640) {
        int kb = idx >> 5, g = (idx >> 3) & 3, fr = idx & 7;
        u32x2 a = *(const u32x2*)&tile[fr][kb * 32 + 4 * g];
        u32x2 b = *(const u32x2*)&tile[fr][kb * 32 + 16 + 4 * g];
        u32x4 w = {a[0], a[1], b[0], b[1]};
        st16(xnP + (((size_t)mt * 20 + kb) * 64 + g * 16 + half * 8 + fr) * 8, w);
      }
    }
    STAMP(3);
    STAMPW();
    return;
  }
  if (blockIdx.x < K1_LN + K1_KG) {
    const int d = blockIdx.x - K1_LN;
    const int n = t & 127;
    char* shm = k1s;
    const u16 r_dt = logdt[d];
    const u16 r_ar = lar[d * NS + n], r_ai = aim[d * NS + n];
    const u16 r_cr = cre[d * NS + n], r_ci = cim[d * NS + n];
    const u16 r_cr2 = cre[d * NS + (n ^ 64)], r_ci2 = cim[d * NS + (n ^ 64)];
    const float dt = bfr(expf(bf2f(r_dt)));
    const float ar = -bfr(expf(bf2f(r_ar))) * dt;
    const float ai = bf2f(r_ai) * dt;
    float cr = bf2f(r_cr) * dt, ci = bf2f(r_ci) * dt;
    float bs = (fabsf(cr) + fabsf(ci)) + (fabsf(bf2f(r_cr2) * dt) + fabsf(bf2f(r_ci2) * dt));
#pragma unroll
    for (int o = 1; o < 64; o <<= 1) bs += __shfl_xor(bs, o);
    const float bound = 2.0f * bs;
    int ex = 0;
    if (bound > 0.f && bound < 3.0e38f) { (void)frexpf(bound, &ex); }
    int es = 15 - ex;
    es = es > 100 ? 100 : (es < -100 ? -100 : es);
    const float sc2 = ldexpf(2.0f, es);
    const float kscale = ldexpf(1.0f, -es);
    cr *= sc2; ci *= sc2;
    const float arl2 = ar * 1.4426950408889634f;
    const float w = ai * 0.15915494309189535f;
    STAMP(1);
    char* wr = shm + n * 4;
    float eHr, eHi;
    cexp_p(arl2, w, 128.0f, eHr, eHi);
    if (wave < 2) {
      float e8r, e8i, e4r, e4i, gr, gi;
      cexp_p(arl2, w, 8.0f, e8r, e8i);
      cexp_p(arl2, w, 4.0f, e4r, e4i);
      cexp_p(arl2, w, 512.0f, gr, gi);
      cmul(gr, gi, cr, ci, gr, gi);
      float lr = 1.f, lim = 0.f;
      *(u32*)(wr) = pk_h(1.f, 0.f);
#pragma unroll
      for (int j = 1; j < 16; ++j) {
        cmul(lr, lim, e8r, e8i, lr, lim);
        *(u32*)(wr + j * WPITCH) = pk_h(lr, -lim);
        if (j < 5) {
          float hr, hi;
          *(u32*)(wr + (40 + (j - 1) * 2) * WPITCH) = pk_h(gr, gi);
          cmul(gr, gi, e4r, e4i, hr, hi);
          *(u32*)(wr + (41 + (j - 1) * 2) * WPITCH) = pk_h(hr, hi);
          cmul(gr, gi, eHr, eHi, gr, gi);
        } else if (j < 14) {
          *(u32*)(wr + (48 + j - 5) * WPITCH) = pk_h(gr, gi);
          if (j < 13) cmul(gr, gi, eHr, eHi, gr, gi);
        }
      }
    } else {
      float Rr[8], Ri[8];
      Rr[0] = 1.f; Ri[0] = 0.f;
      cexp_p(arl2, w, 1.0f, Rr[1], Ri[1]);
#pragma unroll
      for (int r = 2; r < 8; ++r) cmul(Rr[r - 1], Ri[r - 1], Rr[1], Ri[1], Rr[r], Ri[r]);
      float gr = cr, gi = ci;
#pragma unroll
      for (int ii = 0; ii < 2; ++ii) {
        *(u32*)(wr + (16 + ii * 8) * WPITCH) = pk_h(gr, gi);
#pragma unroll
        for (int r = 1; r < 8; ++r) {
          float hr, hi;
          cmul(gr, gi, Rr[r], Ri[r], hr, hi);
          *(u32*)(wr + (16 + ii * 8 + r) * WPITCH) = pk_h(hr, hi);
        }
        cmul(gr, gi, eHr, eHi, gr, gi);
      }
#pragma unroll
      for (int ii = 2; ii < 4; ++ii) {
        *(u32*)(wr + (32 + (ii - 2) * 4) * WPITCH) = pk_h(gr, gi);
#pragma unroll
        for (int r = 1; r < 4; ++r) {
          float hr, hi;
          cmul(gr, gi, Rr[2 * r], Ri[2 * r], hr, hi);
          *(u32*)(wr + (32 + (ii - 2) * 4 + r) * WPITCH) = pk_h(hr, hi);
        }
        if (ii < 3) cmul(gr, gi, eHr, eHi, gr, gi);
      }
    }
    __syncthreads();
    STAMP(2);
    const int b = lane & 15, g = lane >> 4;
    u16* P = Pg + (size_t)d * PPITCH;
    if (wave < 3) {
      const int rowa = 16 + 16 * wave + b;
      const char* ra = shm + (rowa > 56 ? 56 : rowa) * WPITCH + g * 16;
      const char* rb = shm + b * WPITCH + g * 16;
      f4 ka = (f4){0.f, 0.f, 0.f, 0.f};
#pragma unroll
      for (int k8 = 0; k8 < 8; ++k8) {
        h8 a = *(const h8*)(ra + k8 * 64);
        h8 bb = *(const h8*)(rb + k8 * 64);
        ka = __builtin_amdgcn_mfma_f32_16x16x32_f16(a, bb, ka, 0, 0, 0);
      }
      const u32 k01 = pk_h(ka[0], ka[1]), k23 = pk_h(ka[2], ka[3]);
      if (wave == 0) {
        st8(&P[(g >> 1) * 128 + (g & 1) * 4 + 8 * b], (u32x2){k01, k23});
      } else if (wave == 1) {
        const int i1 = g < 2 ? 256 + 64 * g + 4 * b : 384 + 64 * (g - 2) + 2 * b;
        st4(&P[i1], k01);
        st4(&P[i1 + (g < 2 ? 2 : 32)], k23);
      } else {
        const int i2 = 512 + 64 * g + b;
        if (g < 3) st2(&P[i2], k01);
        if (g < 2) {
          st2(&P[i2 + 16], k01 >> 16);
          st2(&P[i2 + 32], k23); st2(&P[i2 + 48], k23 >> 16);
        }
      }
    } else if (lane == 0) {
      st4(&P[704], __float_as_uint(kscale));
    }
    STAMP(3);
    STAMPW();
    return;
  }
  if (blockIdx.x < K1_LN + K1_KG + K1_RP) {
    const int u0 = (blockIdx.x - (K1_LN + K1_KG)) * 2;
    repack_load(w1, FF, u0 / 27, u0 % 27, k1s, t);
    repack_load(w1, FF, (u0 + 1) / 27, (u0 + 1) % 27, k1s + 10240, t);
    __syncthreads();
    repack_store(w1P, DM / 32, u0 / 27, u0 % 27, k1s, t);
    repack_store(w1P, DM / 32, (u0 + 1) / 27, (u0 + 1) % 27, k1s + 10240, t);
    STAMP(3);
    STAMPW();
    return;
  }
  if (blockIdx.x < K1_LN + K1_KG + K1_RP + K1_RQ) {
    const int un = blockIdx.x - (K1_LN + K1_KG + K1_RP);
    const int bn = un / 10, nu = un % 10;
    const int f0 = 16 * (27 * (bn >> 2) + 7 * (bn & 3));
    const int rows = (bn & 3) == 3 ? 96 : 112;
    u32x4 v[4];
#pragma unroll
    for (int i = 0; i < 4; ++i) {
      const int r = (t >> 3) + 32 * i;
      v[i] = *(const u32x4*)(w2 + (size_t)(f0 + (r < rows ? r : rows - 1)) * DM + nu * 64 + (t & 7) * 8);
    }
#pragma unroll
    for (int i = 0; i < 4; ++i) *(u32x4*)(k1s + ((t >> 3) + 32 * i) * 160 + (t & 7) * 16) = v[i];
    __syncthreads();
    const int g = lane >> 4, tq = (lane & 15) >> 2, tp = lane & 3;
    const int nt = nu * 4 + wave;
    const char* p0 = k1s + (4 * g + tq) * 160 + wave * 32 + tp * 8;
#pragma unroll
    for (int p = 0; p < 3; ++p) {
      s4 b0 = TRREAD(p0 + (32 * p) * 160);
      s4 b1v = TRREAD(p0 + (32 * p + 16) * 160);
      s8 bf = {b0[0], b0[1], b0[2], b0[3], b1v[0], b1v[1], b1v[2], b1v[3]};
      st16(w2F + (((size_t)nt * 48 + 3 * bn + p) * 64 + lane) * 8, __builtin_bit_cast(u32x4, bf));
    }
    if (rows == 112) {
      s4 b0 = TRREAD(p0 + 96 * 160);
      st8(w2H + (((size_t)nt * 12 + 3 * (bn >> 2) + (bn & 3)) * 64 + lane) * 4, __builtin_bit_cast(u32x2, b0));
    }
    STAMP(3);
    STAMPW();
    return;
  }
  {
    const int idx = (blockIdx.x - (K1_LN + K1_KG + K1_RP + K1_RQ)) * 256 + t;
    const int sgn = idx >= 4096, i = idx - sgn * 4096;
    u32 td = 0;
    if (idx < 4096) CODE_TOUCH("k2_kernel", "k4_end", (idx >> 11) * 256 + t, td);
    if (idx < FF) st4(b1f + idx, (u32)b1[idx] << 16);
    if (idx >= 2048 && idx < 4096) st4(flags + (idx - 2048), 0u);
    u32 r = 0;
    if (i < LUT_HALF) r = f2bf(mish_bf(bf2f((u16)((i + (LUT_E0 << 7)) | (sgn << 15)))));
    st2(lut + idx, r);
    asm volatile("s_waitcnt vmcnt(0)" :: "v"(td) : "memory");
  }
}

#define K2_G1 256
#define K2_SSM 240
#define KCPITCH 2080
#define SB0() __builtin_amdgcn_sched_barrier(0)
template <int V> struct IC { static constexpr int value = V; };
template <int I, int N, class F> __device__ __forceinline__ void static_for_i(F&& f) {
  if constexpr (I < N) { f(IC<I>{}); static_for_i<I + 1, N>(f); }
}
template <int N, class F> __device__ __forceinline__ void static_for(F&& f) { static_for_i<0, N>(f); }
__device__ __forceinline__ void glds16s(u32 voff, const void* sbase, const void* lp) {
  unsigned keep;
  unsigned lds_dst = (unsigned)(size_t)(const __attribute__((address_space(3))) char*)lp;
  asm volatile("s_mov_b32 %0, m0\n\ts_mov_b32 m0, %3\n\ts_nop 0\n\tglobal_load_lds_dwordx4 %1, %2\n\ts_mov_b32 m0, %0"
               : "=&s"(keep) : "v"(voff), "s"(sbase), "s"(lds_dst) : "memory");
}
#define MFMA_BF(acc, a, b) \
  asm volatile("v_mfma_f32_16x16x32_bf16 %0, %1, %2, %0" : "+v"(acc) : "v"(a), "v"(b))
#define MFMA_BF0(acc, a, b) \
  asm volatile("v_mfma_f32_16x16x32_bf16 %0, %1, %2, 0" : "=&v"(acc) : "v"(a), "v"(b))
#define GLOAD4(dst, voff, sbase) \
  asm volatile("global_load_dwordx4 %0, %1, %2" : "=v"(dst) : "v"(voff), "s"(sbase) : "memory")
#define DSR64(dst, addr, off) asm volatile("ds_read_b64 %0, %1 offset:%2" : "=v"(dst) : "v"(addr), "n"(off) : "memory")
#define DSR128(dst, addr, off) asm volatile("ds_read_b128 %0, %1 offset:%2" : "=v"(dst) : "v"(addr), "n"(off) : "memory")

#define NT1 4
#define G1_D 4
#define LUT_NEG 65536
#define BIAS_OFF 78848
constexpr int g1_chunk(int slot, int w) { return (slot == 2 && w == 3) ? 73728 : 8192 + (slot * 4 + w) * 5120; }
constexpr int g1_piece_lds(int p) {
  return p < 20 * NT1 ? g1_chunk((p / 20) % 3, p % 4) + ((p % 20) / 4) * 1024
                      : g1_chunk((p / 20) % 3, (p % 20) / 5) + (p % 5) * 1024;
}
constexpr int g1_tile(int p) { return p < 20 * NT1 ? p % NT1 : p / 20; }
constexpr int g1_kt(int p) { return p < 20 * NT1 ? p / NT1 : p % 20; }
constexpr int g1_fin(int T) { return T < NT1 ? 19 * NT1 + T : 20 * T + 19; }
constexpr int g1_vmB(int p) { return (p & 1) && p >= 17 && p < 86 && (p - 17) % 20 < 10 ? ((p - 17) / 20) * 5 + ((p - 17) % 20) / 2 : -1; }
constexpr int g1_vmL(int p) { return (p == 47 || p == 49 || p == 51 || p == 53) ? (p - 47) / 2 : -1; }
struct G1S { int vA[140], vS[140], lB[140], ws[24], cs[24], sp[3]; };
constexpr G1S g1_sched() {
  G1S S{};
  for (int w = 0; w < 24; ++w) {
    const int e = g1_fin(w >> 2) + 2;
    S.ws[w] = (w && S.ws[w - 1] + 2 > e) ? S.ws[w - 1] + 2 : e;
  }
  for (int w = 0; w < 24; ++w) S.cs[w] = S.ws[w] + G1_D + 2;
  for (int r = 0; r < 3; ++r) S.sp[r] = S.cs[8 * r + 7] + 1;
  int idxA[20] = {}, idxB[7] = {}, idxL = 0, ldB[140] = {};
  int v = 1;
  for (int h = 0; h < 3; ++h) {
    v += 5; idxB[h] = v;
    if (h < 2) for (int k = 0; k < 5; ++k) { v += 2; idxA[5 * h + k] = v; }
  }
  int l = NT1;
  for (int q = 0; q < G1_D; ++q) ldB[q] = ++l;
  for (int q = 0; q < 140; ++q) {
    const int gq = q / 20, lp = q % 20;
    S.vA[q] = -1; S.vS[q] = -1;
    if (lp == 20 - G1_D && gq < 6) {
      int need = idxB[gq + 1];
      if (gq + 1 == NT1 && idxL > need) need = idxL;
      S.vS[q] = v - need;
    }
    for (int r = 0; r < 3; ++r) if (q == S.sp[r]) v += 2;
    if (q < 20 * NT1 && q % NT1 == 0) S.vA[q] = v - idxA[q / NT1];
    if (q < 40 && (q & 1) == 0) { v += 1; idxA[10 + q / 4] = v; }
    if (g1_vmB(q) >= 0) { v += 1; idxB[3 + g1_vmB(q) / 5] = v; }
    if (g1_vmL(q) >= 0) { v += 1; idxL = v; }
    if (q + G1_D < 140) ldB[q + G1_D] = ++l;
    if (lp == 12 && gq + 1 >= NT1 && gq < 6) ++l;
    S.lB[q] = l - ldB[q];
    for (int w = 0; w < 24; ++w) if (S.ws[w] + 1 == q) l += 2;
  }
  return S;
}
constexpr G1S G1 = g1_sched();
constexpr int g1_word(int p) { for (int w = 0; w < 24; ++w) if (p >= G1.ws[w] && p < G1.ws[w] + 2) return w; return -1; }
constexpr int g1_comb(int p) { for (int w = 0; w < 24; ++w) if (p == G1.cs[w]) return w; return -1; }
static_assert(G1.sp[2] < 140 && G1.ws[0] >= 20 * NT1 - G1_D, "schedule");
#define WAIT_L(n) __builtin_amdgcn_s_waitcnt(0xC07F | (((n) > 15 ? 15 : (n)) << 8))
#define MF_ "v_mfma_f32_16x16x32_bf16 "
#define SDWA_(h) " dst_sel:DWORD dst_unused:UNUSED_PAD src0_sel:DWORD src1_sel:WORD_" #h "\n\t"
template <bool F> __device__ __forceinline__ void step_p(f4& c0, f4& c1, const u32x4& b, const u32x4& x0, const u32x4& x1, const f4& bs) {
  if constexpr (F) asm volatile(MF_ "%0, %2, %3, %5\n\t" MF_ "%1, %2, %4, %5" : "=&v"(c0), "=&v"(c1) : "v"(b), "v"(x0), "v"(x1), "v"(bs));
  else asm volatile(MF_ "%0, %2, %3, %0\n\t" MF_ "%1, %2, %4, %1" : "+v"(c0), "+v"(c1) : "v"(b), "v"(x0), "v"(x1));
}
template <bool F> __device__ __forceinline__ void step_x(f4& c0, f4& c1, const u32x4& b, const u32x4& x0, const u32x4& x1, const f4& bs,
                                                         u32& w, u32& z, u32& mx, float e0, float e1, u32 a0, u32 a1, u32 kb) {
#define X_MID "v_cvt_pk_bf16_f32 %2, %8, %9\n\tv_pk_add_u16 %3, %2, %12\n\t"
#define X_END "\n\tv_max3_u16 %4, %4, %10, %11"
  if constexpr (F) asm volatile(MF_ "%0, %5, %6, %13\n\t" X_MID MF_ "%1, %5, %7, %13" X_END
                                : "=&v"(c0), "=&v"(c1), "=&v"(w), "=&v"(z), "+v"(mx)
                                : "v"(b), "v"(x0), "v"(x1), "v"(e0), "v"(e1), "v"(a0), "v"(a1), "s"(kb), "v"(bs));
  else asm volatile(MF_ "%0, %5, %6, %0\n\t" X_MID MF_ "%1, %5, %7, %1" X_END
                    : "+v"(c0), "+v"(c1), "=&v"(w), "=&v"(z), "+v"(mx)
                    : "v"(b), "v"(x0), "v"(x1), "v"(e0), "v"(e1), "v"(a0), "v"(a1), "s"(kb));
}
template <bool F> __device__ __forceinline__ void step_y(f4& c0, f4& c1, const u32x4& b, const u32x4& x0, const u32x4& x1, const f4& bs,
                                                         u32& a0, u32& a1, u32& r0, u32& r1, u32 z) {
#define Y_MID "v_lshlrev_b32_sdwa %2, 1, %9" SDWA_(0) "v_lshlrev_b32_sdwa %3, 1, %9" SDWA_(1)
#define Y_END "\n\tds_read_u16 %4, %2\n\tds_read_u16 %5, %3"
  if constexpr (F) asm volatile(MF_ "%0, %6, %7, %10\n\t" Y_MID MF_ "%1, %6, %8, %10" Y_END
                                : "=&v"(c0), "=&v"(c1), "=&v"(a0), "=&v"(a1), "=&v"(r0), "=&v"(r1)
                                : "v"(b), "v"(x0), "v"(x1), "v"(z), "v"(bs) : "memory");
  else asm volatile(MF_ "%0, %6, %7, %0\n\t" Y_MID MF_ "%1, %6, %8, %1" Y_END
                    : "+v"(c0), "+v"(c1), "=&v"(a0), "=&v"(a1), "=&v"(r0), "=&v"(r1)
                    : "v"(b), "v"(x0), "v"(x1), "v"(z) : "memory");
}
__device__ __forceinline__ void comb(u32& r, u32 hi) { asm volatile("v_lshl_or_b32 %0, %1, 16, %0" : "+v"(r) : "v"(hi)); }
__device__ __forceinline__ void max3u(u32& mx, u32 a0, u32 a1) { asm volatile("v_max3_u16 %0, %0, %1, %2" : "+v"(mx) : "v"(a0), "v"(a1)); }
__device__ __forceinline__ void epi_word(u32& w, u32& r0, u32& r1, u32& mx, float e0, float e1, u32 kb) {
  u32 z;
  asm volatile("v_cvt_pk_bf16_f32 %0, %5, %6\n\tv_pk_add_u16 %4, %0, %7\n\t"
               "v_lshlrev_b32_sdwa %1, 1, %4" SDWA_(0) "v_lshlrev_b32_sdwa %2, 1, %4" SDWA_(1)
               "v_max3_u16 %3, %3, %1, %2\n\tds_read_u16 %1, %1\n\tds_read_u16 %2, %2"
               : "=&v"(w), "=&v"(r0), "=&v"(r1), "+v"(mx), "=&v"(z) : "v"(e0), "v"(e1), "s"(kb) : "memory");
}
__device__ __forceinline__ void mfma_c(f4& acc, const u32x4& a, const u32x4& b, const f4& c) {
  asm volatile("v_mfma_f32_16x16x32_bf16 %0, %1, %2, %3" : "=&v"(acc) : "v"(a), "v"(b), "v"(c));
}
__device__ __forceinline__ void mfma_a(f4& acc, const u32x4& a, const u32x4& b) { MFMA_BF(acc, a, b); }
template <int OFF, class V> __device__ __forceinline__ void dsr128(V& d, u32 addr) { DSR128(d, addr, OFF); }
__device__ __forceinline__ void pin2(u32x4& a, u32x4& b) { asm volatile("" : "+v"(a), "+v"(b)); }
template <int N> __device__ __forceinline__ void wait_res(u32 (&a)[4], u32 (&b)[4]) {
  asm volatile("s_waitcnt lgkmcnt(%8)"
               : "+v"(a[0]), "+v"(a[1]), "+v"(a[2]), "+v"(a[3]), "+v"(b[0]), "+v"(b[1]), "+v"(b[2]), "+v"(b[3])
               : "n"(N > 15 ? 15 : N) : "memory");
}
template <int OFF> __device__ __forceinline__ void glds_o(u32 voff, const void* sbase, u32 m) {
  asm volatile("s_mov_b32 m0, %2\n\ts_nop 0\n\tglobal_load_lds_dwordx4 %0, %1 offset:%3"
               :: "v"(voff), "s"(sbase), "s"(m), "n"(OFF) : "memory", "m0");
}
__device__ __forceinline__ void gload4(u32x4& d, u32 voff, const void* sbase) { GLOAD4(d, voff, sbase); }
__device__ __forceinline__ void st16s(u32 voff, const void* sbase, u32x4 v) {
  asm volatile("global_store_dwordx4 %0, %1, %2" STF :: "v"(voff), "v"(v), "s"(sbase) : "memory");
}

template <int KID>
__device__ __forceinline__ void ssm_block(char* shm, const int grp, const int e0, const int nch, const u16* __restrict__ xnP,
                                          const u16* __restrict__ Pg, u16* __restrict__ yT, u32* flag) {
    const int t = threadIdx.x, lane = t & 63;
    const int wave = __builtin_amdgcn_readfirstlane(t >> 6);
    STAMPB(KID, (char*)yT - (13u << 20));
    STAMP(0);
    const int eo = (e0 & ~1) > 4 ? 4 : (e0 & ~1);
    const int dbase = (grp >> 2) * 32 + (grp & 3) * 4;
    u16* Kc = (u16*)shm;
    float* red = (float*)(shm + 16896);
#pragma unroll
    for (int c = 0; c < 3; ++c) {
      if (c < nch) {
        const int e = e0 + c;
        const int d = dbase + (e >> 2) * 16 + (e & 3);
        char* buf = shm + 41472 + c * 6144;
        if (wave == 0) glds16s(lane * 16, (const char*)(Pg + (size_t)d * PPITCH), buf);
        if (wave == 1 && lane < 32) glds16s(lane * 16, (const char*)(Pg + (size_t)d * PPITCH) + 1024, buf + 1024);
      }
    }
    {
      u32x2 xv[8];
      const char* xp = (const char*)xnP + ((((size_t)(t >> 3) * 20 + (grp >> 2)) * 64 + (grp & 3) * 16 + ((2 * t) & 15)) * 16 + eo * 2);
#pragma unroll
      for (int i = 0; i < 4; ++i) {
        xv[2 * i] = *(const u32x2*)(xp + (size_t)i * 655360);
        xv[2 * i + 1] = *(const u32x2*)(xp + (size_t)i * 655360 + 16);
      }
#pragma unroll
      for (int c = 0; c < 3; ++c) {
        if (c < nch) {
          const u32 pos2 = 2 * (e0 + c - eo);
          const u32 sel = 0x00000c0cu | (pos2 << 16) | ((pos2 + 1) << 24);
          u16* xs = (u16*)(shm + 41472 + c * 6144 + 2048);
#pragma unroll
          for (int i = 0; i < 4; ++i) {
            const float x0 = __uint_as_float(__builtin_amdgcn_perm(xv[2 * i][1], xv[2 * i][0], sel));
            const float x1 = __uint_as_float(__builtin_amdgcn_perm(xv[2 * i + 1][1], xv[2 * i + 1][0], sel));
            *(u32*)&xs[2 * t + 512 * i] = pk_h(x0, x1);
          }
        }
      }
    }
    if (t < 96) *(u32x4*)(shm + 41472 + (t >> 5) * 6144 + 1536 + (t & 31) * 16) = (u32x4){0u, 0u, 0u, 0u};
    if (t < 8) *(u32x4*)&Kc[(t >> 1) * KCPITCH + (256 + (t & 1)) * 8] = (u32x4){0u, 0u, 0u, 0u};
    WAIT_V(0);
    RAW_BARRIER();
    STAMP(3);
    const int b = lane & 15, g = lane >> 4;
    const int m = 255 - t;
    const u32 lds0 = (u32)(size_t)(const __attribute__((address_space(3))) char*)shm;
    const int abase = ((3 - b) & 3) * KCPITCH + 2028 - 4 * (b >> 2) + 8 * g;
    const int bbase = 256 - 16 + 16 * b + 8 * g;
#pragma unroll 1
    for (int c = 0; c < nch; ++c) {
      const int e = e0 + c;
      const int d = dbase + (e >> 2) * 16 + (e & 3);
      const u16* P = (const u16*)(shm + 41472 + c * 6144);
      const float kscale = *(const float*)&P[704];
      {
        u32 wv[6];
        if (wave < 2) {
          const int i = 512 + m - 128;
          const u32 pc = P[i - 1], pa = P[i], pb = P[i + 1];
          const u32 aa = pa | (pa << 16);
          wv[0] = pb | (pb << 16);
          wv[1] = pb | (((m & 1) ? pb : pa) << 16);
          wv[2] = aa; wv[3] = aa; wv[4] = aa;
          wv[5] = m == 128 ? pc : pa;
        } else if (wave == 2) {
          const int i = 384 + 2 * (m - 64);
          const u32 q0 = *(const u32*)&P[i - 2], q1 = *(const u32*)&P[i], q2 = *(const u32*)&P[i + 2];
          wv[0] = __builtin_amdgcn_perm(q2, q2, 0x01000100u);
          wv[1] = __builtin_amdgcn_perm(q1, q1, 0x03020302u);
          wv[2] = __builtin_amdgcn_perm(q1, q1, 0x01000302u);
          wv[3] = __builtin_amdgcn_perm(q1, q1, 0x01000100u);
          wv[4] = m == 64 ? __builtin_amdgcn_perm(q0, q1, 0x07060100u) : wv[3];
          wv[5] = m == 64 ? q0 : (q0 >> 16);
        } else {
#pragma unroll
          for (int hh = 0; hh < 2; ++hh) {
            const int mm = m - hh;
            const int sg = mm >= 32 ? 1 : 0;
#pragma unroll
            for (int j = 0; j < (hh ? 2 : 4); ++j) {
              u32 pk = 0;
#pragma unroll
              for (int h = 0; h < 2; ++h) {
                if (hh == 0 || 2 * j + h < 3) {
                  int q = 8 * mm + 7 - (2 * j + h);
                  q = q < 0 ? 0 : q;
                  int idx = sg ? 128 + ((q + ((q >> 1) & 1)) >> 1) : q;
                  pk |= ((u32)P[idx]) << (16 * h);
                }
              }
              wv[hh * 4 + j] = (hh && mm < 0) ? 0u : pk;
            }
          }
        }
        *(u32x4*)&Kc[0 * KCPITCH + t * 8] = (u32x4){wv[0], wv[1], wv[2], wv[3]};
        *(u32x4*)&Kc[1 * KCPITCH + t * 8] = (u32x4){__builtin_amdgcn_alignbit(wv[1], wv[0], 16), __builtin_amdgcn_alignbit(wv[2], wv[1], 16),
                                                    __builtin_amdgcn_alignbit(wv[3], wv[2], 16), __builtin_amdgcn_alignbit(wv[4], wv[3], 16)};
        *(u32x4*)&Kc[2 * KCPITCH + t * 8] = (u32x4){wv[1], wv[2], wv[3], wv[4]};
        *(u32x4*)&Kc[3 * KCPITCH + t * 8] = (u32x4){__builtin_amdgcn_alignbit(wv[2], wv[1], 16), __builtin_amdgcn_alignbit(wv[3], wv[2], 16),
                                                    __builtin_amdgcn_alignbit(wv[4], wv[3], 16), __builtin_amdgcn_alignbit(wv[5], wv[4], 16)};
      }
      __syncthreads();
      if (c == 0) { STAMP(4); }
      f4 acc[8];
#pragma unroll
      for (int i = 0; i < 8; ++i) acc[i] = (f4){0.f, 0.f, 0.f, 0.f};
      {
        u32x2 a0[2][8], a1[2][8];
        u32x4 bq[2][8];
        const u32 aaddr = lds0 + (u32)(abase - 64 * wave - 32 - 256 * 7) * 2;
        const u32 baddr = lds0 + 41472 + c * 6144 + 1536 + (u32)(bbase - 64 * wave - 32) * 2;
#pragma unroll
        for (int rr = 0; rr < 2; ++rr) {
#pragma unroll
          for (int u = 0; u < 8; ++u) {
            DSR64(a0[rr][u], aaddr, (7 - u) * 512 + (1 - rr) * 64);
            DSR64(a1[rr][u], aaddr, (7 - u) * 512 + (1 - rr) * 64 + 8);
            DSR128(bq[rr][u], baddr, u * 512 + (1 - rr) * 64);
            if (rr == 1 && u == 3) {
              asm volatile("s_waitcnt lgkmcnt(12)"
                           : "+v"(a0[0][0]), "+v"(a1[0][0]), "+v"(a0[0][1]), "+v"(a1[0][1]), "+v"(a0[0][2]), "+v"(a1[0][2]),
                             "+v"(a0[0][3]), "+v"(a1[0][3]), "+v"(a0[0][4]), "+v"(a1[0][4]), "+v"(a0[0][5]), "+v"(a1[0][5]),
                             "+v"(a0[0][6]), "+v"(a1[0][6]), "+v"(a0[0][7]), "+v"(a1[0][7]),
                             "+v"(bq[0][0]), "+v"(bq[0][1]), "+v"(bq[0][2]), "+v"(bq[0][3]),
                             "+v"(bq[0][4]), "+v"(bq[0][5]), "+v"(bq[0][6]), "+v"(bq[0][7])
                           :: "memory");
            }
          }
        }
        SB0();
#pragma unroll
        for (int rr = 0; rr < 2; ++rr) {
          if (rr == 1) {
            asm volatile("s_waitcnt lgkmcnt(0)"
                         : "+v"(a0[1][0]), "+v"(a1[1][0]), "+v"(a0[1][1]), "+v"(a1[1][1]), "+v"(a0[1][2]), "+v"(a1[1][2]),
                           "+v"(a0[1][3]), "+v"(a1[1][3]), "+v"(a0[1][4]), "+v"(a1[1][4]), "+v"(a0[1][5]), "+v"(a1[1][5]),
                           "+v"(a0[1][6]), "+v"(a1[1][6]), "+v"(a0[1][7]), "+v"(a1[1][7]),
                           "+v"(bq[1][0]), "+v"(bq[1][1]), "+v"(bq[1][2]), "+v"(bq[1][3]),
                           "+v"(bq[1][4]), "+v"(bq[1][5]), "+v"(bq[1][6]), "+v"(bq[1][7])
                         :: "memory");
          }
#pragma unroll
          for (int u = 0; u < 8; ++u) {
            const h8 A = __builtin_bit_cast(h8, (u32x4){a0[rr][u][0], a0[rr][u][1], a1[rr][u][0], a1[rr][u][1]});
#pragma unroll
            for (int v = 0; v + u < 8; ++v)
              acc[u + v] = __builtin_amdgcn_mfma_f32_16x16x32_f16(A, __builtin_bit_cast(h8, bq[rr][v]), acc[u + v], 0, 0, 0);
          }
          SB0();
        }
      }
      if (c == 0) { STAMP(5); }
      auto red_phase = [&](auto WC) {
        constexpr int W = decltype(WC)::value;
#pragma unroll
        for (int o = 1; o < 4; ++o) {
          const int ow = (W + o) & 3;
#pragma unroll
          for (int i = 0; i < 2; ++i) *(f4*)&red[(((ow * 3 + (o - 1)) * 2 + i) * 64 + lane) * 4] = acc[ow * 2 + i];
        }
        __syncthreads();
#pragma unroll
        for (int i = 0; i < 2; ++i) {
          f4 s1 = *(const f4*)&red[(((W * 3 + 0) * 2 + i) * 64 + lane) * 4];
          f4 s2 = *(const f4*)&red[(((W * 3 + 1) * 2 + i) * 64 + lane) * 4];
          f4 s3 = *(const f4*)&red[(((W * 3 + 2) * 2 + i) * 64 + lane) * 4];
          f4 sum = ((acc[W * 2 + i] + s1) + (s2 + s3)) * kscale;
          u32x2 o = {pk_bf(sum[0], sum[1]), pk_bf(sum[2], sum[3])};
          st8(yT + (size_t)d * SEQ + 256 * (W * 2 + i) + 16 * b + 4 * g, o);
        }
      };
      if (wave == 0) red_phase(IC<0>{});
      else if (wave == 1) red_phase(IC<1>{});
      else if (wave == 2) red_phase(IC<2>{});
      else red_phase(IC<3>{});
      if (c == 0) { STAMP(6); }
      if (c == 1) { STAMP(7); }
    }
    if (flag) {
      WAIT_V(0);
      if (lane == 0) st4(flag + wave * 256, 1u);
    }
    STAMP(8);
    STAMPW();
}

#define K3R 3
#define K3SLEEP1 8
#define K3YS 14
__device__ __forceinline__ void glds16c(const void* gsrc, const void* lp) {
  unsigned keep;
  unsigned lds_dst = __builtin_amdgcn_readfirstlane((unsigned)(size_t)(const __attribute__((address_space(3))) char*)lp);
  asm volatile("s_mov_b32 %0, m0\n\ts_mov_b32 m0, %2\n\ts_nop 0\n\tglobal_load_lds_dwordx4 %1, off sc1\n\ts_mov_b32 m0, %0"
               : "=&s"(keep) : "v"(gsrc), "s"(lds_dst) : "memory");
}
#define OUTF ""
__device__ __forceinline__ void so16(void* p, u32x4 v) { asm volatile("global_store_dwordx4 %0, %1, off" OUTF :: "v"(p), "v"(v) : "memory"); }
__device__ __forceinline__ void so8(void* p, u32x2 v) { asm volatile("global_store_dwordx2 %0, %1, off" OUTF :: "v"(p), "v"(v) : "memory"); }
__device__ __forceinline__ void g2_block(char* shm, const int bid, char* ws, const u16* __restrict__ xin,
                                         const u16* __restrict__ dskip, u16* __restrict__ out) {
  constexpr int R = K3R;
  constexpr int NF = 12, NST = 15;
  const u16* w2F = (const u16*)(ws + WS_W2F);
  const u16* w2H = (const u16*)(ws + WS_W2H);
  const float2* stats = (const float2*)(ws + WS_STATS);
  const u16* yT = (const u16*)(ws + WS_YT);
  const u32* flags = (const u32*)(ws + WS_FLAGS);
  const int t = threadIdx.x, lane = t & 63;
  const int wave = __builtin_amdgcn_readfirstlane(t >> 6);
  const int xcd = bid & 7, idx = bid >> 3;
  const int mi = idx & 7, ni = idx >> 3;
  const int bm = (xcd >> 1) * 8 + mi, bn = (xcd & 1) * 4 + ni;
  const int mt0 = bm * 4, m0 = bm * 64, n0 = bn * 80, nt0 = bn * 5;
  STAMPB(3, ws);
  STAMP(0);
  const char* abase = ws + WS_ACTF + ((size_t)mt0 * 48 + wave) * 1024;
  const char* bbase = (const char*)w2F + ((size_t)nt0 * 48 + wave) * 1024;
  const char* ahb = ws + WS_ACTH + ((size_t)(mt0 >> 1) * 12 + wave) * 1024;
  const char* bhb = (const char*)w2H + ((size_t)nt0 * 12 + wave) * 512;
  u32 va[4], vb[5], vh[2];
#pragma unroll
  for (int j = 0; j < 5; ++j) vb[j] = lane * 16 + j * (48 * 1024);
  u32x4 fa[R][4], fb[R][5];
  u32x4 ha[3][2];
  u32x2 hb[3][5];
  auto issue = [&](int s, int what = 3) {
    if (s < NF) {
      const char* ap = abase + s * 4096;
      const char* bp = bbase + s * 4096;
      if (what & 1) {
#pragma unroll
        for (int i = 0; i < 4; ++i) fa[s % R][i] = *(const u32x4*)(ap + va[i]);
      }
      if (what & 2) {
#pragma unroll
        for (int j = 0; j < 5; ++j) fb[s % R][j] = *(const u32x4*)(bp + vb[j]);
      }
    } else {
      const int h = s - NF;
#pragma unroll
      for (int i = 0; i < 2; ++i) ha[h][i] = *(const u32x4*)(ahb + h * 4096 + vh[i]);
#pragma unroll
      for (int j = 0; j < 5; ++j) hb[h][j] = *(const u32x2*)(bhb + h * 2048 + (lane * 8 + j * (12 * 512)));
    }
  };
  auto aux = [&]() {
#pragma unroll
    for (int i = 0; i < 3; ++i) {
      int p = wave + 4 * i;
      if (p > 10) p = 10;
      const int c = p * 64 + lane;
      const u16* src;
      if (p < 10) src = xin + (size_t)(m0 + c / 10) * DM + n0 + (c % 10) * 8;
      else src = (lane < 32) ? (const u16*)(stats + m0) + lane * 8 : dskip + n0 + (lane < 42 ? lane - 32 : 9) * 8;
      GLDS(src, shm + (p < 10 ? p : 20) * 1024);
    }
  };
  auto yload = [&]() {
    const u32* fp = flags + (bm >> 3) * 256 + ((n0 >> 5) * 4) * 3 + (lane < 12 * 3 ? lane : 0);
#define K3SLEEP 16
    if (wave == 0)
    for (int it = 0; it < (1 << 20); ++it) {
      u32 f;
      asm volatile("global_load_dword %0, %1, off sc1\n\ts_waitcnt vmcnt(0)" : "=v"(f) : "v"(fp) : "memory");
      if (__builtin_amdgcn_ballot_w64(f != 1u) == 0) break;
      __builtin_amdgcn_s_sleep(K3SLEEP);
    }
    __builtin_amdgcn_s_barrier();
#pragma unroll
    for (int i = 0; i < 3; ++i) {
      int p = wave + 4 * i;
      if (p > 9) p = 9;
      const int c = p * 64 + lane;
      glds16c(yT + (size_t)(n0 + (c >> 3)) * SEQ + m0 + (c & 7) * 8, shm + (10 + p) * 1024);
    }
  };
  SB0();
#pragma unroll
  for (int s = 0; s < R; ++s) { issue(s, 2); SB0(); }
  aux();
  SB0();
  if (wave == 0) {
    const u32* fp = flags + 1024 + bm * 32 + (lane & 31);
    for (int it = 0; it < (1 << 20); ++it) {
      u32 f;
      asm volatile("global_load_dword %0, %1, off sc1\n\ts_waitcnt vmcnt(0)" : "=v"(f) : "v"(fp) : "memory");
      if (__builtin_amdgcn_ballot_w64(f != 1u) == 0) break;
      __builtin_amdgcn_s_sleep(K3SLEEP1);
    }
  }
  __builtin_amdgcn_s_barrier();
  {
    u32 l16 = lane * 16;
    asm volatile("" : "+v"(l16) :: "memory");
#pragma unroll
    for (int i = 0; i < 4; ++i) va[i] = l16 + ((wave + i) & 3) * (48 * 1024);
#pragma unroll
    for (int i = 0; i < 2; ++i) vh[i] = l16 + (((wave >> 1) + i) & 1) * (12 * 1024);
  }
  STAMP(10);
  SB0();
#pragma unroll
  for (int s = 0; s < R; ++s) { issue(s, 1); SB0(); }
  STAMP(1);
  const int fr = lane & 15, g = lane >> 4, tq = (lane & 15) >> 2, tp = lane & 3;
  const int ml = wave * 16 + fr;
  u32x2 s1p[5];
  float2 st;
  auto resid = [&](int j) {
    const char* sx = shm;
    const char* sm = shm + 20 * 1024;
    if (j == 0) st = *(const float2*)(sm + ml * 8);
    const int nl = j * 16;
    u32x2 xx = *(const u32x2*)(sx + ml * 160 + (nl + 4 * g) * 2);
    u32x2 dd = *(const u32x2*)(sm + 512 + (nl + 4 * g) * 2);
#pragma unroll
    for (int h = 0; h < 2; ++h) {
      const float x0 = lo_f(xx[h]), x1 = hi_f(xx[h]);
      u32 d = pk_bf(x0 - st.x, x1 - st.x);
      u32 xn = pk_bf(lo_f(d) * st.y, hi_f(d) * st.y);
      s1p[j][h] = pk_bf(lo_f(xn) * lo_f(dd[h]), hi_f(xn) * hi_f(dd[h]));
    }
    asm volatile("" : "+v"(s1p[j]));
  };
  auto resid2 = [&](int j) {
    const char* sx = shm;
    const char* sy = shm + 10 * 1024;
    const int nl = j * 16;
    u32x2 xx = *(const u32x2*)(sx + ml * 160 + (nl + 4 * g) * 2);
    s4 yy = TRREAD(sy + (nl + 4 * g + tq) * 128 + (wave * 16 + 4 * tp) * 2);
    const u32x2 yw = __builtin_bit_cast(u32x2, yy);
#pragma unroll
    for (int h = 0; h < 2; ++h) {
      const float x0 = lo_f(xx[h]), x1 = hi_f(xx[h]);
      u32 a = pk_bf(lo_f(yw[h]) + lo_f(s1p[j][h]), hi_f(yw[h]) + hi_f(s1p[j][h]));
      s1p[j][h] = pk_bf(x0 + lo_f(a), x1 + hi_f(a));
    }
  };
  f4 acc[4][5];
#pragma unroll
  for (int i = 0; i < 4; ++i)
#pragma unroll
    for (int j = 0; j < 5; ++j) acc[i][j] = (f4){0.f, 0.f, 0.f, 0.f};
  const bool odd = wave & 1;
  SB0();
#pragma unroll
  for (int s = 0; s < NST; ++s) {
    if (s < NF) {
#pragma unroll
      for (int i = 0; i < 4; ++i)
#pragma unroll
        for (int j = 0; j < 5; ++j)
          acc[i][j] = __builtin_amdgcn_mfma_f32_16x16x32_bf16(__builtin_bit_cast(s8, fb[s % R][j]),
                                                              __builtin_bit_cast(s8, fa[s % R][i]), acc[i][j], 0, 0, 0);
    } else {
      const int h = s - NF;
      u32x2 af[4];
      af[0] = odd ? (u32x2){ha[h][0][2], ha[h][0][3]} : (u32x2){ha[h][0][0], ha[h][0][1]};
      af[1] = odd ? (u32x2){ha[h][1][0], ha[h][1][1]} : (u32x2){ha[h][0][2], ha[h][0][3]};
      af[2] = odd ? (u32x2){ha[h][1][2], ha[h][1][3]} : (u32x2){ha[h][1][0], ha[h][1][1]};
      af[3] = odd ? (u32x2){ha[h][0][0], ha[h][0][1]} : (u32x2){ha[h][1][2], ha[h][1][3]};
#pragma unroll
      for (int i = 0; i < 4; ++i)
#pragma unroll
        for (int j = 0; j < 5; ++j)
          acc[i][j] = __builtin_amdgcn_mfma_f32_16x16x16bf16_1k(__builtin_bit_cast(s4, hb[h][j]),
                                                               __builtin_bit_cast(s4, af[i]), acc[i][j], 0, 0, 0);
    }
    SB0();
    if (s + R < NST) issue(s + R);
    SB0();
    if (s == 4) {
      WAIT_V(R * 9 > 60 ? 60 : R * 9);
      __builtin_amdgcn_s_barrier();
      SB0();
    }
    if (s >= 5 && s < 10) { resid(s - 5); SB0(); }
    if (s == 10) { __builtin_amdgcn_s_barrier(); SB0(); }
    if (s == K3YS) { yload(); SB0(); STAMP(3); }
    if (s == 0) { STAMP(2); }
    if (s == 2) { STAMP(4); }
    if (s == 6) { STAMP(5); }
    if (s == 10) { STAMP(6); }
  }
  STAMP(7);
  char* red = shm + 20 * 1024;
#pragma unroll
  for (int i = 1; i < 4; ++i)
#pragma unroll
    for (int j = 0; j < 5; ++j)
      *(f4*)(red + ((((wave + i) & 3) * 3 + (i - 1)) * 5 + j) * 1024 + lane * 16) = acc[i][j];
  WAIT_V(0);
  __syncthreads();
  STAMP(9);
  u32x2 ov[5];
#pragma unroll
  for (int j = 0; j < 5; ++j) {
    resid2(j);
    f4 p0 = *(const f4*)(red + ((wave * 3 + 0) * 5 + j) * 1024 + lane * 16);
    f4 p1 = *(const f4*)(red + ((wave * 3 + 1) * 5 + j) * 1024 + lane * 16);
    f4 p2 = *(const f4*)(red + ((wave * 3 + 2) * 5 + j) * 1024 + lane * 16);
    f4 sum = (acc[0][j] + p0) + (p1 + p2);
    u32 m0p = pk_bf(sum[0], sum[1]), m1p = pk_bf(sum[2], sum[3]);
    ov[j] = (u32x2){pk_bf(lo_f(s1p[j][0]) + lo_f(m0p), hi_f(s1p[j][0]) + hi_f(m0p)),
                    pk_bf(lo_f(s1p[j][1]) + lo_f(m1p), hi_f(s1p[j][1]) + hi_f(m1p))};
  }
  u16* orow = out + (size_t)(m0 + ml) * DM + n0;
#pragma unroll
  for (int jp = 0; jp < 2; ++jp) {
    auto r0 = __builtin_amdgcn_permlane16_swap(ov[2 * jp][0], ov[2 * jp + 1][0], false, false);
    auto r1 = __builtin_amdgcn_permlane16_swap(ov[2 * jp][1], ov[2 * jp + 1][1], false, false);
    so16(orow + (2 * jp + (g & 1)) * 16 + 8 * (g >> 1), (u32x4){r0[0], r1[0], r0[1], r1[1]});
  }
  so8(orow + 64 + 4 * g, ov[4]);
  STAMP(8);
  STAMPW();
}

extern "C" __global__ __launch_bounds__(256, 2) void k2_kernel(char* ws, const u16* __restrict__ xin,
                                                    const u16* __restrict__ dskip, u16* __restrict__ out) {
  const u16* xnP = (const u16*)(ws + WS_XNP);
  const u16* w1P = (const u16*)(ws + WS_W1P);
  const float* b1f = (const float*)(ws + WS_B1F);
  const u16* lut = (const u16*)(ws + WS_LUT);
  u16* actF = (u16*)(ws + WS_ACTF);
  u16* actH = (u16*)(ws + WS_ACTH);
  u32* flags = (u32*)(ws + WS_FLAGS);
  __shared__ __attribute__((aligned(1024))) char shm[81920];
  const int t = threadIdx.x, lane = t & 63;
  const int wave = __builtin_amdgcn_readfirstlane(t >> 6);
  const int vb = blockIdx.x;
  if (vb >= K2_G1) {
    const int sb = vb - K2_G1, li = sb >> 3, grp = (sb & 7) * 10 + li / 3, part = li % 3;
    ssm_block<2>(shm, grp, 3 * part, part < 2 ? 3 : 2, xnP, (const u16*)(ws + WS_PG), (u16*)(ws + WS_YT), flags + grp * 3 + part);
    return;
  }
  STAMPB(2, (char*)actF - (16u << 20));
  STAMP(0);
  const int xcd = vb & 7, idx = vb >> 3;
  const int mi = idx & 3, ni = idx >> 2;
  const int bm = (xcd >> 1) * 4 + mi, bn = (xcd & 1) * 8 + ni;
  const int mtw = bm * 8 + 2 * wave, nt0 = 27 * (bn >> 2) + 7 * (bn & 3);
  const char* abase = (const char*)xnP + (size_t)mtw * (20 * 1024);
  const u32 va0 = lane * 16, va1 = lane * 16 + 20 * 1024;
  const u32 lds0 = (u32)(size_t)(const __attribute__((address_space(3))) char*)shm;
  const u32 va4 = lane * 16 + 4096;
  const char* bb1 = (const char*)w1P + (size_t)((nt0 + wave) * 20) * 1024;
  const char* bb2 = (const char*)w1P + (size_t)(nt0 * 20 + 5 * wave) * 1024;
  const u32 mc0 = lds0 + 8192 + wave * 5120;
  const u32 mc2 = wave == 3 ? lds0 + 73728 : mc0 + 40960;
  auto stageB = [&](auto GC, auto IC_) {
    constexpr int gq = decltype(GC)::value, i = decltype(IC_)::value;
    const char* src = gq < NT1 ? bb1 + gq * 5120 : bb2 + (size_t)((gq == 6 && bn == 15) ? 5 : gq) * 20480;
    const u32 m = gq % 3 == 2 ? mc2 : mc0 + (gq % 3) * 20480;
    if constexpr (i < 4) glds_o<i * 1024>(va0, src, m);
    else glds_o<0>(va4, src, m + 4096);
  };
  u32x4 fa[20][2];
  auto loadA = [&](int s) {
    const char* ap = abase + s * 1024;
    GLOAD4(fa[s][0], va0, ap);
    GLOAD4(fa[s][1], va1, ap);
  };
  glds_o<0>((lane < 28 ? lane : 27) * 16, (const char*)(b1f + nt0 * 16), lds0 + BIAS_OFF);
  static_for<5>([&](auto I) { stageB(IC<0>{}, I); });
#pragma unroll
  for (int s = 0; s < 5; ++s) loadA(s);
  static_for<5>([&](auto I) { stageB(IC<1>{}, I); });
#pragma unroll
  for (int s = 5; s < 10; ++s) loadA(s);
  static_for<5>([&](auto I) { stageB(IC<2>{}, I); });
  STAMP(1);
  WAIT_V(30);
  __builtin_amdgcn_s_barrier();
  STAMP(2);
  typedef unsigned short us2 __attribute__((ext_vector_type(2)));
  const u32 lb = lds0 + lane * 16, lbh = lds0 + lane * 16 + 65536, lbb = lds0 + BIAS_OFF + (lane >> 4) * 16;
  f4 acc[7][2];
  u32x4 fb[G1_D + 1];
  f4 bias[7];
  u32 hw[7][4];
  u32 res[7][4], rhi[7][4];
  u32 mx[4] = {0u, 0u, 0u, 0u}, zz, a0 = 0, a1 = 0;
  auto readB = [&](auto PC) {
    constexpr int q = decltype(PC)::value, off = g1_piece_lds(q);
    if constexpr (off < 65536) dsr128<off>(fb[q % (G1_D + 1)], lb);
    else dsr128<off - 65536>(fb[q % (G1_D + 1)], lbh);
  };
  const u32 kb = (0x10000u - (LUT_E0 << 7)) * 0x10001u;
  auto epi_fix = [&](int T) __attribute__((always_inline)) {
#pragma unroll
    for (int q = 0; q < 4; ++q) {
      const u32 z0 = hw[T][q] & 0x7FFFu, z1 = (hw[T][q] >> 16) & 0x7FFFu;
      const bool b0 = (z0 - (LUT_E0 << 7)) >= (u32)LUT_HALF, b1v = (z1 - (LUT_E0 << 7)) >= (u32)LUT_HALF;
      const float h0 = lo_f(hw[T][q]), h1 = hi_f(hw[T][q]);
      const float f0 = (z0 < (LUT_E0 << 7)) ? h0 * 0.59765625f : (h0 > 0.f ? h0 : h0 * 0.0f);
      const float f1 = (z1 < (LUT_E0 << 7)) ? h1 * 0.59765625f : (h1 > 0.f ? h1 : h1 * 0.0f);
      const u32 fw = pk_bf(f0, f1);
      res[T][q] = ((b0 ? fw : res[T][q]) & 0xFFFFu) | ((b1v ? fw : res[T][q]) & 0xFFFF0000u);
    }
  };
  const char* obase = (const char*)actF + ((size_t)mtw * 48 + 3 * bn) * 1024;
#pragma unroll
  for (int T = 0; T < NT1; ++T) DSR128(bias[T], lbb, T * 64);
  static_for<G1_D>(readB);
  static_for<140>([&](auto PC) {
    constexpr int p = decltype(PC)::value;
    constexpr int T = g1_tile(p), kt = g1_kt(p), gq = p / 20, lp = p % 20;
    if constexpr (G1.vS[p] >= 0) {
      WAIT_L(0);
      WAIT_V(G1.vS[p]);
      __builtin_amdgcn_s_barrier();
      if (gq == 0) { STAMP(3); }
      if (gq == 1) { STAMP(4); }
      if (gq == 3) { STAMP(5); }
      if (gq == 5) { STAMP(6); }
    }
    static_for<3>([&](auto RC) {
      constexpr int r = decltype(RC)::value;
      if constexpr (p == G1.sp[r]) {
        if constexpr (!(r < 2 && G1.ws[r < 2 ? 8 * r + 8 : 0] < p)) max3u(mx[r], a0, a1);
        if (__builtin_expect(__builtin_amdgcn_ballot_w64((mx[r] & 0xFFFFu) >= 2u * LUT_HALF) != 0, 0)) {
          epi_fix(2 * r);
          epi_fix(2 * r + 1);
        }
        SB0();
#pragma unroll
        for (int i = 0; i < 2; ++i)
          st16s(va0, obase + (i * 48 + r) * 1024,
                (u32x4){res[2 * r][2 * i], res[2 * r][2 * i + 1], res[2 * r + 1][2 * i], res[2 * r + 1][2 * i + 1]});
      }
    });
    if constexpr (G1.vA[p] >= 0) {
      WAIT_V(G1.vA[p]);
      pin2(fa[kt][0], fa[kt][1]);
    }
    if constexpr (p < 40 && (p & 1) == 0) gload4(fa[10 + p / 4][(p >> 1) & 1], (p & 2) ? va1 : va0, abase + (10 + p / 4) * 1024);
    if constexpr (g1_vmB(p) >= 0) stageB(IC<3 + g1_vmB(p) / 5>{}, IC<g1_vmB(p) % 5>{});
    if constexpr (g1_vmL(p) >= 0) {
      glds_o<g1_vmL(p) * 1024>(va0, (const char*)lut + wave * 4096, lds0 + (wave & 1) * 4096 + (wave >> 1) * LUT_NEG);
    }
    if constexpr (p + G1_D < 140) readB(IC<p + G1_D>{});
    if constexpr (lp == 12 && gq + 1 >= NT1 && gq < 6) dsr128<(gq + 1) * 64>(bias[gq + 1], lbb);
    WAIT_L(G1.lB[p]);
    SB0();
    constexpr int w = g1_word(p), wT = w >= 0 ? w >> 2 : 0, wq = w & 3;
    if constexpr (w < 0) step_p<kt == 0>(acc[T][0], acc[T][1], fb[p % (G1_D + 1)], fa[kt][0], fa[kt][1], bias[T]);
    else if constexpr (p == G1.ws[w])
      step_x<kt == 0>(acc[T][0], acc[T][1], fb[p % (G1_D + 1)], fa[kt][0], fa[kt][1], bias[T], hw[wT][wq], zz, mx[w > 0 ? (w - 1) >> 3 : 0],
                      acc[wT][wq >> 1][2 * (wq & 1)], acc[wT][wq >> 1][2 * (wq & 1) + 1], a0, a1, kb);
    else step_y<kt == 0>(acc[T][0], acc[T][1], fb[p % (G1_D + 1)], fa[kt][0], fa[kt][1], bias[T], a0, a1, res[wT][wq], rhi[wT][wq], zz);
    constexpr int cw = g1_comb(p);
    if constexpr (cw >= 0) comb(res[cw >> 2][cw & 3], rhi[cw >> 2][cw & 3]);
    SB0();
  });
  STAMP(7);
  asm volatile("s_nop 7\n\ts_nop 7\n\ts_nop 7" : "+v"(acc[6][0]), "+v"(acc[6][1]) :: "memory");
  if ((bn & 3) != 3) {
#pragma unroll
    for (int q = 0; q < 4; ++q) epi_word(hw[6][q], res[6][q], rhi[6][q], mx[3], acc[6][q >> 1][2 * (q & 1)], acc[6][q >> 1][2 * (q & 1) + 1], kb);
    asm volatile("s_waitcnt lgkmcnt(0)" : "+v"(res[6][0]), "+v"(res[6][1]), "+v"(res[6][2]), "+v"(res[6][3]),
                 "+v"(rhi[6][0]), "+v"(rhi[6][1]), "+v"(rhi[6][2]), "+v"(rhi[6][3]) :: "memory");
#pragma unroll
    for (int q = 0; q < 4; ++q) res[6][q] |= rhi[6][q] << 16;
    if (__builtin_expect(__builtin_amdgcn_ballot_w64((mx[3] & 0xFFFFu) >= 2u * LUT_HALF) != 0, 0)) epi_fix(6);
    st16s(va0, (const char*)actH + ((size_t)(mtw >> 1) * 12 + 3 * (bn >> 2) + (bn & 3)) * 1024,
          (u32x4){res[6][0], res[6][1], res[6][2], res[6][3]});
  }
  STAMP(8);
  WAIT_V(0);
  if (lane == 0) st4(flags + 1024 + (bm * 4 + wave) * 16 + bn, 1u);
  STAMP(11);
  STAMPW();
  RAW_BARRIER();
  g2_block(shm, vb, ws, xin, dskip, out);
}
extern "C" __global__ void k4_end() {}
extern "C" void kernel_launch(void* const* d_in, const int* in_sizes, int n_in,
                              void* d_out, int out_size, void* d_ws, size_t ws_size,
                              hipStream_t stream) {
  const u16* u = (const u16*)d_in[0];
  const u16* lar = (const u16*)d_in[1];
  const u16* aim = (const u16*)d_in[2];
  const u16* cre = (const u16*)d_in[3];
  const u16* cim = (const u16*)d_in[4];
  const u16* logdt = (const u16*)d_in[5];
  const u16* dskip = (const u16*)d_in[6];
  const u16* w1 = (const u16*)d_in[8];
  const u16* b1 = (const u16*)d_in[9];
  const u16* w2 = (const u16*)d_in[10];
  u16* out = (u16*)d_out;
  char* ws = (char*)d_ws;
  if (ws_size < (32u << 20)) return;
  u16* xnP = (u16*)(ws);
  u16* w1P = (u16*)(ws + (4u << 20));
  float2* stats = (float2*)(ws + (12u << 20) + 4096);
  u16* lut = (u16*)(ws + (12u << 20) + 32768);
  float* b1f = (float*)(ws + (12u << 20) + 65536);
  u32* flags = (u32*)(ws + (12u << 20) + 98304);
  u16* yT = (u16*)(ws + (13u << 20));
  u16* actF = (u16*)(ws + (16u << 20));
  u16* actH = (u16*)(ws + (22u << 20));
  u16* w2F = (u16*)(ws + (24u << 20));
  u16* w2H = (u16*)(ws + (26u << 20));
  u16* Pg = (u16*)(ws + (28u << 20));

  k1_kernel<<<K1_LN + K1_KG + K1_RP + K1_RQ + K1_LUT, 256, 0, stream>>>(u, xnP, stats, lut, w1, w1P, w2, w2F, w2H, b1, b1f,
                                                                        lar, aim, cre, cim, logdt, Pg, flags);
  k2_kernel<<<K2_G1 + K2_SSM, 256, 0, stream>>>(ws, u, dskip, out);
}
```

```cpp
#include <hip/hip_runtime.h>
#include <hip/hip_bf16.h>

#define SEQ 2048
#define DM 640
#define NS 128
#define FF 1728

typedef unsigned short u16;
typedef short s4 __attribute__((ext_vector_type(4)));
typedef short s8 __attribute__((ext_vector_type(8)));
typedef _Float16 h8 __attribute__((ext_vector_type(8)));
typedef float f4 __attribute__((ext_vector_type(4)));
typedef unsigned int u32;
typedef unsigned int u32x4 __attribute__((ext_vector_type(4)));
typedef unsigned int u32x2 __attribute__((ext_vector_type(2)));

__device__ __forceinline__ float bf2f(u16 v) { return __uint_as_float(((u32)v) << 16); }
__device__ __forceinline__ u16 f2bf(float f) {
  u32 u = __float_as_uint(f);
  u32 r = (u + 0x7FFFu + ((u >> 16) & 1u)) >> 16;
  return (u16)r;
}
__device__ __forceinline__ float bfr(float f) { return bf2f(f2bf(f)); }

#define STAMPB(kid, base)
#define STAMP(k)
#define STAMPW()
typedef __bf16 bf2_t __attribute__((ext_vector_type(2)));
typedef float f2_t __attribute__((ext_vector_type(2)));
__device__ __forceinline__ u32 pk_bf(float a, float b) {
  f2_t v = {a, b};
  bf2_t r = __builtin_convertvector(v, bf2_t);
  return __builtin_bit_cast(u32, r);
}
__device__ __forceinline__ float lo_f(u32 w) { return __uint_as_float(w << 16); }
__device__ __forceinline__ float hi_f(u32 w) { return __uint_as_float(w & 0xFFFF0000u); }

#define WAIT_V(n) asm volatile("s_waitcnt vmcnt(%0)" ::"n"(n) : "memory")
#define RAW_BARRIER() do { asm volatile("s_waitcnt lgkmcnt(0)" ::: "memory"); __builtin_amdgcn_s_barrier(); } while (0)
__device__ __forceinline__ void glds16(const void* gsrc, const void* lp) {
  unsigned keep;
  unsigned lds_dst = (unsigned)(size_t)(const __attribute__((address_space(3))) char*)lp;
  asm volatile("s_mov_b32 %0, m0\n\ts_mov_b32 m0, %2\n\ts_nop 0\n\tglobal_load_lds_dwordx4 %1, off\n\ts_mov_b32 m0, %0"
               : "=&s"(keep) : "v"(gsrc), "s"(lds_dst) : "memory");
}
#define GLDS(gp, lp) glds16((const void*)(gp), (const void*)(lp))

#define STF " sc1"
__device__ __forceinline__ void st16(void* p, u32x4 v) { asm volatile("global_store_dwordx4 %0, %1, off" STF :: "v"(p), "v"(v) : "memory"); }
__device__ __forceinline__ void st8(void* p, u32x2 v) { asm volatile("global_store_dwordx2 %0, %1, off" STF :: "v"(p), "v"(v) : "memory"); }
__device__ __forceinline__ void st4(void* p, u32 v) { asm volatile("global_store_dword %0, %1, off" STF :: "v"(p), "v"(v) : "memory"); }
__device__ __forceinline__ void st2(void* p, u32 v) { asm volatile("global_store_short %0, %1, off" STF :: "v"(p), "v"(v) : "memory"); }

#define LUT_E0 102
#define LUT_E1 134
#define LUT_HALF ((LUT_E1 - LUT_E0) * 128)
#define LUT_N (2 * LUT_HALF)

__device__ __forceinline__ float mish_bf(float h) {
  float e = bfr(expf(-fabsf(h)));
  float l = bfr(log1pf(e));
  float sp = bfr(l + (h > 0.f ? h : 0.f));
  float th = bfr(tanhf(sp));
  return bfr(h * th);
}

#define KRLEN 2080
#define WFENCE() do { asm volatile("" ::: "memory"); __builtin_amdgcn_wave_barrier(); asm volatile("" ::: "memory"); } while (0)
__device__ __forceinline__ void cexp_p(float arl2, float w, float p, float& re, float& im) {
  float mag = __builtin_amdgcn_exp2f(arl2 * p);
  float rev = w * p;
  rev = rev - floorf(rev);
  re = mag * __builtin_amdgcn_cosf(rev);
  im = mag * __builtin_amdgcn_sinf(rev);
}
__device__ __forceinline__ void cmul(float ar, float ai, float br, float bi, float& cr, float& ci) {
  float r = fmaf(ar, br, -(ai * bi));
  float i = fmaf(ar, bi, ai * br);
  cr = r; ci = i;
}
__device__ __forceinline__ u32x2 split2(float a, float b) {
  u32 h = pk_bf(a, b);
  u32 l = pk_bf(a - lo_f(h), b - hi_f(h));
  return (u32x2){h, l};
}

#define CODE_TOUCH(sym0, sym1, line, d0) do { \
    unsigned long long pc_; int o0_, o1_; \
    asm volatile("s_getpc_b64 %0\n.Lpc_here%=:\n\ts_mov_b32 %1, " sym0 "-.Lpc_here%=\n\ts_mov_b32 %2, " sym1 "-.Lpc_here%=" \
                 : "=s"(pc_), "=s"(o0_), "=s"(o1_)); \
    int len_ = o1_ - o0_; \
    if (len_ < 4 || len_ > 65536) { len_ = 4; o0_ = 0; } \
    const char* cb_ = (const char*)(pc_ + (long long)o0_); \
    const int oa_ = (line) * 128 < len_ - 4 ? (line) * 128 : len_ - 4; \
    asm volatile("global_load_dword %0, %1, %2" : "=v"(d0) : "v"(oa_ & ~3), "s"(cb_) : "memory"); \
  } while (0)

#define WS_XNP 0u
#define WS_W1P (4u << 20)
#define WS_STATS ((12u << 20) + 4096)
#define WS_LUT ((12u << 20) + 32768)
#define WS_B1F ((12u << 20) + 65536)
#define WS_FLAGS ((12u << 20) + 98304)
#define WS_YT (13u << 20)
#define WS_ACTF (16u << 20)
#define WS_ACTH (22u << 20)
#define WS_W2F (24u << 20)
#define WS_W2H (26u << 20)
#define WS_PG (28u << 20)
#define K1_LN 256
#define K1_KG 640
#define K1_RP 135
#define K1_RQ 160
#define PPITCH 768
#define K1_LUT 32
#define WPITCH 544
typedef _Float16 h2_t __attribute__((ext_vector_type(2)));
__device__ __forceinline__ u32 pk_h(float a, float b) {
  f2_t v = {a, b};
  h2_t r = __builtin_convertvector(v, h2_t);
  return __builtin_bit_cast(u32, r);
}
#define TRREAD(p) __builtin_amdgcn_ds_read_tr16_b64_v4i16((__attribute__((address_space(3))) s4*)(p))

__device__ __forceinline__ void repack_load(const u16* __restrict__ w, int N, int ku, int nu, char* lds, int tl) {
#pragma unroll
  for (int i = 0; i < 2; ++i) {
    int idx = tl + 256 * i, r = idx >> 3, c = idx & 7;
    u32x4 v = *(const u32x4*)(w + (size_t)(ku * 64 + r) * N + nu * 64 + c * 8);
    *(u32x4*)(lds + r * 160 + c * 16) = v;
  }
}
__device__ __forceinline__ void repack_store(u16* __restrict__ wP, int KB, int ku, int nu, const char* lds, int tl) {
  const int wl = tl >> 6, lane = tl & 63, g = lane >> 4, tq = (lane & 15) >> 2, tp = lane & 3;
#pragma unroll
  for (int kb2 = 0; kb2 < 2; ++kb2) {
    const char* p0 = lds + (kb2 * 32 + 4 * g + tq) * 160 + wl * 32 + tp * 8;
    s4 b0 = TRREAD(p0);
    s4 b1 = TRREAD(p0 + 16 * 160);
    s8 bf = {b0[0], b0[1], b0[2], b0[3], b1[0], b1[1], b1[2], b1[3]};
    st16(wP + (((size_t)(nu * 4 + wl) * KB + ku * 2 + kb2) * 64 + lane) * 8, __builtin_bit_cast(u32x4, bf));
  }
}

extern "C" __global__ __launch_bounds__(256) void k1_kernel(const u16* __restrict__ u, u16* __restrict__ xnP,
                                                 float2* __restrict__ stats, u16* __restrict__ lut,
                                                 const u16* __restrict__ w1, u16* __restrict__ w1P,
                                                 const u16* __restrict__ w2, u16* __restrict__ w2F, u16* __restrict__ w2H,
                                                 const u16* __restrict__ b1, float* __restrict__ b1f,
                                                 const u16* __restrict__ lar, const u16* __restrict__ aim,
                                                 const u16* __restrict__ cre, const u16* __restrict__ cim,
                                                 const u16* __restrict__ logdt, u16* __restrict__ Pg, u32* __restrict__ flags) {
  __shared__ __attribute__((aligned(16))) char k1s[57 * WPITCH];
  const int t = threadIdx.x, lane = t & 63;
  const int wave = __builtin_amdgcn_readfirstlane(t >> 6);
  STAMPB(0, xnP);
  STAMP(0);
  if (blockIdx.x < K1_LN) {
    u16 (*tile)[DM + 8] = (u16 (*)[DM + 8])k1s;
    const int r = wave * 2 + (lane >> 5), q = lane & 31;
    const int rg = blockIdx.x;
    const int row = rg * 8 + r;
    float v[20];
    const u32x2* src = (const u32x2*)(u + (size_t)row * DM);
#pragma unroll
    for (int i = 0; i < 5; ++i) {
      u32x2 w = src[q + 32 * i];
      v[i * 4 + 0] = lo_f(w[0]); v[i * 4 + 1] = hi_f(w[0]);
      v[i * 4 + 2] = lo_f(w[1]); v[i * 4 + 3] = hi_f(w[1]);
    }
    float s = 0.f;
#pragma unroll
    for (int i = 0; i < 20; ++i) s += v[i];
    STAMP(1);
#pragma unroll
    for (int o = 1; o < 32; o <<= 1) s += __shfl_xor(s, o);
    const float mean = s * (1.0f / DM);
    float ss = 0.f;
#pragma unroll
    for (int i = 0; i < 20; ++i) { float d = v[i] - mean; ss += d * d; }
#pragma unroll
    for (int o = 1; o < 32; o <<= 1) ss += __shfl_xor(ss, o);
    const float var = ss * (1.0f / DM);
    const float mb = bfr(mean);
    const float vb = bfr(var);
    const float den = bfr(sqrtf(bfr(vb + 1e-5f)));
    const float rden = 1.0f / den;
    if (q == 0) st8(stats + row, (u32x2){__float_as_uint(mb), __float_as_uint(rden)});
#pragma unroll
    for (int i = 0; i < 5; ++i) {
      u32 d0 = pk_bf(v[i * 4 + 0] - mb, v[i * 4 + 1] - mb);
      u32 d1 = pk_bf(v[i * 4 + 2] - mb, v[i * 4 + 3] - mb);
      u32x2 w = {pk_bf(lo_f(d0) * rden, hi_f(d0) * rden), pk_bf(lo_f(d1) * rden, hi_f(d1) * rden)};
      *(u32x2*)&tile[r][(q + 32 * i) * 4] = w;
    }
    __syncthreads();
    STAMP(2);
    const int mt = rg >> 1, half = rg & 1;
#pragma unroll
    for (int i = 0; i < 3; ++i) {
      int idx = t + 256 * i;
      if (idx < 640) {
        int kb = idx >> 5, g = (idx >> 3) & 3, fr = idx & 7;
        u32x2 a = *(const u32x2*)&tile[fr][kb * 32 + 4 * g];
        u32x2 b = *(const u32x2*)&tile[fr][kb * 32 + 16 + 4 * g];
        u32x4 w = {a[0], a[1], b[0], b[1]};
        st16(xnP + (((size_t)mt * 20 + kb) * 64 + g * 16 + half * 8 + fr) * 8, w);
      }
    }
    STAMP(3);
    STAMPW();
    return;
  }
  if (blockIdx.x < K1_LN + K1_KG) {
    const int d = blockIdx.x - K1_LN;
    const int n = t & 127;
    char* shm = k1s;
    const u16 r_dt = logdt[d];
    const u16 r_ar = lar[d * NS + n], r_ai = aim[d * NS + n];
    const u16 r_cr = cre[d * NS + n], r_ci = cim[d * NS + n];
    const u16 r_cr2 = cre[d * NS + (n ^ 64)], r_ci2 = cim[d * NS + (n ^ 64)];
    const float dt = bfr(expf(bf2f(r_dt)));
    const float ar = -bfr(expf(bf2f(r_ar))) * dt;
    const float ai = bf2f(r_ai) * dt;
    float cr = bf2f(r_cr) * dt, ci = bf2f(r_ci) * dt;
    float bs = (fabsf(cr) + fabsf(ci)) + (fabsf(bf2f(r_cr2) * dt) + fabsf(bf2f(r_ci2) * dt));
#pragma unroll
    for (int o = 1; o < 64; o <<= 1) bs += __shfl_xor(bs, o);
    const float bound = 2.0f * bs;
    int ex = 0;
    if (bound > 0.f && bound < 3.0e38f) { (void)frexpf(bound, &ex); }
    int es = 15 - ex;
    es = es > 100 ? 100 : (es < -100 ? -100 : es);
    const float sc2 = ldexpf(2.0f, es);
    const float kscale = ldexpf(1.0f, -es);
    cr *= sc2; ci *= sc2;
    const float arl2 = ar * 1.4426950408889634f;
    const float w = ai * 0.15915494309189535f;
    STAMP(1);
    char* wr = shm + n * 4;
    float eHr, eHi;
    cexp_p(arl2, w, 128.0f, eHr, eHi);
    if (wave < 2) {
      float e8r, e8i, e4r, e4i, gr, gi;
      cexp_p(arl2, w, 8.0f, e8r, e8i);
      cexp_p(arl2, w, 4.0f, e4r, e4i);
      cexp_p(arl2, w, 512.0f, gr, gi);
      cmul(gr, gi, cr, ci, gr, gi);
      float lr = 1.f, lim = 0.f;
      *(u32*)(wr) = pk_h(1.f, 0.f);
#pragma unroll
      for (int j = 1; j < 16; ++j) {
        cmul(lr, lim, e8r, e8i, lr, lim);
        *(u32*)(wr + j * WPITCH) = pk_h(lr, -lim);
        if (j < 5) {
          float hr, hi;
          *(u32*)(wr + (40 + (j - 1) * 2) * WPITCH) = pk_h(gr, gi);
          cmul(gr, gi, e4r, e4i, hr, hi);
          *(u32*)(wr + (41 + (j - 1) * 2) * WPITCH) = pk_h(hr, hi);
          cmul(gr, gi, eHr, eHi, gr, gi);
        } else if (j < 14) {
          *(u32*)(wr + (48 + j - 5) * WPITCH) = pk_h(gr, gi);
          if (j < 13) cmul(gr, gi, eHr, eHi, gr, gi);
        }
      }
    } else {
      float Rr[8], Ri[8];
      Rr[0] = 1.f; Ri[0] = 0.f;
      cexp_p(arl2, w, 1.0f, Rr[1], Ri[1]);
#pragma unroll
      for (int r = 2; r < 8; ++r) cmul(Rr[r - 1], Ri[r - 1], Rr[1], Ri[1], Rr[r], Ri[r]);
      float gr = cr, gi = ci;
#pragma unroll
      for (int ii = 0; ii < 2; ++ii) {
        *(u32*)(wr + (16 + ii * 8) * WPITCH) = pk_h(gr, gi);
#pragma unroll
        for (int r = 1; r < 8; ++r) {
          float hr, hi;
          cmul(gr, gi, Rr[r], Ri[r], hr, hi);
          *(u32*)(wr + (16 + ii * 8 + r) * WPITCH) = pk_h(hr, hi);
        }
        cmul(gr, gi, eHr, eHi, gr, gi);
      }
#pragma unroll
      for (int ii = 2; ii < 4; ++ii) {
        *(u32*)(wr + (32 + (ii - 2) * 4) * WPITCH) = pk_h(gr, gi);
#pragma unroll
        for (int r = 1; r < 4; ++r) {
          float hr, hi;
          cmul(gr, gi, Rr[2 * r], Ri[2 * r], hr, hi);
          *(u32*)(wr + (32 + (ii - 2) * 4 + r) * WPITCH) = pk_h(hr, hi);
        }
        if (ii < 3) cmul(gr, gi, eHr, eHi, gr, gi);
      }
    }
    __syncthreads();
    STAMP(2);
    const int b = lane & 15, g = lane >> 4;
    u16* P = Pg + (size_t)d * PPITCH;
    if (wave < 3) {
      const int rowa = 16 + 16 * wave + b;
      const char* ra = shm + (rowa > 56 ? 56 : rowa) * WPITCH + g * 16;
      const char* rb = shm + b * WPITCH + g * 16;
      f4 ka = (f4){0.f, 0.f, 0.f, 0.f};
#pragma unroll
      for (int k8 = 0; k8 < 8; ++k8) {
        h8 a = *(const h8*)(ra + k8 * 64);
        h8 bb = *(const h8*)(rb + k8 * 64);
        ka = __builtin_amdgcn_mfma_f32_16x16x32_f16(a, bb, ka, 0, 0, 0);
      }
      const u32 k01 = pk_h(ka[0], ka[1]), k23 = pk_h(ka[2], ka[3]);
      if (wave == 0) {
        st8(&P[(g >> 1) * 128 + (g & 1) * 4 + 8 * b], (u32x2){k01, k23});
      } else if (wave == 1) {
        const int i1 = g < 2 ? 256 + 64 * g + 4 * b : 384 + 64 * (g - 2) + 2 * b;
        st4(&P[i1], k01);
        st4(&P[i1 + (g < 2 ? 2 : 32)], k23);
      } else {
        const int i2 = 512 + 64 * g + b;
        if (g < 3) st2(&P[i2], k01);
        if (g < 2) {
          st2(&P[i2 + 16], k01 >> 16);
          st2(&P[i2 + 32], k23); st2(&P[i2 + 48], k23 >> 16);
        }
      }
    } else if (lane == 0) {
      st4(&P[704], __float_as_uint(kscale));
    }
    STAMP(3);
    STAMPW();
    return;
  }
  if (blockIdx.x < K1_LN + K1_KG + K1_RP) {
    const int u0 = (blockIdx.x - (K1_LN + K1_KG)) * 2;
    repack_load(w1, FF, u0 / 27, u0 % 27, k1s, t);
    repack_load(w1, FF, (u0 + 1) / 27, (u0 + 1) % 27, k1s + 10240, t);
    __syncthreads();
    repack_store(w1P, DM / 32, u0 / 27, u0 % 27, k1s, t);
    repack_store(w1P, DM / 32, (u0 + 1) / 27, (u0 + 1) % 27, k1s + 10240, t);
    STAMP(3);
    STAMPW();
    return;
  }
  if (blockIdx.x < K1_LN + K1_KG + K1_RP + K1_RQ) {
    const int un = blockIdx.x - (K1_LN + K1_KG + K1_RP);
    const int bn = un / 10, nu = un % 10;
    const int f0 = 16 * (27 * (bn >> 2) + 7 * (bn & 3));
    const int rows = (bn & 3) == 3 ? 96 : 112;
    u32x4 v[4];
#pragma unroll
    for (int i = 0; i < 4; ++i) {
      const int r = (t >> 3) + 32 * i;
      v[i] = *(const u32x4*)(w2 + (size_t)(f0 + (r < rows ? r : rows - 1)) * DM + nu * 64 + (t & 7) * 8);
    }
#pragma unroll
    for (int i = 0; i < 4; ++i) *(u32x4*)(k1s + ((t >> 3) + 32 * i) * 160 + (t & 7) * 16) = v[i];
    __syncthreads();
    const int g = lane >> 4, tq = (lane & 15) >> 2, tp = lane & 3;
    const int nt = nu * 4 + wave;
    const char* p0 = k1s + (4 * g + tq) * 160 + wave * 32 + tp * 8;
#pragma unroll
    for (int p = 0; p < 3; ++p) {
      s4 b0 = TRREAD(p0 + (32 * p) * 160);
      s4 b1v = TRREAD(p0 + (32 * p + 16) * 160);
      s8 bf = {b0[0], b0[1], b0[2], b0[3], b1v[0], b1v[1], b1v[2], b1v[3]};
      st16(w2F + (((size_t)nt * 48 + 3 * bn + p) * 64 + lane) * 8, __builtin_bit_cast(u32x4, bf));
    }
    if (rows == 112) {
      s4 b0 = TRREAD(p0 + 96 * 160);
      st8(w2H + (((size_t)nt * 12 + 3 * (bn >> 2) + (bn & 3)) * 64 + lane) * 4, __builtin_bit_cast(u32x2, b0));
    }
    STAMP(3);
    STAMPW();
    return;
  }
  {
    const int idx = (blockIdx.x - (K1_LN + K1_KG + K1_RP + K1_RQ)) * 256 + t;
    const int sgn = idx >= 4096, i = idx - sgn * 4096;
    u32 td = 0;
    if (idx < 4096) CODE_TOUCH("k2_kernel", "k4_end", (idx >> 11) * 256 + t, td);
    if (idx < FF) st4(b1f + idx, (u32)b1[idx] << 16);
    if (idx >= 2048 && idx < 4096) st4(flags + (idx - 2048), 0u);
    u32 r = 0;
    if (i < LUT_HALF) r = f2bf(mish_bf(bf2f((u16)((i + (LUT_E0 << 7)) | (sgn << 15)))));
    st2(lut + idx, r);
    asm volatile("s_waitcnt vmcnt(0)" :: "v"(td) : "memory");
  }
}

#define K2_G1 256
#define K2_SSM 240
#define KCPITCH 2080
#define SB0() __builtin_amdgcn_sched_barrier(0)
template <int V> struct IC { static constexpr int value = V; };
template <int I, int N, class F> __device__ __forceinline__ void static_for_i(F&& f) {
  if constexpr (I < N) { f(IC<I>{}); static_for_i<I + 1, N>(f); }
}
template <int N, class F> __device__ __forceinline__ void static_for(F&& f) { static_for_i<0, N>(f); }
__device__ __forceinline__ void glds16s(u32 voff, const void* sbase, const void* lp) {
  unsigned keep;
  unsigned lds_dst = (unsigned)(size_t)(const __attribute__((address_space(3))) char*)lp;
  asm volatile("s_mov_b32 %0, m0\n\ts_mov_b32 m0, %3\n\ts_nop 0\n\tglobal_load_lds_dwordx4 %1, %2\n\ts_mov_b32 m0, %0"
               : "=&s"(keep) : "v"(voff), "s"(sbase), "s"(lds_dst) : "memory");
}
#define MFMA_BF(acc, a, b) \
  asm volatile("v_mfma_f32_16x16x32_bf16 %0, %1, %2, %0" : "+v"(acc) : "v"(a), "v"(b))
#define MFMA_BF0(acc, a, b) \
  asm volatile("v_mfma_f32_16x16x32_bf16 %0, %1, %2, 0" : "=&v"(acc) : "v"(a), "v"(b))
#define GLOAD4(dst, voff, sbase) \
  asm volatile("global_load_dwordx4 %0, %1, %2" : "=v"(dst) : "v"(voff), "s"(sbase) : "memory")
#define DSR64(dst, addr, off) asm volatile("ds_read_b64 %0, %1 offset:%2" : "=v"(dst) : "v"(addr), "n"(off) : "memory")
#define DSR128(dst, addr, off) asm volatile("ds_read_b128 %0, %1 offset:%2" : "=v"(dst) : "v"(addr), "n"(off) : "memory")

#define NT1 4
#define G1_D 4
#define LUT_NEG 65536
#define BIAS_OFF 78848
constexpr int g1_chunk(int slot, int w) { return (slot == 2 && w == 3) ? 73728 : 8192 + (slot * 4 + w) * 5120; }
constexpr int g1_piece_lds(int p) {
  return p < 20 * NT1 ? g1_chunk((p / 20) % 3, p % 4) + ((p % 20) / 4) * 1024
                      : g1_chunk((p / 20) % 3, (p % 20) / 5) + (p % 5) * 1024;
}
constexpr int g1_tile(int p) { return p < 20 * NT1 ? p % NT1 : p / 20; }
constexpr int g1_kt(int p) { return p < 20 * NT1 ? p / NT1 : p % 20; }
constexpr int g1_fin(int T) { return T < NT1 ? 19 * NT1 + T : 20 * T + 19; }
constexpr int g1_vmB(int p) { return (p & 1) && p >= 17 && p < 86 && (p - 17) % 20 < 10 ? ((p - 17) / 20) * 5 + ((p - 17) % 20) / 2 : -1; }
constexpr int g1_vmL(int p) { return (p == 47 || p == 49 || p == 51 || p == 53) ? (p - 47) / 2 : -1; }
struct G1S { int vA[140], vS[140], lB[140], ws[24], cs[24], sp[3]; };
constexpr G1S g1_sched() {
  G1S S{};
  for (int w = 0; w < 24; ++w) {
    const int e = g1_fin(w >> 2) + 2;
    S.ws[w] = (w && S.ws[w - 1] + 2 > e) ? S.ws[w - 1] + 2 : e;
  }
  for (int w = 0; w < 24; ++w) S.cs[w] = S.ws[w] + G1_D + 2;
  for (int r = 0; r < 3; ++r) S.sp[r] = S.cs[8 * r + 7] + 1;
  int idxA[20] = {}, idxB[7] = {}, idxL = 0, ldB[140] = {};
  int v = 1;
  for (int h = 0; h < 3; ++h) {
    v += 5; idxB[h] = v;
    if (h < 2) for (int k = 0; k < 5; ++k) { v += 2; idxA[5 * h + k] = v; }
  }
  int l = NT1;
  for (int q = 0; q < G1_D; ++q) ldB[q] = ++l;
  for (int q = 0; q < 140; ++q) {
    const int gq = q / 20, lp = q % 20;
    S.vA[q] = -1; S.vS[q] = -1;
    if (lp == 20 - G1_D && gq < 6) {
      int need = idxB[gq + 1];
      if (gq + 1 == NT1 && idxL > need) need = idxL;
      S.vS[q] = v - need;
    }
    for (int r = 0; r < 3; ++r) if (q == S.sp[r]) v += 2;
    if (q < 20 * NT1 && q % NT1 == 0) S.vA[q] = v - idxA[q / NT1];
    if (q < 40 && (q & 1) == 0) { v += 1; idxA[10 + q / 4] = v; }
    if (g1_vmB(q) >= 0) { v += 1; idxB[3 + g1_vmB(q) / 5] = v; }
    if (g1_vmL(q) >= 0) { v += 1; idxL = v; }
    if (q + G1_D < 140) ldB[q + G1_D] = ++l;
    if (lp == 12 && gq + 1 >= NT1 && gq < 6) ++l;
    S.lB[q] = l - ldB[q];
    for (int w = 0; w < 24; ++w) if (S.ws[w] + 1 == q) l += 2;
  }
  return S;
}
constexpr G1S G1 = g1_sched();
constexpr int g1_word(int p) { for (int w = 0; w < 24; ++w) if (p >= G1.ws[w] && p < G1.ws[w] + 2) return w; return -1; }
constexpr int g1_comb(int p) { for (int w = 0; w < 24; ++w) if (p == G1.cs[w]) return w; return -1; }
static_assert(G1.sp[2] < 140 && G1.ws[0] >= 20 * NT1 - G1_D, "schedule");
#define WAIT_L(n) __builtin_amdgcn_s_waitcnt(0xC07F | (((n) > 15 ? 15 : (n)) << 8))
#define MF_ "v_mfma_f32_16x16x32_bf16 "
#define SDWA_(h) " dst_sel:DWORD dst_unused:UNUSED_PAD src0_sel:DWORD src1_sel:WORD_" #h "\n\t"
template <bool F> __device__ __forceinline__ void step_p(f4& c0, f4& c1, const u32x4& b, const u32x4& x0, const u32x4& x1, const f4& bs) {
  if constexpr (F) asm volatile(MF_ "%0, %2, %3, %5\n\t" MF_ "%1, %2, %4, %5" : "=&v"(c0), "=&v"(c1) : "v"(b), "v"(x0), "v"(x1), "v"(bs));
  else asm volatile(MF_ "%0, %2, %3, %0\n\t" MF_ "%1, %2, %4, %1" : "+v"(c0), "+v"(c1) : "v"(b), "v"(x0), "v"(x1));
}
template <bool F> __device__ __forceinline__ void step_x(f4& c0, f4& c1, const u32x4& b, const u32x4& x0, const u32x4& x1, const f4& bs,
                                                         u32& w, u32& z, u32& mx, float e0, float e1, u32 a0, u32 a1, u32 kb) {
#define X_MID "v_cvt_pk_bf16_f32 %2, %8, %9\n\tv_pk_add_u16 %3, %2, %12\n\t"
#define X_END "\n\tv_max3_u16 %4, %4, %10, %11"
  if constexpr (F) asm volatile(MF_ "%0, %5, %6, %13\n\t" X_MID MF_ "%1, %5, %7, %13" X_END
                                : "=&v"(c0), "=&v"(c1), "=&v"(w), "=&v"(z), "+v"(mx)
                                : "v"(b), "v"(x0), "v"(x1), "v"(e0), "v"(e1), "v"(a0), "v"(a1), "s"(kb), "v"(bs));
  else asm volatile(MF_ "%0, %5, %6, %0\n\t" X_MID MF_ "%1, %5, %7, %1" X_END
                    : "+v"(c0), "+v"(c1), "=&v"(w), "=&v"(z), "+v"(mx)
                    : "v"(b), "v"(x0), "v"(x1), "v"(e0), "v"(e1), "v"(a0), "v"(a1), "s"(kb));
}
template <bool F> __device__ __forceinline__ void step_y(f4& c0, f4& c1, const u32x4& b, const u32x4& x0, const u32x4& x1, const f4& bs,
                                                         u32& a0, u32& a1, u32& r0, u32& r1, u32 z) {
#define Y_MID "v_lshlrev_b32_sdwa %2, 1, %9" SDWA_(0) "v_lshlrev_b32_sdwa %3, 1, %9" SDWA_(1)
#define Y_END "\n\tds_read_u16 %4, %2\n\tds_read_u16 %5, %3"
  if constexpr (F) asm volatile(MF_ "%0, %6, %7, %10\n\t" Y_MID MF_ "%1, %6, %8, %10" Y_END
                                : "=&v"(c0), "=&v"(c1), "=&v"(a0), "=&v"(a1), "=&v"(r0), "=&v"(r1)
                                : "v"(b), "v"(x0), "v"(x1), "v"(z), "v"(bs) : "memory");
  else asm volatile(MF_ "%0, %6, %7, %0\n\t" Y_MID MF_ "%1, %6, %8, %1" Y_END
                    : "+v"(c0), "+v"(c1), "=&v"(a0), "=&v"(a1), "=&v"(r0), "=&v"(r1)
                    : "v"(b), "v"(x0), "v"(x1), "v"(z) : "memory");
}
__device__ __forceinline__ void comb(u32& r, u32 hi) { asm volatile("v_lshl_or_b32 %0, %1, 16, %0" : "+v"(r) : "v"(hi)); }
__device__ __forceinline__ void max3u(u32& mx, u32 a0, u32 a1) { asm volatile("v_max3_u16 %0, %0, %1, %2" : "+v"(mx) : "v"(a0), "v"(a1)); }
__device__ __forceinline__ void epi_word(u32& w, u32& r0, u32& r1, u32& mx, float e0, float e1, u32 kb) {
  u32 z;
  asm volatile("v_cvt_pk_bf16_f32 %0, %5, %6\n\tv_pk_add_u16 %4, %0, %7\n\t"
               "v_lshlrev_b32_sdwa %1, 1, %4" SDWA_(0) "v_lshlrev_b32_sdwa %2, 1, %4" SDWA_(1)
               "v_max3_u16 %3, %3, %1, %2\n\tds_read_u16 %1, %1\n\tds_read_u16 %2, %2"
               : "=&v"(w), "=&v"(r0), "=&v"(r1), "+v"(mx), "=&v"(z) : "v"(e0), "v"(e1), "s"(kb) : "memory");
}
__device__ __forceinline__ void mfma_c(f4& acc, const u32x4& a, const u32x4& b, const f4& c) {
  asm volatile("v_mfma_f32_16x16x32_bf16 %0, %1, %2, %3" : "=&v"(acc) : "v"(a), "v"(b), "v"(c));
}
__device__ __forceinline__ void mfma_a(f4& acc, const u32x4& a, const u32x4& b) { MFMA_BF(acc, a, b); }
template <int OFF, class V> __device__ __forceinline__ void dsr128(V& d, u32 addr) { DSR128(d, addr, OFF); }
__device__ __forceinline__ void pin2(u32x4& a, u32x4& b) { asm volatile("" : "+v"(a), "+v"(b)); }
template <int N> __device__ __forceinline__ void wait_res(u32 (&a)[4], u32 (&b)[4]) {
  asm volatile("s_waitcnt lgkmcnt(%8)"
               : "+v"(a[0]), "+v"(a[1]), "+v"(a[2]), "+v"(a[3]), "+v"(b[0]), "+v"(b[1]), "+v"(b[2]), "+v"(b[3])
               : "n"(N > 15 ? 15 : N) : "memory");
}
template <int OFF> __device__ __forceinline__ void glds_o(u32 voff, const void* sbase, u32 m) {
  asm volatile("s_mov_b32 m0, %2\n\ts_nop 0\n\tglobal_load_lds_dwordx4 %0, %1 offset:%3"
               :: "v"(voff), "s"(sbase), "s"(m), "n"(OFF) : "memory", "m0");
}
__device__ __forceinline__ void gload4(u32x4& d, u32 voff, const void* sbase) { GLOAD4(d, voff, sbase); }
__device__ __forceinline__ void st16s(u32 voff, const void* sbase, u32x4 v) {
  asm volatile("global_store_dwordx4 %0, %1, %2" STF :: "v"(voff), "v"(v), "s"(sbase) : "memory");
}

template <int KID>
__device__ __forceinline__ void ssm_block(char* shm, const int grp, const int e0, const int nch, const u16* __restrict__ xnP,
                                          const u16* __restrict__ Pg, u16* __restrict__ yT, u32* flag) {
    const int t = threadIdx.x, lane = t & 63;
    const int wave = __builtin_amdgcn_readfirstlane(t >> 6);
    STAMPB(KID, (char*)yT - (13u << 20));
    STAMP(0);
    const int eo = (e0 & ~1) > 4 ? 4 : (e0 & ~1);
    const int dbase = (grp >> 2) * 32 + (grp & 3) * 4;
    u16* Kc = (u16*)shm;
    float* red = (float*)(shm + 16896);
#pragma unroll
    for (int c = 0; c < 3; ++c) {
      if (c < nch) {
        const int e = e0 + c;
        const int d = dbase + (e >> 2) * 16 + (e & 3);
        char* buf = shm + 41472 + c * 6144;
        if (wave == 0) glds16s(lane * 16, (const char*)(Pg + (size_t)d * PPITCH), buf);
        if (wave == 1 && lane < 32) glds16s(lane * 16, (const char*)(Pg + (size_t)d * PPITCH) + 1024, buf + 1024);
      }
    }
    {
      u32x2 xv[8];
      const char* xp = (const char*)xnP + ((((size_t)(t >> 3) * 20 + (grp >> 2)) * 64 + (grp & 3) * 16 + ((2 * t) & 15)) * 16 + eo * 2);
#pragma unroll
      for (int i = 0; i < 4; ++i) {
        xv[2 * i] = *(const u32x2*)(xp + (size_t)i * 655360);
        xv[2 * i + 1] = *(const u32x2*)(xp + (size_t)i * 655360 + 16);
      }
#pragma unroll
      for (int c = 0; c < 3; ++c) {
        if (c < nch) {
          const u32 pos2 = 2 * (e0 + c - eo);
          const u32 sel = 0x00000c0cu | (pos2 << 16) | ((pos2 + 1) << 24);
          u16* xs = (u16*)(shm + 41472 + c * 6144 + 2048);
#pragma unroll
          for (int i = 0; i < 4; ++i) {
            const float x0 = __uint_as_float(__builtin_amdgcn_perm(xv[2 * i][1], xv[2 * i][0], sel));
            const float x1 = __uint_as_float(__builtin_amdgcn_perm(xv[2 * i + 1][1], xv[2 * i + 1][0], sel));
            *(u32*)&xs[2 * t + 512 * i] = pk_h(x0, x1);
          }
        }
      }
    }
    if (t < 96) *(u32x4*)(shm + 41472 + (t >> 5) * 6144 + 1536 + (t & 31) * 16) = (u32x4){0u, 0u, 0u, 0u};
    if (t < 8) *(u32x4*)&Kc[(t >> 1) * KCPITCH + (256 + (t & 1)) * 8] = (u32x4){0u, 0u, 0u, 0u};
    WAIT_V(0);
    RAW_BARRIER();
    STAMP(3);
    const int b = lane & 15, g = lane >> 4;
    const int m = 255 - t;
    const u32 lds0 = (u32)(size_t)(const __attribute__((address_space(3))) char*)shm;
    const int abase = ((3 - b) & 3) * KCPITCH + 2028 - 4 * (b >> 2) + 8 * g;
    const int bbase = 256 - 16 + 16 * b + 8 * g;
#pragma unroll 1
    for (int c = 0; c < nch; ++c) {
      const int e = e0 + c;
      const int d = dbase + (e >> 2) * 16 + (e & 3);
      const u16* P = (const u16*)(shm + 41472 + c * 6144);
      const float kscale = *(const float*)&P[704];
      {
        u32 wv[6];
        if (wave < 2) {
          const int i = 512 + m - 128;
          const u32 pc = P[i - 1], pa = P[i], pb = P[i + 1];
          const u32 aa = pa | (pa << 16);
          wv[0] = pb | (pb << 16);
          wv[1] = pb | (((m & 1) ? pb : pa) << 16);
          wv[2] = aa; wv[3] = aa; wv[4] = aa;
          wv[5] = m == 128 ? pc : pa;
        } else if (wave == 2) {
          const int i = 384 + 2 * (m - 64);
          const u32 q0 = *(const u32*)&P[i - 2], q1 = *(const u32*)&P[i], q2 = *(const u32*)&P[i + 2];
          wv[0] = __builtin_amdgcn_perm(q2, q2, 0x01000100u);
          wv[1] = __builtin_amdgcn_perm(q1, q1, 0x03020302u);
          wv[2] = __builtin_amdgcn_perm(q1, q1, 0x01000302u);
          wv[3] = __builtin_amdgcn_perm(q1, q1, 0x01000100u);
          wv[4] = m == 64 ? __builtin_amdgcn_perm(q0, q1, 0x07060100u) : wv[3];
          wv[5] = m == 64 ? q0 : (q0 >> 16);
        } else {
#pragma unroll
          for (int hh = 0; hh < 2; ++hh) {
            const int mm = m - hh;
            const int sg = mm >= 32 ? 1 : 0;
#pragma unroll
            for (int j = 0; j < (hh ? 2 : 4); ++j) {
              u32 pk = 0;
#pragma unroll
              for (int h = 0; h < 2; ++h) {
                if (hh == 0 || 2 * j + h < 3) {
                  int q = 8 * mm + 7 - (2 * j + h);
                  q = q < 0 ? 0 : q;
                  int idx = sg ? 128 + ((q + ((q >> 1) & 1)) >> 1) : q;
                  pk |= ((u32)P[idx]) << (16 * h);
                }
              }
              wv[hh * 4 + j] = (hh && mm < 0) ? 0u : pk;
            }
          }
        }
        *(u32x4*)&Kc[0 * KCPITCH + t * 8] = (u32x4){wv[0], wv[1], wv[2], wv[3]};
        *(u32x4*)&Kc[1 * KCPITCH + t * 8] = (u32x4){__builtin_amdgcn_alignbit(wv[1], wv[0], 16), __builtin_amdgcn_alignbit(wv[2], wv[1], 16),
                                                    __builtin_amdgcn_alignbit(wv[3], wv[2], 16), __builtin_amdgcn_alignbit(wv[4], wv[3], 16)};
        *(u32x4*)&Kc[2 * KCPITCH + t * 8] = (u32x4){wv[1], wv[2], wv[3], wv[4]};
        *(u32x4*)&Kc[3 * KCPITCH + t * 8] = (u32x4){__builtin_amdgcn_alignbit(wv[2], wv[1], 16), __builtin_amdgcn_alignbit(wv[3], wv[2], 16),
                                                    __builtin_amdgcn_alignbit(wv[4], wv[3], 16), __builtin_amdgcn_alignbit(wv[5], wv[4], 16)};
      }
      __syncthreads();
      if (c == 0) { STAMP(4); }
      if (c == 2) { STAMP(9); }
      f4 acc[8];
#pragma unroll
      for (int i = 0; i < 8; ++i) acc[i] = (f4){0.f, 0.f, 0.f, 0.f};
      {
        u32x2 a0[2][8], a1[2][8];
        u32x4 bq[2][8];
        const u32 aaddr = lds0 + (u32)(abase - 64 * wave - 32 - 256 * 7) * 2;
        const u32 baddr = lds0 + 41472 + c * 6144 + 1536 + (u32)(bbase - 64 * wave - 32) * 2;
#pragma unroll
        for (int rr = 0; rr < 2; ++rr) {
#pragma unroll
          for (int u = 0; u < 8; ++u) {
            DSR64(a0[rr][u], aaddr, (7 - u) * 512 + (1 - rr) * 64);
            DSR64(a1[rr][u], aaddr, (7 - u) * 512 + (1 - rr) * 64 + 8);
            DSR128(bq[rr][u], baddr, u * 512 + (1 - rr) * 64);
            if (rr == 1 && u == 3) {
              asm volatile("s_waitcnt lgkmcnt(12)"
                           : "+v"(a0[0][0]), "+v"(a1[0][0]), "+v"(a0[0][1]), "+v"(a1[0][1]), "+v"(a0[0][2]), "+v"(a1[0][2]),
                             "+v"(a0[0][3]), "+v"(a1[0][3]), "+v"(a0[0][4]), "+v"(a1[0][4]), "+v"(a0[0][5]), "+v"(a1[0][5]),
                             "+v"(a0[0][6]), "+v"(a1[0][6]), "+v"(a0[0][7]), "+v"(a1[0][7]),
                             "+v"(bq[0][0]), "+v"(bq[0][1]), "+v"(bq[0][2]), "+v"(bq[0][3]),
                             "+v"(bq[0][4]), "+v"(bq[0][5]), "+v"(bq[0][6]), "+v"(bq[0][7])
                           :: "memory");
            }
          }
        }
        SB0();
#pragma unroll
        for (int rr = 0; rr < 2; ++rr) {
          if (rr == 1) {
            asm volatile("s_waitcnt lgkmcnt(0)"
                         : "+v"(a0[1][0]), "+v"(a1[1][0]), "+v"(a0[1][1]), "+v"(a1[1][1]), "+v"(a0[1][2]), "+v"(a1[1][2]),
                           "+v"(a0[1][3]), "+v"(a1[1][3]), "+v"(a0[1][4]), "+v"(a1[1][4]), "+v"(a0[1][5]), "+v"(a1[1][5]),
                           "+v"(a0[1][6]), "+v"(a1[1][6]), "+v"(a0[1][7]), "+v"(a1[1][7]),
                           "+v"(bq[1][0]), "+v"(bq[1][1]), "+v"(bq[1][2]), "+v"(bq[1][3]),
                           "+v"(bq[1][4]), "+v"(bq[1][5]), "+v"(bq[1][6]), "+v"(bq[1][7])
                         :: "memory");
          }
#pragma unroll
          for (int u = 0; u < 8; ++u) {
            const h8 A = __builtin_bit_cast(h8, (u32x4){a0[rr][u][0], a0[rr][u][1], a1[rr][u][0], a1[rr][u][1]});
#pragma unroll
            for (int v = 0; v + u < 8; ++v)
              acc[u + v] = __builtin_amdgcn_mfma_f32_16x16x32_f16(A, __builtin_bit_cast(h8, bq[rr][v]), acc[u + v], 0, 0, 0);
          }
          SB0();
        }
      }
      if (c == 0) { STAMP(5); }
      if (c == 2) { STAMP(10); }
      auto red_phase = [&](auto WC) {
        constexpr int W = decltype(WC)::value;
#pragma unroll
        for (int o = 1; o < 4; ++o) {
          const int ow = (W + o) & 3;
#pragma unroll
          for (int i = 0; i < 2; ++i) *(f4*)&red[(((ow * 3 + (o - 1)) * 2 + i) * 64 + lane) * 4] = acc[ow * 2 + i];
        }
        __syncthreads();
        u32x2 o[2];
#pragma unroll
        for (int i = 0; i < 2; ++i) {
          f4 s1 = *(const f4*)&red[(((W * 3 + 0) * 2 + i) * 64 + lane) * 4];
          f4 s2 = *(const f4*)&red[(((W * 3 + 1) * 2 + i) * 64 + lane) * 4];
          f4 s3 = *(const f4*)&red[(((W * 3 + 2) * 2 + i) * 64 + lane) * 4];
          f4 sum = ((acc[W * 2 + i] + s1) + (s2 + s3)) * kscale;
          o[i] = (u32x2){pk_bf(sum[0], sum[1]), pk_bf(sum[2], sum[3])};
        }
#pragma unroll
        for (int i = 0; i < 2; ++i) st8(yT + (size_t)d * SEQ + 256 * (W * 2 + i) + 16 * b + 4 * g, o[i]);
      };
      if (wave == 0) red_phase(IC<0>{});
      else if (wave == 1) red_phase(IC<1>{});
      else if (wave == 2) red_phase(IC<2>{});
      else red_phase(IC<3>{});
      if (c == 0) { STAMP(6); }
      if (c == 1) { STAMP(7); }
      if (c == 2) { STAMP(11); }
    }
    if (flag) {
      WAIT_V(0);
      if (lane == 0) st4(flag + wave * 256, 1u);
    }
    STAMP(8);
    STAMPW();
}

#define K3R 3
#define K3HB 1
#define K3NB 3
#define K3SLEEP1 8
#define K3YS 14
__device__ __forceinline__ void glds16c(const void* gsrc, const void* lp) {
  unsigned keep;
  unsigned lds_dst = __builtin_amdgcn_readfirstlane((unsigned)(size_t)(const __attribute__((address_space(3))) char*)lp);
  asm volatile("s_mov_b32 %0, m0\n\ts_mov_b32 m0, %2\n\ts_nop 0\n\tglobal_load_lds_dwordx4 %1, off sc1\n\ts_mov_b32 m0, %0"
               : "=&s"(keep) : "v"(gsrc), "s"(lds_dst) : "memory");
}
#define OUTF ""
__device__ __forceinline__ void so16(void* p, u32x4 v) { asm volatile("global_store_dwordx4 %0, %1, off" OUTF :: "v"(p), "v"(v) : "memory"); }
__device__ __forceinline__ void so8(void* p, u32x2 v) { asm volatile("global_store_dwordx2 %0, %1, off" OUTF :: "v"(p), "v"(v) : "memory"); }
__device__ __forceinline__ void g2_block(char* shm, const int bid, char* ws, const u16* __restrict__ xin,
                                         const u16* __restrict__ dskip, u16* __restrict__ out) {
  constexpr int R = K3R;
  constexpr int NB = K3NB;
  constexpr int NF = 12, NST = 15;
  const u16* w2F = (const u16*)(ws + WS_W2F);
  const u16* w2H = (const u16*)(ws + WS_W2H);
  const float2* stats = (const float2*)(ws + WS_STATS);
  const u16* yT = (const u16*)(ws + WS_YT);
  const u32* flags = (const u32*)(ws + WS_FLAGS);
  const int t = threadIdx.x, lane = t & 63;
  const int wave = __builtin_amdgcn_readfirstlane(t >> 6);
  const int xcd = bid & 7, idx = bid >> 3;
  const int mi = idx & 7, ni = idx >> 3;
  const int bm = (xcd >> 1) * 8 + mi, bn = (xcd & 1) * 4 + ni;
  const int mt0 = bm * 4, m0 = bm * 64, n0 = bn * 80, nt0 = bn * 5;
  STAMPB(3, ws);
  STAMP(0);
  const char* abase = ws + WS_ACTF + ((size_t)mt0 * 48 + wave) * 1024;
  const char* bbase = (const char*)w2F + ((size_t)nt0 * 48 + wave) * 1024;
  const char* ahb = ws + WS_ACTH + ((size_t)(mt0 >> 1) * 12 + wave) * 1024;
  const char* bhb = (const char*)w2H + ((size_t)nt0 * 12 + wave) * 512;
  u32 va[4], vb[5], vh[2];
  const __amdgpu_buffer_rsrc_t ra = __builtin_amdgcn_make_buffer_rsrc((void*)abase, 0, 4 * 48 * 1024, 0x00020000);
  const __amdgpu_buffer_rsrc_t rh = __builtin_amdgcn_make_buffer_rsrc((void*)ahb, 0, 2 * 12 * 1024, 0x00020000);
#define LDA(r, p, v, so) __builtin_bit_cast(u32x4, __builtin_amdgcn_raw_buffer_load_b128(r, v, so, 16))
#pragma unroll
  for (int j = 0; j < 5; ++j) vb[j] = lane * 16 + j * (48 * 1024);
  u32x4 fa[R][4], fb[R][5];
  u32x4 ha[3][2];
  u32x2 hb[3][5];
  auto issue = [&](int s, int what = 3) {
    if (s < NF) {
      const char* ap = abase + s * 4096;
      const char* bp = bbase + s * 4096;
      if (what & 1) {
#pragma unroll
        for (int i = 0; i < 4; ++i) fa[s % R][i] = LDA(ra, ap, va[i], s * 4096);
      }
      if (what & 2) {
        if (s >= R && s < R + NB) {
#pragma unroll
          for (int j = 0; j < 5; ++j) fb[s % R][j] = *(const u32x4*)(shm + 20480 + (((s - R) * 4 + wave) * 5 + j) * 1024 + lane * 16);
        } else {
#pragma unroll
          for (int j = 0; j < 5; ++j) fb[s % R][j] = *(const u32x4*)(bp + vb[j]);
        }
      }
    } else {
      const int h = s - NF;
#pragma unroll
      for (int i = 0; i < 2; ++i) ha[h][i] = LDA(rh, ahb + h * 4096, vh[i], h * 4096);
      if (h >= K3HB) {
#pragma unroll
        for (int j = 0; j < 5; ++j) hb[h][j] = *(const u32x2*)(bhb + h * 2048 + (lane * 8 + j * (12 * 512)));
      }
    }
  };
  auto aux = [&]() {
#pragma unroll
    for (int i = 0; i < 3; ++i) {
      int p = wave + 4 * i;
      if (p > 10) p = 10;
      const int c = p * 64 + lane;
      const u16* src;
      if (p < 10) src = xin + (size_t)(m0 + c / 10) * DM + n0 + (c % 10) * 8;
      else src = (lane < 32) ? (const u16*)(stats + m0) + lane * 8 : dskip + n0 + (lane < 42 ? lane - 32 : 9) * 8;
      GLDS(src, shm + p * 1024);
    }
  };
  auto yload = [&]() {
    const u32* fp = flags + (bm >> 3) * 256 + ((n0 >> 5) * 4) * 3 + (lane < 12 * 3 ? lane : 0);
#define K3SLEEP 16
    if (wave == 0)
    for (int it = 0; it < (1 << 20); ++it) {
      u32 f;
      asm volatile("global_load_dword %0, %1, off sc1\n\ts_waitcnt vmcnt(0)" : "=v"(f) : "v"(fp) : "memory");
      if (__builtin_amdgcn_ballot_w64(f != 1u) == 0) break;
      __builtin_amdgcn_s_sleep(K3SLEEP);
    }
    __builtin_amdgcn_s_barrier();
#pragma unroll
    for (int i = 0; i < 3; ++i) {
      int p = wave + 4 * i;
      if (p > 9) p = 9;
      const int c = p * 64 + lane;
      glds16c(yT + (size_t)(n0 + (c >> 3)) * SEQ + m0 + (c & 7) * 8, shm + (10 + p) * 1024);
    }
  };
  SB0();
  const u32* fp1 = flags + 1024 + bm * 32 + (lane & 31);
  u32 f1 = 0;
  SB0();
#pragma unroll
  for (int s = 0; s < R; ++s) { issue(s, 2); SB0(); }
#pragma unroll
  for (int h = 0; h < K3HB; ++h) {
#pragma unroll
    for (int j = 0; j < 5; ++j) hb[h][j] = *(const u32x2*)(bhb + h * 2048 + (lane * 8 + j * (12 * 512)));
    SB0();
  }
  aux();
  SB0();
  {
    const u32 lb0 = (u32)(size_t)(const __attribute__((address_space(3))) char*)shm + 20480 + wave * 5120;
#pragma unroll
    for (int s = 0; s < NB; ++s)
#pragma unroll
      for (int j = 0; j < 5; ++j) glds_o<0>(vb[j], bbase + (R + s) * 4096, lb0 + s * 20480 + j * 1024);
  }
  SB0();
  if (wave == 0) {
    asm volatile("global_load_dword %0, %1, off sc1\n\ts_waitcnt vmcnt(0)" : "=v"(f1) : "v"(fp1) : "memory");
    for (int it = 0; it < (1 << 20); ++it) {
      if (__builtin_amdgcn_ballot_w64(f1 != 1u) == 0) break;
      __builtin_amdgcn_s_sleep(K3SLEEP1);
      asm volatile("global_load_dword %0, %1, off sc1\n\ts_waitcnt vmcnt(0)" : "=v"(f1) : "v"(fp1) : "memory");
    }
  }
  __builtin_amdgcn_s_barrier();
  {
    u32 l16 = lane * 16;
    asm volatile("" : "+v"(l16) :: "memory");
#pragma unroll
    for (int i = 0; i < 4; ++i) va[i] = l16 + ((wave + i) & 3) * (48 * 1024);
#pragma unroll
    for (int i = 0; i < 2; ++i) vh[i] = l16 + (((wave >> 1) + i) & 1) * (12 * 1024);
  }
  STAMP(10);
  SB0();
#pragma unroll
  for (int s = 0; s < R; ++s) { issue(s, 1); SB0(); }
  STAMP(1);
  const int fr = lane & 15, g = lane >> 4, tq = (lane & 15) >> 2, tp = lane & 3;
  const int ml = wave * 16 + fr;
  u32x2 s1p[5];
  float2 st;
  auto resid = [&](int j) {
    const char* sx = shm;
    const char* sm = shm + 10 * 1024;
    if (j == 0) st = *(const float2*)(sm + ml * 8);
    const int nl = j * 16;
    u32x2 xx = *(const u32x2*)(sx + ml * 160 + (nl + 4 * g) * 2);
    u32x2 dd = *(const u32x2*)(sm + 512 + (nl + 4 * g) * 2);
#pragma unroll
    for (int h = 0; h < 2; ++h) {
      const float x0 = lo_f(xx[h]), x1 = hi_f(xx[h]);
      u32 d = pk_bf(x0 - st.x, x1 - st.x);
      u32 xn = pk_bf(lo_f(d) * st.y, hi_f(d) * st.y);
      s1p[j][h] = pk_bf(lo_f(xn) * lo_f(dd[h]), hi_f(xn) * hi_f(dd[h]));
    }
    asm volatile("" : "+v"(s1p[j]));
  };
  auto resid2 = [&](int j) {
    const char* sx = shm;
    const char* sy = shm + 10 * 1024;
    const int nl = j * 16;
    u32x2 xx = *(const u32x2*)(sx + ml * 160 + (nl + 4 * g) * 2);
    s4 yy = TRREAD(sy + (nl + 4 * g + tq) * 128 + (wave * 16 + 4 * tp) * 2);
    const u32x2 yw = __builtin_bit_cast(u32x2, yy);
#pragma unroll
    for (int h = 0; h < 2; ++h) {
      const float x0 = lo_f(xx[h]), x1 = hi_f(xx[h]);
      u32 a = pk_bf(lo_f(yw[h]) + lo_f(s1p[j][h]), hi_f(yw[h]) + hi_f(s1p[j][h]));
      s1p[j][h] = pk_bf(x0 + lo_f(a), x1 + hi_f(a));
    }
  };
  f4 acc[4][5];
#pragma unroll
  for (int i = 0; i < 4; ++i)
#pragma unroll
    for (int j = 0; j < 5; ++j) acc[i][j] = (f4){0.f, 0.f, 0.f, 0.f};
  const bool odd = wave & 1;
  SB0();
#pragma unroll
  for (int s = 0; s < NST; ++s) {
    if (s < NF) {
#pragma unroll
      for (int i = 0; i < 4; ++i)
#pragma unroll
        for (int j = 0; j < 5; ++j)
          acc[i][j] = __builtin_amdgcn_mfma_f32_16x16x32_bf16(__builtin_bit_cast(s8, fb[s % R][j]),
                                                              __builtin_bit_cast(s8, fa[s % R][i]), acc[i][j], 0, 0, 0);
    } else {
      const int h = s - NF;
      u32x2 af[4];
      af[0] = odd ? (u32x2){ha[h][0][2], ha[h][0][3]} : (u32x2){ha[h][0][0], ha[h][0][1]};
      af[1] = odd ? (u32x2){ha[h][1][0], ha[h][1][1]} : (u32x2){ha[h][0][2], ha[h][0][3]};
      af[2] = odd ? (u32x2){ha[h][1][2], ha[h][1][3]} : (u32x2){ha[h][1][0], ha[h][1][1]};
      af[3] = odd ? (u32x2){ha[h][0][0], ha[h][0][1]} : (u32x2){ha[h][1][2], ha[h][1][3]};
#pragma unroll
      for (int i = 0; i < 4; ++i)
#pragma unroll
        for (int j = 0; j < 5; ++j)
          acc[i][j] = __builtin_amdgcn_mfma_f32_16x16x16bf16_1k(__builtin_bit_cast(s4, hb[h][j]),
                                                               __builtin_bit_cast(s4, af[i]), acc[i][j], 0, 0, 0);
    }
    SB0();
    if (s + R < NST) issue(s + R);
    SB0();
    if (s == 4) {
      WAIT_V(R * 9 > 60 ? 60 : R * 9);
      __builtin_amdgcn_s_barrier();
      SB0();
    }
    if (s >= 5 && s < 10) { resid(s - 5); SB0(); }
    if (s == 10) { __builtin_amdgcn_s_barrier(); SB0(); }
    if (s == K3YS) { yload(); SB0(); STAMP(3); }
    if (s == 0) { STAMP(2); }
    if (s == 2) { STAMP(4); }
    if (s == 6) { STAMP(5); }
    if (s == 10) { STAMP(6); }
  }
  STAMP(7);
  char* red = shm + 20 * 1024;
#pragma unroll
  for (int i = 1; i < 4; ++i)
#pragma unroll
    for (int j = 0; j < 5; ++j)
      *(f4*)(red + ((((wave + i) & 3) * 3 + (i - 1)) * 5 + j) * 1024 + lane * 16) = acc[i][j];
  WAIT_V(0);
  __syncthreads();
  STAMP(9);
  u32x2 ov[5];
#pragma unroll
  for (int j = 0; j < 5; ++j) {
    resid2(j);
    f4 p0 = *(const f4*)(red + ((wave * 3 + 0) * 5 + j) * 1024 + lane * 16);
    f4 p1 = *(const f4*)(red + ((wave * 3 + 1) * 5 + j) * 1024 + lane * 16);
    f4 p2 = *(const f4*)(red + ((wave * 3 + 2) * 5 + j) * 1024 + lane * 16);
    f4 sum = (acc[0][j] + p0) + (p1 + p2);
    u32 m0p = pk_bf(sum[0], sum[1]), m1p = pk_bf(sum[2], sum[3]);
    ov[j] = (u32x2){pk_bf(lo_f(s1p[j][0]) + lo_f(m0p), hi_f(s1p[j][0]) + hi_f(m0p)),
                    pk_bf(lo_f(s1p[j][1]) + lo_f(m1p), hi_f(s1p[j][1]) + hi_f(m1p))};
  }
  u16* orow = out + (size_t)(m0 + ml) * DM + n0;
#pragma unroll
  for (int jp = 0; jp < 2; ++jp) {
    auto r0 = __builtin_amdgcn_permlane16_swap(ov[2 * jp][0], ov[2 * jp + 1][0], false, false);
    auto r1 = __builtin_amdgcn_permlane16_swap(ov[2 * jp][1], ov[2 * jp + 1][1], false, false);
    so16(orow + (2 * jp + (g & 1)) * 16 + 8 * (g >> 1), (u32x4){r0[0], r1[0], r0[1], r1[1]});
  }
  so8(orow + 64 + 4 * g, ov[4]);
  STAMP(8);
  STAMPW();
}

extern "C" __global__ __launch_bounds__(256, 2) void k2_kernel(char* ws, const u16* __restrict__ xin,
                                                    const u16* __restrict__ dskip, u16* __restrict__ out) {
  const u16* xnP = (const u16*)(ws + WS_XNP);
  const u16* w1P = (const u16*)(ws + WS_W1P);
  const float* b1f = (const float*)(ws + WS_B1F);
  const u16* lut = (const u16*)(ws + WS_LUT);
  u16* actF = (u16*)(ws + WS_ACTF);
  u16* actH = (u16*)(ws + WS_ACTH);
  u32* flags = (u32*)(ws + WS_FLAGS);
  __shared__ __attribute__((aligned(1024))) char shm[81920];
  const int t = threadIdx.x, lane = t & 63;
  const int wave = __builtin_amdgcn_readfirstlane(t >> 6);
  const int vb = blockIdx.x;
  if (vb >= K2_G1) {
    const int sb = vb - K2_G1, li = sb >> 3, grp = (sb & 7) * 10 + li / 3, part = li % 3;
    ssm_block<2>(shm, grp, 3 * part, part < 2 ? 3 : 2, xnP, (const u16*)(ws + WS_PG), (u16*)(ws + WS_YT), flags + grp * 3 + part);
    return;
  }
  const int xcd = vb & 7, idx = vb >> 3;
  const int mi = idx & 3, ni = idx >> 2;
  const int bm = (xcd >> 1) * 4 + mi, bn = (xcd & 1) * 8 + ni;
  STAMPB(2, (char*)actF - (16u << 20));
  STAMP(0);
  const int mtw = bm * 8 + 2 * wave, nt0 = 27 * (bn >> 2) + 7 * (bn & 3);
  const char* abase = (const char*)xnP + (size_t)mtw * (20 * 1024);
  const u32 va0 = lane * 16, va1 = lane * 16 + 20 * 1024;
  const u32 lds0 = (u32)(size_t)(const __attribute__((address_space(3))) char*)shm;
  const u32 va4 = lane * 16 + 4096;
  const char* bb1 = (const char*)w1P + (size_t)((nt0 + wave) * 20) * 1024;
  const char* bb2 = (const char*)w1P + (size_t)(nt0 * 20 + 5 * wave) * 1024;
  const u32 mc0 = lds0 + 8192 + wave * 5120;
  const u32 mc2 = wave == 3 ? lds0 + 73728 : mc0 + 40960;
  auto stageB = [&](auto GC, auto IC_) {
    constexpr int gq = decltype(GC)::value, i = decltype(IC_)::value;
    const char* src = gq < NT1 ? bb1 + gq * 5120 : bb2 + (size_t)((gq == 6 && bn == 15) ? 5 : gq) * 20480;
    const u32 m = gq % 3 == 2 ? mc2 : mc0 + (gq % 3) * 20480;
    if constexpr (i < 4) glds_o<i * 1024>(va0, src, m);
    else glds_o<0>(va4, src, m + 4096);
  };
  u32x4 fa[20][2];
  auto loadA = [&](int s) {
    const char* ap = abase + s * 1024;
    GLOAD4(fa[s][0], va0, ap);
    GLOAD4(fa[s][1], va1, ap);
  };
  glds_o<0>((lane < 28 ? lane : 27) * 16, (const char*)(b1f + nt0 * 16), lds0 + BIAS_OFF);
  static_for<5>([&](auto I) { stageB(IC<0>{}, I); });
#pragma unroll
  for (int s = 0; s < 5; ++s) loadA(s);
  static_for<5>([&](auto I) { stageB(IC<1>{}, I); });
#pragma unroll
  for (int s = 5; s < 10; ++s) loadA(s);
  static_for<5>([&](auto I) { stageB(IC<2>{}, I); });
  STAMP(1);
  WAIT_V(30);
  __builtin_amdgcn_s_barrier();
  STAMP(2);
  typedef unsigned short us2 __attribute__((ext_vector_type(2)));
  const u32 lb = lds0 + lane * 16, lbh = lds0 + lane * 16 + 65536, lbb = lds0 + BIAS_OFF + (lane >> 4) * 16;
  f4 acc[7][2];
  u32x4 fb[G1_D + 1];
  f4 bias[7];
  u32 hw[7][4];
  u32 res[7][4], rhi[7][4];
  u32 mx[4] = {0u, 0u, 0u, 0u}, zz, a0 = 0, a1 = 0;
  auto readB = [&](auto PC) {
    constexpr int q = decltype(PC)::value, off = g1_piece_lds(q);
    if constexpr (off < 65536) dsr128<off>(fb[q % (G1_D + 1)], lb);
    else dsr128<off - 65536>(fb[q % (G1_D + 1)], lbh);
  };
  const u32 kb = (0x10000u - (LUT_E0 << 7)) * 0x10001u;
  auto epi_fix = [&](int T) __attribute__((always_inline)) {
#pragma unroll
    for (int q = 0; q < 4; ++q) {
      const u32 z0 = hw[T][q] & 0x7FFFu, z1 = (hw[T][q] >> 16) & 0x7FFFu;
      const bool b0 = (z0 - (LUT_E0 << 7)) >= (u32)LUT_HALF, b1v = (z1 - (LUT_E0 << 7)) >= (u32)LUT_HALF;
      const float h0 = lo_f(hw[T][q]), h1 = hi_f(hw[T][q]);
      const float f0 = (z0 < (LUT_E0 << 7)) ? h0 * 0.59765625f : (h0 > 0.f ? h0 : h0 * 0.0f);
      const float f1 = (z1 < (LUT_E0 << 7)) ? h1 * 0.59765625f : (h1 > 0.f ? h1 : h1 * 0.0f);
      const u32 fw = pk_bf(f0, f1);
      res[T][q] = ((b0 ? fw : res[T][q]) & 0xFFFFu) | ((b1v ? fw : res[T][q]) & 0xFFFF0000u);
    }
  };
  const char* obase = (const char*)actF + ((size_t)mtw * 48 + 3 * bn) * 1024;
#pragma unroll
  for (int T = 0; T < NT1; ++T) DSR128(bias[T], lbb, T * 64);
  static_for<G1_D>(readB);
  static_for<140>([&](auto PC) {
    constexpr int p = decltype(PC)::value;
    constexpr int T = g1_tile(p), kt = g1_kt(p), gq = p / 20, lp = p % 20;
    if constexpr (G1.vS[p] >= 0) {
      WAIT_L(0);
      WAIT_V(G1.vS[p]);
      __builtin_amdgcn_s_barrier();
      if (gq == 0) { STAMP(3); }
      if (gq == 1) { STAMP(4); }
      if (gq == 3) { STAMP(5); }
      if (gq == 5) { STAMP(6); }
    }
    static_for<3>([&](auto RC) {
      constexpr int r = decltype(RC)::value;
      if constexpr (p == G1.sp[r]) {
        if constexpr (!(r < 2 && G1.ws[r < 2 ? 8 * r + 8 : 0] < p)) max3u(mx[r], a0, a1);
        if (__builtin_expect(__builtin_amdgcn_ballot_w64((mx[r] & 0xFFFFu) >= 2u * LUT_HALF) != 0, 0)) {
          epi_fix(2 * r);
          epi_fix(2 * r + 1);
        }
        SB0();
#pragma unroll
        for (int i = 0; i < 2; ++i)
          st16s(va0, obase + (i * 48 + r) * 1024,
                (u32x4){res[2 * r][2 * i], res[2 * r][2 * i + 1], res[2 * r + 1][2 * i], res[2 * r + 1][2 * i + 1]});
      }
    });
    if constexpr (G1.vA[p] >= 0) {
      WAIT_V(G1.vA[p]);
      pin2(fa[kt][0], fa[kt][1]);
    }
    if constexpr (p < 40 && (p & 1) == 0) gload4(fa[10 + p / 4][(p >> 1) & 1], (p & 2) ? va1 : va0, abase + (10 + p / 4) * 1024);
    if constexpr (g1_vmB(p) >= 0) stageB(IC<3 + g1_vmB(p) / 5>{}, IC<g1_vmB(p) % 5>{});
    if constexpr (g1_vmL(p) >= 0) {
      glds_o<g1_vmL(p) * 1024>(va0, (const char*)lut + wave * 4096, lds0 + (wave & 1) * 4096 + (wave >> 1) * LUT_NEG);
    }
    if constexpr (p + G1_D < 140) readB(IC<p + G1_D>{});
    if constexpr (lp == 12 && gq + 1 >= NT1 && gq < 6) dsr128<(gq + 1) * 64>(bias[gq + 1], lbb);
    WAIT_L(G1.lB[p]);
    SB0();
    constexpr int w = g1_word(p), wT = w >= 0 ? w >> 2 : 0, wq = w & 3;
    if constexpr (w < 0) step_p<kt == 0>(acc[T][0], acc[T][1], fb[p % (G1_D + 1)], fa[kt][0], fa[kt][1], bias[T]);
    else if constexpr (p == G1.ws[w])
      step_x<kt == 0>(acc[T][0], acc[T][1], fb[p % (G1_D + 1)], fa[kt][0], fa[kt][1], bias[T], hw[wT][wq], zz, mx[w > 0 ? (w - 1) >> 3 : 0],
                      acc[wT][wq >> 1][2 * (wq & 1)], acc[wT][wq >> 1][2 * (wq & 1) + 1], a0, a1, kb);
    else step_y<kt == 0>(acc[T][0], acc[T][1], fb[p % (G1_D + 1)], fa[kt][0], fa[kt][1], bias[T], a0, a1, res[wT][wq], rhi[wT][wq], zz);
    constexpr int cw = g1_comb(p);
    if constexpr (cw >= 0) comb(res[cw >> 2][cw & 3], rhi[cw >> 2][cw & 3]);
    SB0();
  });
  STAMP(7);
  asm volatile("s_nop 7\n\ts_nop 7\n\ts_nop 7" : "+v"(acc[6][0]), "+v"(acc[6][1]) :: "memory");
  if ((bn & 3) != 3) {
#pragma unroll
    for (int q = 0; q < 4; ++q) epi_word(hw[6][q], res[6][q], rhi[6][q], mx[3], acc[6][q >> 1][2 * (q & 1)], acc[6][q >> 1][2 * (q & 1) + 1], kb);
    asm volatile("s_waitcnt lgkmcnt(0)" : "+v"(res[6][0]), "+v"(res[6][1]), "+v"(res[6][2]), "+v"(res[6][3]),
                 "+v"(rhi[6][0]), "+v"(rhi[6][1]), "+v"(rhi[6][2]), "+v"(rhi[6][3]) :: "memory");
#pragma unroll
    for (int q = 0; q < 4; ++q) res[6][q] |= rhi[6][q] << 16;
    if (__builtin_expect(__builtin_amdgcn_ballot_w64((mx[3] & 0xFFFFu) >= 2u * LUT_HALF) != 0, 0)) epi_fix(6);
    st16s(va0, (const char*)actH + ((size_t)(mtw >> 1) * 12 + 3 * (bn >> 2) + (bn & 3)) * 1024,
          (u32x4){res[6][0], res[6][1], res[6][2], res[6][3]});
  }
  STAMP(8);
  WAIT_V(0);
  if (lane == 0) st4(flags + 1024 + (bm * 4 + wave) * 16 + bn, 1u);
  STAMP(11);
  STAMPW();
  RAW_BARRIER();
  g2_block(shm, vb, ws, xin, dskip, out);
}
extern "C" __global__ void k4_end() {}
extern "C" void kernel_launch(void* const* d_in, const int* in_sizes, int n_in,
                              void* d_out, int out_size, void* d_ws, size_t ws_size,
                              hipStream_t stream) {
  const u16* u = (const u16*)d_in[0];
  const u16* lar = (const u16*)d_in[1];
  const u16* aim = (const u16*)d_in[2];
  const u16* cre = (const u16*)d_in[3];
  const u16* cim = (const u16*)d_in[4];
  const u16* logdt = (const u16*)d_in[5];
  const u16* dskip = (const u16*)d_in[6];
  const u16* w1 = (const u16*)d_in[8];
  const u16* b1 = (const u16*)d_in[9];
  const u16* w2 = (const u16*)d_in[10];
  u16* out = (u16*)d_out;
  char* ws = (char*)d_ws;
  if (ws_size < (32u << 20)) return;
  u16* xnP = (u16*)(ws);
  u16* w1P = (u16*)(ws + (4u << 20));
  float2* stats = (float2*)(ws + (12u << 20) + 4096);
  u16* lut = (u16*)(ws + (12u << 20) + 32768);
  float* b1f = (float*)(ws + (12u << 20) + 65536);
  u32* flags = (u32*)(ws + (12u << 20) + 98304);
  u16* yT = (u16*)(ws + (13u << 20));
  u16* actF = (u16*)(ws + (16u << 20));
  u16* actH = (u16*)(ws + (22u << 20));
  u16* w2F = (u16*)(ws + (24u << 20));
  u16* w2H = (u16*)(ws + (26u << 20));
  u16* Pg = (u16*)(ws + (28u << 20));

  k1_kernel<<<K1_LN + K1_KG + K1_RP + K1_RQ + K1_LUT, 256, 0, stream>>>(u, xnP, stats, lut, w1, w1P, w2, w2F, w2H, b1, b1f,
                                                                        lar, aim, cre, cim, logdt, Pg, flags);
  k2_kernel<<<K2_G1 + K2_SSM, 256, 0, stream>>>(ws, u, dskip, out);
}
```

```cpp
#include <hip/hip_runtime.h>
#include <hip/hip_bf16.h>

#define SEQ 2048
#define DM 640
#define NS 128
#define FF 1728

typedef unsigned short u16;
typedef short s4 __attribute__((ext_vector_type(4)));
typedef short s8 __attribute__((ext_vector_type(8)));
typedef _Float16 h8 __attribute__((ext_vector_type(8)));
typedef float f4 __attribute__((ext_vector_type(4)));
typedef unsigned int u32;
typedef unsigned int u32x4 __attribute__((ext_vector_type(4)));
typedef unsigned int u32x2 __attribute__((ext_vector_type(2)));

__device__ __forceinline__ float bf2f(u16 v) { return __uint_as_float(((u32)v) << 16); }
__device__ __forceinline__ u16 f2bf(float f) {
  u32 u = __float_as_uint(f);
  u32 r = (u + 0x7FFFu + ((u >> 16) & 1u)) >> 16;
  return (u16)r;
}
__device__ __forceinline__ float bfr(float f) { return bf2f(f2bf(f)); }

#define STAMPB(kid, base)
#define STAMP(k)
#define STAMPW()
typedef __bf16 bf2_t __attribute__((ext_vector_type(2)));
typedef float f2_t __attribute__((ext_vector_type(2)));
__device__ __forceinline__ u32 pk_bf(float a, float b) {
  f2_t v = {a, b};
  bf2_t r = __builtin_convertvector(v, bf2_t);
  return __builtin_bit_cast(u32, r);
}
__device__ __forceinline__ float lo_f(u32 w) { return __uint_as_float(w << 16); }
__device__ __forceinline__ float hi_f(u32 w) { return __uint_as_float(w & 0xFFFF0000u); }

#define WAIT_V(n) asm volatile("s_waitcnt vmcnt(%0)" ::"n"(n) : "memory")
#define RAW_BARRIER() do { asm volatile("s_waitcnt lgkmcnt(0)" ::: "memory"); __builtin_amdgcn_s_barrier(); } while (0)
__device__ __forceinline__ void glds16(const void* gsrc, const void* lp) {
  unsigned keep;
  unsigned lds_dst = (unsigned)(size_t)(const __attribute__((address_space(3))) char*)lp;
  asm volatile("s_mov_b32 %0, m0\n\ts_mov_b32 m0, %2\n\ts_nop 0\n\tglobal_load_lds_dwordx4 %1, off\n\ts_mov_b32 m0, %0"
               : "=&s"(keep) : "v"(gsrc), "s"(lds_dst) : "memory");
}
#define GLDS(gp, lp) glds16((const void*)(gp), (const void*)(lp))

#define STF " sc1"
__device__ __forceinline__ void st16(void* p, u32x4 v) { asm volatile("global_store_dwordx4 %0, %1, off" STF :: "v"(p), "v"(v) : "memory"); }
__device__ __forceinline__ void st8(void* p, u32x2 v) { asm volatile("global_store_dwordx2 %0, %1, off" STF :: "v"(p), "v"(v) : "memory"); }
__device__ __forceinline__ void st4(void* p, u32 v) { asm volatile("global_store_dword %0, %1, off" STF :: "v"(p), "v"(v) : "memory"); }
__device__ __forceinline__ void st2(void* p, u32 v) { asm volatile("global_store_short %0, %1, off" STF :: "v"(p), "v"(v) : "memory"); }

#define LUT_E0 102
#define LUT_E1 134
#define LUT_HALF ((LUT_E1 - LUT_E0) * 128)
#define LUT_N (2 * LUT_HALF)

__device__ __forceinline__ float mish_bf(float h) {
  float e = bfr(expf(-fabsf(h)));
  float l = bfr(log1pf(e));
  float sp = bfr(l + (h > 0.f ? h : 0.f));
  float th = bfr(tanhf(sp));
  return bfr(h * th);
}

#define KRLEN 2080
#define WFENCE() do { asm volatile("" ::: "memory"); __builtin_amdgcn_wave_barrier(); asm volatile("" ::: "memory"); } while (0)
__device__ __forceinline__ void cexp_p(float arl2, float w, float p, float& re, float& im) {
  float mag = __builtin_amdgcn_exp2f(arl2 * p);
  float rev = w * p;
  rev = rev - floorf(rev);
  re = mag * __builtin_amdgcn_cosf(rev);
  im = mag * __builtin_amdgcn_sinf(rev);
}
__device__ __forceinline__ void cmul(float ar, float ai, float br, float bi, float& cr, float& ci) {
  float r = fmaf(ar, br, -(ai * bi));
  float i = fmaf(ar, bi, ai * br);
  cr = r; ci = i;
}
__device__ __forceinline__ u32x2 split2(float a, float b) {
  u32 h = pk_bf(a, b);
  u32 l = pk_bf(a - lo_f(h), b - hi_f(h));
  return (u32x2){h, l};
}

#define CODE_TOUCH(sym0, sym1, line, d0) do { \
    unsigned long long pc_; int o0_, o1_; \
    asm volatile("s_getpc_b64 %0\n.Lpc_here%=:\n\ts_mov_b32 %1, " sym0 "-.Lpc_here%=\n\ts_mov_b32 %2, " sym1 "-.Lpc_here%=" \
                 : "=s"(pc_), "=s"(o0_), "=s"(o1_)); \
    int len_ = o1_ - o0_; \
    if (len_ < 4 || len_ > 65536) { len_ = 4; o0_ = 0; } \
    const char* cb_ = (const char*)(pc_ + (long long)o0_); \
    const int oa_ = (line) * 128 < len_ - 4 ? (line) * 128 : len_ - 4; \
    asm volatile("global_load_dword %0, %1, %2" : "=v"(d0) : "v"(oa_ & ~3), "s"(cb_) : "memory"); \
  } while (0)

#define WS_XNP 0u
#define WS_W1P (4u << 20)
#define WS_STATS ((12u << 20) + 4096)
#define WS_LUT ((12u << 20) + 32768)
#define WS_B1F ((12u << 20) + 65536)
#define WS_FLAGS ((12u << 20) + 98304)
#define WS_YT (13u << 20)
#define WS_ACTF (16u << 20)
#define WS_ACTH (22u << 20)
#define WS_W2F (24u << 20)
#define WS_W2H (26u << 20)
#define WS_PG (28u << 20)
#define K1_LN 256
#define K1_KG 640
#define K1_RP 135
#define K1_RQ 160
#define PPITCH 768
#define K1_LUT 32
#define WPITCH 544
typedef _Float16 h2_t __attribute__((ext_vector_type(2)));
__device__ __forceinline__ u32 pk_h(float a, float b) {
  f2_t v = {a, b};
  h2_t r = __builtin_convertvector(v, h2_t);
  return __builtin_bit_cast(u32, r);
}
#define TRREAD(p) __builtin_amdgcn_ds_read_tr16_b64_v4i16((__attribute__((address_space(3))) s4*)(p))

__device__ __forceinline__ void repack_load(const u16* __restrict__ w, int N, int ku, int nu, char* lds, int tl) {
#pragma unroll
  for (int i = 0; i < 2; ++i) {
    int idx = tl + 256 * i, r = idx >> 3, c = idx & 7;
    u32x4 v = *(const u32x4*)(w + (size_t)(ku * 64 + r) * N + nu * 64 + c * 8);
    *(u32x4*)(lds + r * 160 + c * 16) = v;
  }
}
__device__ __forceinline__ void repack_store(u16* __restrict__ wP, int KB, int ku, int nu, const char* lds, int tl) {
  const int wl = tl >> 6, lane = tl & 63, g = lane >> 4, tq = (lane & 15) >> 2, tp = lane & 3;
#pragma unroll
  for (int kb2 = 0; kb2 < 2; ++kb2) {
    const char* p0 = lds + (kb2 * 32 + 4 * g + tq) * 160 + wl * 32 + tp * 8;
    s4 b0 = TRREAD(p0);
    s4 b1 = TRREAD(p0 + 16 * 160);
    s8 bf = {b0[0], b0[1], b0[2], b0[3], b1[0], b1[1], b1[2], b1[3]};
    st16(wP + (((size_t)(nu * 4 + wl) * KB + ku * 2 + kb2) * 64 + lane) * 8, __builtin_bit_cast(u32x4, bf));
  }
}

extern "C" __global__ __launch_bounds__(256) void k1_kernel(const u16* __restrict__ u, u16* __restrict__ xnP,
                                                 float2* __restrict__ stats, u16* __restrict__ lut,
                                                 const u16* __restrict__ w1, u16* __restrict__ w1P,
                                                 const u16* __restrict__ w2, u16* __restrict__ w2F, u16* __restrict__ w2H,
                                                 const u16* __restrict__ b1, float* __restrict__ b1f,
                                                 const u16* __restrict__ lar, const u16* __restrict__ aim,
                                                 const u16* __restrict__ cre, const u16* __restrict__ cim,
                                                 const u16* __restrict__ logdt, u16* __restrict__ Pg, u32* __restrict__ flags) {
  __shared__ __attribute__((aligned(16))) char k1s[57 * WPITCH];
  const int t = threadIdx.x, lane = t & 63;
  const int wave = __builtin_amdgcn_readfirstlane(t >> 6);
  STAMPB(0, xnP);
  STAMP(0);
#define CT1 32
  u32 tc = 0;
  if (__builtin_expect(blockIdx.x < K1_LN, 1)) {
    if (t < 96 ? blockIdx.x < CT1 : t < 112) CODE_TOUCH("k1_kernel", "k4_end", t < 96 ? t : (int)(blockIdx.x >> 3) * 16 + t - 16, tc);
  }
  if (blockIdx.x < K1_LN) {
    u16 (*tile)[DM + 8] = (u16 (*)[DM + 8])k1s;
    const int r = wave * 2 + (lane >> 5), q = lane & 31;
    const int rg = blockIdx.x;
    const int row = rg * 8 + r;
    float v[20];
    const u32x2* src = (const u32x2*)(u + (size_t)row * DM);
#pragma unroll
    for (int i = 0; i < 5; ++i) {
      u32x2 w = src[q + 32 * i];
      v[i * 4 + 0] = lo_f(w[0]); v[i * 4 + 1] = hi_f(w[0]);
      v[i * 4 + 2] = lo_f(w[1]); v[i * 4 + 3] = hi_f(w[1]);
    }
    float s = 0.f;
#pragma unroll
    for (int i = 0; i < 20; ++i) s += v[i];
    STAMP(1);
#pragma unroll
    for (int o = 1; o < 32; o <<= 1) s += __shfl_xor(s, o);
    const float mean = s * (1.0f / DM);
    float ss = 0.f;
#pragma unroll
    for (int i = 0; i < 20; ++i) { float d = v[i] - mean; ss += d * d; }
#pragma unroll
    for (int o = 1; o < 32; o <<= 1) ss += __shfl_xor(ss, o);
    const float var = ss * (1.0f / DM);
    const float mb = bfr(mean);
    const float vb = bfr(var);
    const float den = bfr(sqrtf(bfr(vb + 1e-5f)));
    const float rden = 1.0f / den;
    if (q == 0) st8(stats + row, (u32x2){__float_as_uint(mb), __float_as_uint(rden)});
#pragma unroll
    for (int i = 0; i < 5; ++i) {
      u32 d0 = pk_bf(v[i * 4 + 0] - mb, v[i * 4 + 1] - mb);
      u32 d1 = pk_bf(v[i * 4 + 2] - mb, v[i * 4 + 3] - mb);
      u32x2 w = {pk_bf(lo_f(d0) * rden, hi_f(d0) * rden), pk_bf(lo_f(d1) * rden, hi_f(d1) * rden)};
      *(u32x2*)&tile[r][(q + 32 * i) * 4] = w;
    }
    __syncthreads();
    asm volatile("" :: "v"(tc));
    STAMP(2);
    const int mt = rg >> 1, half = rg & 1;
#pragma unroll
    for (int i = 0; i < 3; ++i) {
      int idx = t + 256 * i;
      if (idx < 640) {
        int kb = idx >> 5, g = (idx >> 3) & 3, fr = idx & 7;
        u32x2 a = *(const u32x2*)&tile[fr][kb * 32 + 4 * g];
        u32x2 b = *(const u32x2*)&tile[fr][kb * 32 + 16 + 4 * g];
        u32x4 w = {a[0], a[1], b[0], b[1]};
        st16(xnP + (((size_t)mt * 20 + kb) * 64 + g * 16 + half * 8 + fr) * 8, w);
      }
    }
    STAMP(3);
    STAMPW();
    return;
  }
  if (blockIdx.x < K1_LN + K1_KG) {
    const int d = blockIdx.x - K1_LN;
    const int n = t & 127;
    char* shm = k1s;
    const u16 r_dt = logdt[d];
    const u16 r_ar = lar[d * NS + n], r_ai = aim[d * NS + n];
    const u16 r_cr = cre[d * NS + n], r_ci = cim[d * NS + n];
    const u16 r_cr2 = cre[d * NS + (n ^ 64)], r_ci2 = cim[d * NS + (n ^ 64)];
    const float dt = bfr(expf(bf2f(r_dt)));
    const float ar = -bfr(expf(bf2f(r_ar))) * dt;
    const float ai = bf2f(r_ai) * dt;
    float cr = bf2f(r_cr) * dt, ci = bf2f(r_ci) * dt;
    float bs = (fabsf(cr) + fabsf(ci)) + (fabsf(bf2f(r_cr2) * dt) + fabsf(bf2f(r_ci2) * dt));
#pragma unroll
    for (int o = 1; o < 64; o <<= 1) bs += __shfl_xor(bs, o);
    const float bound = 2.0f * bs;
    int ex = 0;
    if (bound > 0.f && bound < 3.0e38f) { (void)frexpf(bound, &ex); }
    int es = 15 - ex;
    es = es > 100 ? 100 : (es < -100 ? -100 : es);
    const float sc2 = ldexpf(2.0f, es);
    const float kscale = ldexpf(1.0f, -es);
    cr *= sc2; ci *= sc2;
    const float arl2 = ar * 1.4426950408889634f;
    const float w = ai * 0.15915494309189535f;
    STAMP(1);
    char* wr = shm + n * 4;
    float eHr, eHi;
    cexp_p(arl2, w, 128.0f, eHr, eHi);
    if (wave < 2) {
      float e8r, e8i, e4r, e4i, gr, gi;
      cexp_p(arl2, w, 8.0f, e8r, e8i);
      cexp_p(arl2, w, 4.0f, e4r, e4i);
      cexp_p(arl2, w, 512.0f, gr, gi);
      cmul(gr, gi, cr, ci, gr, gi);
      float lr = 1.f, lim = 0.f;
      *(u32*)(wr) = pk_h(1.f, 0.f);
#pragma unroll
      for (int j = 1; j < 16; ++j) {
        cmul(lr, lim, e8r, e8i, lr, lim);
        *(u32*)(wr + j * WPITCH) = pk_h(lr, -lim);
        if (j < 5) {
          float hr, hi;
          *(u32*)(wr + (40 + (j - 1) * 2) * WPITCH) = pk_h(gr, gi);
          cmul(gr, gi, e4r, e4i, hr, hi);
          *(u32*)(wr + (41 + (j - 1) * 2) * WPITCH) = pk_h(hr, hi);
          cmul(gr, gi, eHr, eHi, gr, gi);
        } else if (j < 14) {
          *(u32*)(wr + (48 + j - 5) * WPITCH) = pk_h(gr, gi);
          if (j < 13) cmul(gr, gi, eHr, eHi, gr, gi);
        }
      }
    } else {
      float Rr[8], Ri[8];
      Rr[0] = 1.f; Ri[0] = 0.f;
      cexp_p(arl2, w, 1.0f, Rr[1], Ri[1]);
#pragma unroll
      for (int r = 2; r < 8; ++r) cmul(Rr[r - 1], Ri[r - 1], Rr[1], Ri[1], Rr[r], Ri[r]);
      float gr = cr, gi = ci;
#pragma unroll
      for (int ii = 0; ii < 2; ++ii) {
        *(u32*)(wr + (16 + ii * 8) * WPITCH) = pk_h(gr, gi);
#pragma unroll
        for (int r = 1; r < 8; ++r) {
          float hr, hi;
          cmul(gr, gi, Rr[r], Ri[r], hr, hi);
          *(u32*)(wr + (16 + ii * 8 + r) * WPITCH) = pk_h(hr, hi);
        }
        cmul(gr, gi, eHr, eHi, gr, gi);
      }
#pragma unroll
      for (int ii = 2; ii < 4; ++ii) {
        *(u32*)(wr + (32 + (ii - 2) * 4) * WPITCH) = pk_h(gr, gi);
#pragma unroll
        for (int r = 1; r < 4; ++r) {
          float hr, hi;
          cmul(gr, gi, Rr[2 * r], Ri[2 * r], hr, hi);
          *(u32*)(wr + (32 + (ii - 2) * 4 + r) * WPITCH) = pk_h(hr, hi);
        }
        if (ii < 3) cmul(gr, gi, eHr, eHi, gr, gi);
      }
    }
    __syncthreads();
    STAMP(2);
    const int b = lane & 15, g = lane >> 4;
    u16* P = Pg + (size_t)d * PPITCH;
    if (wave < 3) {
      const int rowa = 16 + 16 * wave + b;
      const char* ra = shm + (rowa > 56 ? 56 : rowa) * WPITCH + g * 16;
      const char* rb = shm + b * WPITCH + g * 16;
      f4 ka = (f4){0.f, 0.f, 0.f, 0.f};
#pragma unroll
      for (int k8 = 0; k8 < 8; ++k8) {
        h8 a = *(const h8*)(ra + k8 * 64);
        h8 bb = *(const h8*)(rb + k8 * 64);
        ka = __builtin_amdgcn_mfma_f32_16x16x32_f16(a, bb, ka, 0, 0, 0);
      }
      const u32 k01 = pk_h(ka[0], ka[1]), k23 = pk_h(ka[2], ka[3]);
      if (wave == 0) {
        st8(&P[(g >> 1) * 128 + (g & 1) * 4 + 8 * b], (u32x2){k01, k23});
      } else if (wave == 1) {
        const int i1 = g < 2 ? 256 + 64 * g + 4 * b : 384 + 64 * (g - 2) + 2 * b;
        st4(&P[i1], k01);
        st4(&P[i1 + (g < 2 ? 2 : 32)], k23);
      } else {
        const int i2 = 512 + 64 * g + b;
        if (g < 3) st2(&P[i2], k01);
        if (g < 2) {
          st2(&P[i2 + 16], k01 >> 16);
          st2(&P[i2 + 32], k23); st2(&P[i2 + 48], k23 >> 16);
        }
      }
    } else if (lane == 0) {
      st4(&P[704], __float_as_uint(kscale));
    }
    STAMP(3);
    STAMPW();
    return;
  }
  if (blockIdx.x < K1_LN + K1_KG + K1_RP) {
    const int u0 = (blockIdx.x - (K1_LN + K1_KG)) * 2;
    repack_load(w1, FF, u0 / 27, u0 % 27, k1s, t);
    repack_load(w1, FF, (u0 + 1) / 27, (u0 + 1) % 27, k1s + 10240, t);
    __syncthreads();
    repack_store(w1P, DM / 32, u0 / 27, u0 % 27, k1s, t);
    repack_store(w1P, DM / 32, (u0 + 1) / 27, (u0 + 1) % 27, k1s + 10240, t);
    STAMP(3);
    STAMPW();
    return;
  }
  if (blockIdx.x < K1_LN + K1_KG + K1_RP + K1_RQ) {
    const int un = blockIdx.x - (K1_LN + K1_KG + K1_RP);
    const int bn = un / 10, nu = un % 10;
    const int f0 = 16 * (27 * (bn >> 2) + 7 * (bn & 3));
    const int rows = (bn & 3) == 3 ? 96 : 112;
    u32x4 v[4];
#pragma unroll
    for (int i = 0; i < 4; ++i) {
      const int r = (t >> 3) + 32 * i;
      v[i] = *(const u32x4*)(w2 + (size_t)(f0 + (r < rows ? r : rows - 1)) * DM + nu * 64 + (t & 7) * 8);
    }
#pragma unroll
    for (int i = 0; i < 4; ++i) *(u32x4*)(k1s + ((t >> 3) + 32 * i) * 160 + (t & 7) * 16) = v[i];
    __syncthreads();
    const int g = lane >> 4, tq = (lane & 15) >> 2, tp = lane & 3;
    const int nt = nu * 4 + wave;
    const char* p0 = k1s + (4 * g + tq) * 160 + wave * 32 + tp * 8;
#pragma unroll
    for (int p = 0; p < 3; ++p) {
      s4 b0 = TRREAD(p0 + (32 * p) * 160);
      s4 b1v = TRREAD(p0 + (32 * p + 16) * 160);
      s8 bf = {b0[0], b0[1], b0[2], b0[3], b1v[0], b1v[1], b1v[2], b1v[3]};
      st16(w2F + (((size_t)nt * 48 + 3 * bn + p) * 64 + lane) * 8, __builtin_bit_cast(u32x4, bf));
    }
    if (rows == 112) {
      s4 b0 = TRREAD(p0 + 96 * 160);
      st8(w2H + (((size_t)nt * 12 + 3 * (bn >> 2) + (bn & 3)) * 64 + lane) * 4, __builtin_bit_cast(u32x2, b0));
    }
    STAMP(3);
    STAMPW();
    return;
  }
  {
    const int idx = (blockIdx.x - (K1_LN + K1_KG + K1_RP + K1_RQ)) * 256 + t;
    const int sgn = idx >= 4096, i = idx - sgn * 4096;
    u32 td = 0;
    if (idx < FF) st4(b1f + idx, (u32)b1[idx] << 16);
    if (idx >= 2048 && idx < 4096) st4(flags + (idx - 2048), 0u);
    u32 r = 0;
    if (i < LUT_HALF) r = f2bf(mish_bf(bf2f((u16)((i + (LUT_E0 << 7)) | (sgn << 15)))));
    st2(lut + idx, r);
    asm volatile("s_waitcnt vmcnt(0)" :: "v"(td) : "memory");
  }
}

#define K2_G1 256
#define K2_SSM 240
#define KCPITCH 2080
#define SB0() __builtin_amdgcn_sched_barrier(0)
template <int V> struct IC { static constexpr int value = V; };
template <int I, int N, class F> __device__ __forceinline__ void static_for_i(F&& f) {
  if constexpr (I < N) { f(IC<I>{}); static_for_i<I + 1, N>(f); }
}
template <int N, class F> __device__ __forceinline__ void static_for(F&& f) { static_for_i<0, N>(f); }
__device__ __forceinline__ void glds16s(u32 voff, const void* sbase, const void* lp) {
  unsigned keep;
  unsigned lds_dst = (unsigned)(size_t)(const __attribute__((address_space(3))) char*)lp;
  asm volatile("s_mov_b32 %0, m0\n\ts_mov_b32 m0, %3\n\ts_nop 0\n\tglobal_load_lds_dwordx4 %1, %2\n\ts_mov_b32 m0, %0"
               : "=&s"(keep) : "v"(voff), "s"(sbase), "s"(lds_dst) : "memory");
}
#define MFMA_BF(acc, a, b) \
  asm volatile("v_mfma_f32_16x16x32_bf16 %0, %1, %2, %0" : "+v"(acc) : "v"(a), "v"(b))
#define MFMA_BF0(acc, a, b) \
  asm volatile("v_mfma_f32_16x16x32_bf16 %0, %1, %2, 0" : "=&v"(acc) : "v"(a), "v"(b))
#define GLOAD4(dst, voff, sbase) \
  asm volatile("global_load_dwordx4 %0, %1, %2" : "=v"(dst) : "v"(voff), "s"(sbase) : "memory")
#define DSR64(dst, addr, off) asm volatile("ds_read_b64 %0, %1 offset:%2" : "=v"(dst) : "v"(addr), "n"(off) : "memory")
#define DSR128(dst, addr, off) asm volatile("ds_read_b128 %0, %1 offset:%2" : "=v"(dst) : "v"(addr), "n"(off) : "memory")

#define NT1 4
#define G1_D 4
#define LUT_NEG 65536
#define BIAS_OFF 78848
constexpr int g1_chunk(int slot, int w) { return (slot == 2 && w == 3) ? 73728 : 8192 + (slot * 4 + w) * 5120; }
constexpr int g1_piece_lds(int p) {
  return p < 20 * NT1 ? g1_chunk((p / 20) % 3, p % 4) + ((p % 20) / 4) * 1024
                      : g1_chunk((p / 20) % 3, (p % 20) / 5) + (p % 5) * 1024;
}
constexpr int g1_tile(int p) { return p < 20 * NT1 ? p % NT1 : p / 20; }
constexpr int g1_kt(int p) { return p < 20 * NT1 ? p / NT1 : p % 20; }
constexpr int g1_fin(int T) { return T < NT1 ? 19 * NT1 + T : 20 * T + 19; }
constexpr int g1_vmB(int p) { return (p & 1) && p >= 17 && p < 86 && (p - 17) % 20 < 10 ? ((p - 17) / 20) * 5 + ((p - 17) % 20) / 2 : -1; }
constexpr int g1_vmL(int p) { return (p == 47 || p == 49 || p == 51 || p == 53) ? (p - 47) / 2 : -1; }
struct G1S { int vA[140], vS[140], lB[140], ws[24], cs[24], sp[3]; };
constexpr G1S g1_sched() {
  G1S S{};
  for (int w = 0; w < 24; ++w) {
    const int e = g1_fin(w >> 2) + 2;
    S.ws[w] = (w && S.ws[w - 1] + 2 > e) ? S.ws[w - 1] + 2 : e;
  }
  for (int w = 0; w < 24; ++w) S.cs[w] = S.ws[w] + G1_D + 2;
  for (int r = 0; r < 3; ++r) S.sp[r] = S.cs[8 * r + 7] + 1;
  int idxA[20] = {}, idxB[7] = {}, idxL = 0, ldB[140] = {};
  int v = 1;
  for (int h = 0; h < 3; ++h) {
    v += 5; idxB[h] = v;
    if (h < 2) for (int k = 0; k < 5; ++k) { v += 2; idxA[5 * h + k] = v; }
  }
  int l = NT1;
  for (int q = 0; q < G1_D; ++q) ldB[q] = ++l;
  for (int q = 0; q < 140; ++q) {
    const int gq = q / 20, lp = q % 20;
    S.vA[q] = -1; S.vS[q] = -1;
    if (lp == 20 - G1_D && gq < 6) {
      int need = idxB[gq + 1];
      if (gq + 1 == NT1 && idxL > need) need = idxL;
      S.vS[q] = v - need;
    }
    for (int r = 0; r < 3; ++r) if (q == S.sp[r]) v += 2;
    if (q < 20 * NT1 && q % NT1 == 0) S.vA[q] = v - idxA[q / NT1];
    if (q < 40 && (q & 1) == 0) { v += 1; idxA[10 + q / 4] = v; }
    if (g1_vmB(q) >= 0) { v += 1; idxB[3 + g1_vmB(q) / 5] = v; }
    if (g1_vmL(q) >= 0) { v += 1; idxL = v; }
    if (q + G1_D < 140) ldB[q + G1_D] = ++l;
    if (lp == 12 && gq + 1 >= NT1 && gq < 6) ++l;
    S.lB[q] = l - ldB[q];
    for (int w = 0; w < 24; ++w) if (S.ws[w] + 1 == q) l += 2;
  }
  return S;
}
constexpr G1S G1 = g1_sched();
constexpr int g1_word(int p) { for (int w = 0; w < 24; ++w) if (p >= G1.ws[w] && p < G1.ws[w] + 2) return w; return -1; }
constexpr int g1_comb(int p) { for (int w = 0; w < 24; ++w) if (p == G1.cs[w]) return w; return -1; }
static_assert(G1.sp[2] < 140 && G1.ws[0] >= 20 * NT1 - G1_D, "schedule");
#define WAIT_L(n) __builtin_amdgcn_s_waitcnt(0xC07F | (((n) > 15 ? 15 : (n)) << 8))
#define MF_ "v_mfma_f32_16x16x32_bf16 "
#define SDWA_(h) " dst_sel:DWORD dst_unused:UNUSED_PAD src0_sel:DWORD src1_sel:WORD_" #h "\n\t"
template <bool F> __device__ __forceinline__ void step_p(f4& c0, f4& c1, const u32x4& b, const u32x4& x0, const u32x4& x1, const f4& bs) {
  if constexpr (F) asm volatile(MF_ "%0, %2, %3, %5\n\t" MF_ "%1, %2, %4, %5" : "=&v"(c0), "=&v"(c1) : "v"(b), "v"(x0), "v"(x1), "v"(bs));
  else asm volatile(MF_ "%0, %2, %3, %0\n\t" MF_ "%1, %2, %4, %1" : "+v"(c0), "+v"(c1) : "v"(b), "v"(x0), "v"(x1));
}
template <bool F> __device__ __forceinline__ void step_x(f4& c0, f4& c1, const u32x4& b, const u32x4& x0, const u32x4& x1, const f4& bs,
                                                         u32& w, u32& z, u32& mx, float e0, float e1, u32 a0, u32 a1, u32 kb) {
#define X_MID "v_cvt_pk_bf16_f32 %2, %8, %9\n\tv_pk_add_u16 %3, %2, %12\n\t"
#define X_END "\n\tv_max3_u16 %4, %4, %10, %11"
  if constexpr (F) asm volatile(MF_ "%0, %5, %6, %13\n\t" X_MID MF_ "%1, %5, %7, %13" X_END
                                : "=&v"(c0), "=&v"(c1), "=&v"(w), "=&v"(z), "+v"(mx)
                                : "v"(b), "v"(x0), "v"(x1), "v"(e0), "v"(e1), "v"(a0), "v"(a1), "s"(kb), "v"(bs));
  else asm volatile(MF_ "%0, %5, %6, %0\n\t" X_MID MF_ "%1, %5, %7, %1" X_END
                    : "+v"(c0), "+v"(c1), "=&v"(w), "=&v"(z), "+v"(mx)
                    : "v"(b), "v"(x0), "v"(x1), "v"(e0), "v"(e1), "v"(a0), "v"(a1), "s"(kb));
}
template <bool F> __device__ __forceinline__ void step_y(f4& c0, f4& c1, const u32x4& b, const u32x4& x0, const u32x4& x1, const f4& bs,
                                                         u32& a0, u32& a1, u32& r0, u32& r1, u32 z) {
#define Y_MID "v_lshlrev_b32_sdwa %2, 1, %9" SDWA_(0) "v_lshlrev_b32_sdwa %3, 1, %9" SDWA_(1)
#define Y_END "\n\tds_read_u16 %4, %2\n\tds_read_u16 %5, %3"
  if constexpr (F) asm volatile(MF_ "%0, %6, %7, %10\n\t" Y_MID MF_ "%1, %6, %8, %10" Y_END
                                : "=&v"(c0), "=&v"(c1), "=&v"(a0), "=&v"(a1), "=&v"(r0), "=&v"(r1)
                                : "v"(b), "v"(x0), "v"(x1), "v"(z), "v"(bs) : "memory");
  else asm volatile(MF_ "%0, %6, %7, %0\n\t" Y_MID MF_ "%1, %6, %8, %1" Y_END
                    : "+v"(c0), "+v"(c1), "=&v"(a0), "=&v"(a1), "=&v"(r0), "=&v"(r1)
                    : "v"(b), "v"(x0), "v"(x1), "v"(z) : "memory");
}
__device__ __forceinline__ void comb(u32& r, u32 hi) { asm volatile("v_lshl_or_b32 %0, %1, 16, %0" : "+v"(r) : "v"(hi)); }
__device__ __forceinline__ void max3u(u32& mx, u32 a0, u32 a1) { asm volatile("v_max3_u16 %0, %0, %1, %2" : "+v"(mx) : "v"(a0), "v"(a1)); }
__device__ __forceinline__ void epi_word(u32& w, u32& r0, u32& r1, u32& mx, float e0, float e1, u32 kb) {
  u32 z;
  asm volatile("v_cvt_pk_bf16_f32 %0, %5, %6\n\tv_pk_add_u16 %4, %0, %7\n\t"
               "v_lshlrev_b32_sdwa %1, 1, %4" SDWA_(0) "v_lshlrev_b32_sdwa %2, 1, %4" SDWA_(1)
               "v_max3_u16 %3, %3, %1, %2\n\tds_read_u16 %1, %1\n\tds_read_u16 %2, %2"
               : "=&v"(w), "=&v"(r0), "=&v"(r1), "+v"(mx), "=&v"(z) : "v"(e0), "v"(e1), "s"(kb) : "memory");
}
__device__ __forceinline__ void mfma_c(f4& acc, const u32x4& a, const u32x4& b, const f4& c) {
  asm volatile("v_mfma_f32_16x16x32_bf16 %0, %1, %2, %3" : "=&v"(acc) : "v"(a), "v"(b), "v"(c));
}
__device__ __forceinline__ void mfma_a(f4& acc, const u32x4& a, const u32x4& b) { MFMA_BF(acc, a, b); }
template <int OFF, class V> __device__ __forceinline__ void dsr128(V& d, u32 addr) { DSR128(d, addr, OFF); }
__device__ __forceinline__ void pin2(u32x4& a, u32x4& b) { asm volatile("" : "+v"(a), "+v"(b)); }
template <int N> __device__ __forceinline__ void wait_res(u32 (&a)[4], u32 (&b)[4]) {
  asm volatile("s_waitcnt lgkmcnt(%8)"
               : "+v"(a[0]), "+v"(a[1]), "+v"(a[2]), "+v"(a[3]), "+v"(b[0]), "+v"(b[1]), "+v"(b[2]), "+v"(b[3])
               : "n"(N > 15 ? 15 : N) : "memory");
}
template <int OFF> __device__ __forceinline__ void glds_o(u32 voff, const void* sbase, u32 m) {
  asm volatile("s_mov_b32 m0, %2\n\ts_nop 0\n\tglobal_load_lds_dwordx4 %0, %1 offset:%3"
               :: "v"(voff), "s"(sbase), "s"(m), "n"(OFF) : "memory", "m0");
}
__device__ __forceinline__ void gload4(u32x4& d, u32 voff, const void* sbase) { GLOAD4(d, voff, sbase); }
__device__ __forceinline__ void st16s(u32 voff, const void* sbase, u32x4 v) {
  asm volatile("global_store_dwordx4 %0, %1, %2" STF :: "v"(voff), "v"(v), "s"(sbase) : "memory");
}

template <int KID>
__device__ __forceinline__ void ssm_block(char* shm, const int grp, const int e0, const int nch, const u16* __restrict__ xnP,
                                          const u16* __restrict__ Pg, u16* __restrict__ yT, u32* flag) {
    const int t = threadIdx.x, lane = t & 63;
    const int wave = __builtin_amdgcn_readfirstlane(t >> 6);
    STAMPB(KID, (char*)yT - (13u << 20));
    STAMP(0);
    const int eo = (e0 & ~1) > 4 ? 4 : (e0 & ~1);
    const int dbase = (grp >> 2) * 32 + (grp & 3) * 4;
    u16* Kc = (u16*)shm;
    float* red = (float*)(shm + 16896);
#pragma unroll
    for (int c = 0; c < 3; ++c) {
      if (c < nch) {
        const int e = e0 + c;
        const int d = dbase + (e >> 2) * 16 + (e & 3);
        char* buf = shm + 41472 + c * 6144;
        if (wave == 0) glds16s(lane * 16, (const char*)(Pg + (size_t)d * PPITCH), buf);
        if (wave == 1 && lane < 32) glds16s(lane * 16, (const char*)(Pg + (size_t)d * PPITCH) + 1024, buf + 1024);
      }
    }
    {
      u32x2 xv[8];
      const char* xp = (const char*)xnP + ((((size_t)(t >> 3) * 20 + (grp >> 2)) * 64 + (grp & 3) * 16 + ((2 * t) & 15)) * 16 + eo * 2);
#pragma unroll
      for (int i = 0; i < 4; ++i) {
        xv[2 * i] = *(const u32x2*)(xp + (size_t)i * 655360);
        xv[2 * i + 1] = *(const u32x2*)(xp + (size_t)i * 655360 + 16);
      }
#pragma unroll
      for (int c = 0; c < 3; ++c) {
        if (c < nch) {
          const u32 pos2 = 2 * (e0 + c - eo);
          const u32 sel = 0x00000c0cu | (pos2 << 16) | ((pos2 + 1) << 24);
          u16* xs = (u16*)(shm + 41472 + c * 6144 + 2048);
#pragma unroll
          for (int i = 0; i < 4; ++i) {
            const float x0 = __uint_as_float(__builtin_amdgcn_perm(xv[2 * i][1], xv[2 * i][0], sel));
            const float x1 = __uint_as_float(__builtin_amdgcn_perm(xv[2 * i + 1][1], xv[2 * i + 1][0], sel));
            *(u32*)&xs[2 * t + 512 * i] = pk_h(x0, x1);
          }
        }
      }
    }
    if (t < 96) *(u32x4*)(shm + 41472 + (t >> 5) * 6144 + 1536 + (t & 31) * 16) = (u32x4){0u, 0u, 0u, 0u};
    if (t < 8) *(u32x4*)&Kc[(t >> 1) * KCPITCH + (256 + (t & 1)) * 8] = (u32x4){0u, 0u, 0u, 0u};
    WAIT_V(0);
    RAW_BARRIER();
    STAMP(3);
    const int b = lane & 15, g = lane >> 4;
    const int m = 255 - t;
    const u32 lds0 = (u32)(size_t)(const __attribute__((address_space(3))) char*)shm;
    const int abase = ((3 - b) & 3) * KCPITCH + 2028 - 4 * (b >> 2) + 8 * g;
    const int bbase = 256 - 16 + 16 * b + 8 * g;
#pragma unroll 1
    for (int c = 0; c < nch; ++c) {
      const int e = e0 + c;
      const int d = dbase + (e >> 2) * 16 + (e & 3);
      const u16* P = (const u16*)(shm + 41472 + c * 6144);
      const float kscale = *(const float*)&P[704];
      {
        u32 wv[6];
        if (wave < 2) {
          const int i = 512 + m - 128;
          const u32 pc = P[i - 1], pa = P[i], pb = P[i + 1];
          const u32 aa = pa | (pa << 16);
          wv[0] = pb | (pb << 16);
          wv[1] = pb | (((m & 1) ? pb : pa) << 16);
          wv[2] = aa; wv[3] = aa; wv[4] = aa;
          wv[5] = m == 128 ? pc : pa;
        } else if (wave == 2) {
          const int i = 384 + 2 * (m - 64);
          const u32 q0 = *(const u32*)&P[i - 2], q1 = *(const u32*)&P[i], q2 = *(const u32*)&P[i + 2];
          wv[0] = __builtin_amdgcn_perm(q2, q2, 0x01000100u);
          wv[1] = __builtin_amdgcn_perm(q1, q1, 0x03020302u);
          wv[2] = __builtin_amdgcn_perm(q1, q1, 0x01000302u);
          wv[3] = __builtin_amdgcn_perm(q1, q1, 0x01000100u);
          wv[4] = m == 64 ? __builtin_amdgcn_perm(q0, q1, 0x07060100u) : wv[3];
          wv[5] = m == 64 ? q0 : (q0 >> 16);
        } else {
#pragma unroll
          for (int hh = 0; hh < 2; ++hh) {
            const int mm = m - hh;
            const int sg = mm >= 32 ? 1 : 0;
#pragma unroll
            for (int j = 0; j < (hh ? 2 : 4); ++j) {
              u32 pk = 0;
#pragma unroll
              for (int h = 0; h < 2; ++h) {
                if (hh == 0 || 2 * j + h < 3) {
                  int q = 8 * mm + 7 - (2 * j + h);
                  q = q < 0 ? 0 : q;
                  int idx = sg ? 128 + ((q + ((q >> 1) & 1)) >> 1) : q;
                  pk |= ((u32)P[idx]) << (16 * h);
                }
              }
              wv[hh * 4 + j] = (hh && mm < 0) ? 0u : pk;
            }
          }
        }
        *(u32x4*)&Kc[0 * KCPITCH + t * 8] = (u32x4){wv[0], wv[1], wv[2], wv[3]};
        *(u32x4*)&Kc[1 * KCPITCH + t * 8] = (u32x4){__builtin_amdgcn_alignbit(wv[1], wv[0], 16), __builtin_amdgcn_alignbit(wv[2], wv[1], 16),
                                                    __builtin_amdgcn_alignbit(wv[3], wv[2], 16), __builtin_amdgcn_alignbit(wv[4], wv[3], 16)};
        *(u32x4*)&Kc[2 * KCPITCH + t * 8] = (u32x4){wv[1], wv[2], wv[3], wv[4]};
        *(u32x4*)&Kc[3 * KCPITCH + t * 8] = (u32x4){__builtin_amdgcn_alignbit(wv[2], wv[1], 16), __builtin_amdgcn_alignbit(wv[3], wv[2], 16),
                                                    __builtin_amdgcn_alignbit(wv[4], wv[3], 16), __builtin_amdgcn_alignbit(wv[5], wv[4], 16)};
      }
      __syncthreads();
      if (c == 0) { STAMP(4); }
      if (c == 2) { STAMP(9); }
      f4 acc[8];
#pragma unroll
      for (int i = 0; i < 8; ++i) acc[i] = (f4){0.f, 0.f, 0.f, 0.f};
      {
        u32x2 a0[2][8], a1[2][8];
        u32x4 bq[2][8];
        const u32 aaddr = lds0 + (u32)(abase - 64 * wave - 32 - 256 * 7) * 2;
        const u32 baddr = lds0 + 41472 + c * 6144 + 1536 + (u32)(bbase - 64 * wave - 32) * 2;
#pragma unroll
        for (int rr = 0; rr < 2; ++rr) {
#pragma unroll
          for (int u = 0; u < 8; ++u) {
            DSR64(a0[rr][u], aaddr, (7 - u) * 512 + (1 - rr) * 64);
            DSR64(a1[rr][u], aaddr, (7 - u) * 512 + (1 - rr) * 64 + 8);
            DSR128(bq[rr][u], baddr, u * 512 + (1 - rr) * 64);
            if (rr == 1 && u == 3) {
              asm volatile("s_waitcnt lgkmcnt(12)"
                           : "+v"(a0[0][0]), "+v"(a1[0][0]), "+v"(a0[0][1]), "+v"(a1[0][1]), "+v"(a0[0][2]), "+v"(a1[0][2]),
                             "+v"(a0[0][3]), "+v"(a1[0][3]), "+v"(a0[0][4]), "+v"(a1[0][4]), "+v"(a0[0][5]), "+v"(a1[0][5]),
                             "+v"(a0[0][6]), "+v"(a1[0][6]), "+v"(a0[0][7]), "+v"(a1[0][7]),
                             "+v"(bq[0][0]), "+v"(bq[0][1]), "+v"(bq[0][2]), "+v"(bq[0][3]),
                             "+v"(bq[0][4]), "+v"(bq[0][5]), "+v"(bq[0][6]), "+v"(bq[0][7])
                           :: "memory");
            }
          }
        }
        SB0();
#pragma unroll
        for (int rr = 0; rr < 2; ++rr) {
          if (rr == 1) {
            asm volatile("s_waitcnt lgkmcnt(0)"
                         : "+v"(a0[1][0]), "+v"(a1[1][0]), "+v"(a0[1][1]), "+v"(a1[1][1]), "+v"(a0[1][2]), "+v"(a1[1][2]),
                           "+v"(a0[1][3]), "+v"(a1[1][3]), "+v"(a0[1][4]), "+v"(a1[1][4]), "+v"(a0[1][5]), "+v"(a1[1][5]),
                           "+v"(a0[1][6]), "+v"(a1[1][6]), "+v"(a0[1][7]), "+v"(a1[1][7]),
                           "+v"(bq[1][0]), "+v"(bq[1][1]), "+v"(bq[1][2]), "+v"(bq[1][3]),
                           "+v"(bq[1][4]), "+v"(bq[1][5]), "+v"(bq[1][6]), "+v"(bq[1][7])
                         :: "memory");
          }
#pragma unroll
          for (int u = 0; u < 8; ++u) {
            const h8 A = __builtin_bit_cast(h8, (u32x4){a0[rr][u][0], a0[rr][u][1], a1[rr][u][0], a1[rr][u][1]});
#pragma unroll
            for (int v = 0; v + u < 8; ++v)
              acc[u + v] = __builtin_amdgcn_mfma_f32_16x16x32_f16(A, __builtin_bit_cast(h8, bq[rr][v]), acc[u + v], 0, 0, 0);
          }
          SB0();
        }
      }
      if (c == 0) { STAMP(5); }
      if (c == 2) { STAMP(10); }
      auto red_phase = [&](auto WC) {
        constexpr int W = decltype(WC)::value;
#pragma unroll
        for (int o = 1; o < 4; ++o) {
          const int ow = (W + o) & 3;
#pragma unroll
          for (int i = 0; i < 2; ++i) *(f4*)&red[(((ow * 3 + (o - 1)) * 2 + i) * 64 + lane) * 4] = acc[ow * 2 + i];
        }
        __syncthreads();
        u32x2 o[2];
#pragma unroll
        for (int i = 0; i < 2; ++i) {
          f4 s1 = *(const f4*)&red[(((W * 3 + 0) * 2 + i) * 64 + lane) * 4];
          f4 s2 = *(const f4*)&red[(((W * 3 + 1) * 2 + i) * 64 + lane) * 4];
          f4 s3 = *(const f4*)&red[(((W * 3 + 2) * 2 + i) * 64 + lane) * 4];
          f4 sum = ((acc[W * 2 + i] + s1) + (s2 + s3)) * kscale;
          o[i] = (u32x2){pk_bf(sum[0], sum[1]), pk_bf(sum[2], sum[3])};
        }
#pragma unroll
        for (int i = 0; i < 2; ++i) st8(yT + (size_t)d * SEQ + 256 * (W * 2 + i) + 16 * b + 4 * g, o[i]);
      };
      if (wave == 0) red_phase(IC<0>{});
      else if (wave == 1) red_phase(IC<1>{});
      else if (wave == 2) red_phase(IC<2>{});
      else red_phase(IC<3>{});
      if (c == 0) { STAMP(6); }
      if (c == 1) { STAMP(7); }
      if (c == 2) { STAMP(11); }
    }
    if (flag) {
      WAIT_V(0);
      if (lane == 0) st4(flag + wave * 256, 1u);
    }
    STAMP(8);
    STAMPW();
}

#define K3R 3
#define K3HB 1
#define K3NB 3
#define K3SLEEP1 8
#define K3YS 14
__device__ __forceinline__ void glds16c(const void* gsrc, const void* lp) {
  unsigned keep;
  unsigned lds_dst = __builtin_amdgcn_readfirstlane((unsigned)(size_t)(const __attribute__((address_space(3))) char*)lp);
  asm volatile("s_mov_b32 %0, m0\n\ts_mov_b32 m0, %2\n\ts_nop 0\n\tglobal_load_lds_dwordx4 %1, off sc1\n\ts_mov_b32 m0, %0"
               : "=&s"(keep) : "v"(gsrc), "s"(lds_dst) : "memory");
}
#define OUTF ""
__device__ __forceinline__ void so16(void* p, u32x4 v) { asm volatile("global_store_dwordx4 %0, %1, off" OUTF :: "v"(p), "v"(v) : "memory"); }
__device__ __forceinline__ void so8(void* p, u32x2 v) { asm volatile("global_store_dwordx2 %0, %1, off" OUTF :: "v"(p), "v"(v) : "memory"); }
__device__ __forceinline__ void g2_block(char* shm, const int bid, char* ws, const u16* __restrict__ xin,
                                         const u16* __restrict__ dskip, u16* __restrict__ out) {
  constexpr int R = K3R;
  constexpr int NB = K3NB;
  constexpr int NF = 12, NST = 15;
  const u16* w2F = (const u16*)(ws + WS_W2F);
  const u16* w2H = (const u16*)(ws + WS_W2H);
  const float2* stats = (const float2*)(ws + WS_STATS);
  const u16* yT = (const u16*)(ws + WS_YT);
  const u32* flags = (const u32*)(ws + WS_FLAGS);
  const int t = threadIdx.x, lane = t & 63;
  const int wave = __builtin_amdgcn_readfirstlane(t >> 6);
  const int xcd = bid & 7, idx = bid >> 3;
  const int mi = idx & 7, ni = idx >> 3;
  const int bm = (xcd >> 1) * 8 + mi, bn = (xcd & 1) * 4 + ni;
  const int mt0 = bm * 4, m0 = bm * 64, n0 = bn * 80, nt0 = bn * 5;
  STAMPB(3, ws);
  STAMP(0);
  const char* abase = ws + WS_ACTF + ((size_t)mt0 * 48 + wave) * 1024;
  const char* bbase = (const char*)w2F + ((size_t)nt0 * 48 + wave) * 1024;
  const char* ahb = ws + WS_ACTH + ((size_t)(mt0 >> 1) * 12 + wave) * 1024;
  const char* bhb = (const char*)w2H + ((size_t)nt0 * 12 + wave) * 512;
  u32 va[4], vb[5], vh[2];
  const __amdgpu_buffer_rsrc_t ra = __builtin_amdgcn_make_buffer_rsrc((void*)abase, 0, 4 * 48 * 1024, 0x00020000);
  const __amdgpu_buffer_rsrc_t rh = __builtin_amdgcn_make_buffer_rsrc((void*)ahb, 0, 2 * 12 * 1024, 0x00020000);
#define LDA(r, p, v, so) __builtin_bit_cast(u32x4, __builtin_amdgcn_raw_buffer_load_b128(r, v, so, 16))
#pragma unroll
  for (int j = 0; j < 5; ++j) vb[j] = lane * 16 + j * (48 * 1024);
  u32x4 fa[R][4], fb[R][5];
  u32x4 ha[3][2];
  u32x2 hb[3][5];
  auto issue = [&](int s, int what = 3) {
    if (s < NF) {
      const char* ap = abase + s * 4096;
      const char* bp = bbase + s * 4096;
      if (what & 1) {
#pragma unroll
        for (int i = 0; i < 4; ++i) fa[s % R][i] = LDA(ra, ap, va[i], s * 4096);
      }
      if (what & 2) {
        if (s >= R && s < R + NB) {
#pragma unroll
          for (int j = 0; j < 5; ++j) fb[s % R][j] = *(const u32x4*)(shm + 20480 + (((s - R) * 4 + wave) * 5 + j) * 1024 + lane * 16);
        } else {
#pragma unroll
          for (int j = 0; j < 5; ++j) fb[s % R][j] = *(const u32x4*)(bp + vb[j]);
        }
      }
    } else {
      const int h = s - NF;
#pragma unroll
      for (int i = 0; i < 2; ++i) ha[h][i] = LDA(rh, ahb + h * 4096, vh[i], h * 4096);
      if (h >= K3HB) {
#pragma unroll
        for (int j = 0; j < 5; ++j) hb[h][j] = *(const u32x2*)(bhb + h * 2048 + (lane * 8 + j * (12 * 512)));
      }
    }
  };
  auto aux = [&]() {
#pragma unroll
    for (int i = 0; i < 3; ++i) {
      int p = wave + 4 * i;
      if (p > 10) p = 10;
      const int c = p * 64 + lane;
      const u16* src;
      if (p < 10) src = xin + (size_t)(m0 + c / 10) * DM + n0 + (c % 10) * 8;
      else src = (lane < 32) ? (const u16*)(stats + m0) + lane * 8 : dskip + n0 + (lane < 42 ? lane - 32 : 9) * 8;
      GLDS(src, shm + p * 1024);
    }
  };
  auto yload = [&]() {
    const u32* fp = flags + (bm >> 3) * 256 + ((n0 >> 5) * 4) * 3 + (lane < 12 * 3 ? lane : 0);
#define K3SLEEP 16
    if (wave == 0)
    for (int it = 0; it < (1 << 20); ++it) {
      u32 f;
      asm volatile("global_load_dword %0, %1, off sc1\n\ts_waitcnt vmcnt(0)" : "=v"(f) : "v"(fp) : "memory");
      if (__builtin_amdgcn_ballot_w64(f != 1u) == 0) break;
      __builtin_amdgcn_s_sleep(K3SLEEP);
    }
    __builtin_amdgcn_s_barrier();
#pragma unroll
    for (int i = 0; i < 3; ++i) {
      int p = wave + 4 * i;
      if (p > 9) p = 9;
      const int c = p * 64 + lane;
      glds16c(yT + (size_t)(n0 + (c >> 3)) * SEQ + m0 + (c & 7) * 8, shm + (10 + p) * 1024);
    }
  };
  SB0();
  const u32* fp1 = flags + 1024 + bm * 32 + (lane & 31);
  u32 f1 = 0;
  SB0();
#pragma unroll
  for (int s = 0; s < R; ++s) { issue(s, 2); SB0(); }
#pragma unroll
  for (int h = 0; h < K3HB; ++h) {
#pragma unroll
    for (int j = 0; j < 5; ++j) hb[h][j] = *(const u32x2*)(bhb + h * 2048 + (lane * 8 + j * (12 * 512)));
    SB0();
  }
  aux();
  SB0();
  {
    const u32 lb0 = (u32)(size_t)(const __attribute__((address_space(3))) char*)shm + 20480 + wave * 5120;
#pragma unroll
    for (int s = 0; s < NB; ++s)
#pragma unroll
      for (int j = 0; j < 5; ++j) glds_o<0>(vb[j], bbase + (R + s) * 4096, lb0 + s * 20480 + j * 1024);
  }
  SB0();
  if (wave == 0) {
    asm volatile("global_load_dword %0, %1, off sc1\n\ts_waitcnt vmcnt(0)" : "=v"(f1) : "v"(fp1) : "memory");
    for (int it = 0; it < (1 << 20); ++it) {
      if (__builtin_amdgcn_ballot_w64(f1 != 1u) == 0) break;
      __builtin_amdgcn_s_sleep(K3SLEEP1);
      asm volatile("global_load_dword %0, %1, off sc1\n\ts_waitcnt vmcnt(0)" : "=v"(f1) : "v"(fp1) : "memory");
    }
  }
  __builtin_amdgcn_s_barrier();
  {
    u32 l16 = lane * 16;
    asm volatile("" : "+v"(l16) :: "memory");
#pragma unroll
    for (int i = 0; i < 4; ++i) va[i] = l16 + ((wave + i) & 3) * (48 * 1024);
#pragma unroll
    for (int i = 0; i < 2; ++i) vh[i] = l16 + (((wave >> 1) + i) & 1) * (12 * 1024);
  }
  STAMP(10);
  SB0();
#pragma unroll
  for (int s = 0; s < R; ++s) { issue(s, 1); SB0(); }
  STAMP(1);
  const int fr = lane & 15, g = lane >> 4, tq = (lane & 15) >> 2, tp = lane & 3;
  const int ml = wave * 16 + fr;
  u32x2 s1p[5];
  float2 st;
  auto resid = [&](int j) {
    const char* sx = shm;
    const char* sm = shm + 10 * 1024;
    if (j == 0) st = *(const float2*)(sm + ml * 8);
    const int nl = j * 16;
    u32x2 xx = *(const u32x2*)(sx + ml * 160 + (nl + 4 * g) * 2);
    u32x2 dd = *(const u32x2*)(sm + 512 + (nl + 4 * g) * 2);
#pragma unroll
    for (int h = 0; h < 2; ++h) {
      const float x0 = lo_f(xx[h]), x1 = hi_f(xx[h]);
      u32 d = pk_bf(x0 - st.x, x1 - st.x);
      u32 xn = pk_bf(lo_f(d) * st.y, hi_f(d) * st.y);
      s1p[j][h] = pk_bf(lo_f(xn) * lo_f(dd[h]), hi_f(xn) * hi_f(dd[h]));
    }
    asm volatile("" : "+v"(s1p[j]));
  };
  auto resid2 = [&](int j) {
    const char* sx = shm;
    const char* sy = shm + 10 * 1024;
    const int nl = j * 16;
    u32x2 xx = *(const u32x2*)(sx + ml * 160 + (nl + 4 * g) * 2);
    s4 yy = TRREAD(sy + (nl + 4 * g + tq) * 128 + (wave * 16 + 4 * tp) * 2);
    const u32x2 yw = __builtin_bit_cast(u32x2, yy);
#pragma unroll
    for (int h = 0; h < 2; ++h) {
      const float x0 = lo_f(xx[h]), x1 = hi_f(xx[h]);
      u32 a = pk_bf(lo_f(yw[h]) + lo_f(s1p[j][h]), hi_f(yw[h]) + hi_f(s1p[j][h]));
      s1p[j][h] = pk_bf(x0 + lo_f(a), x1 + hi_f(a));
    }
  };
  f4 acc[4][5];
#pragma unroll
  for (int i = 0; i < 4; ++i)
#pragma unroll
    for (int j = 0; j < 5; ++j) acc[i][j] = (f4){0.f, 0.f, 0.f, 0.f};
  const bool odd = wave & 1;
  SB0();
#pragma unroll
  for (int s = 0; s < NST; ++s) {
    if (s < NF) {
#pragma unroll
      for (int i = 0; i < 4; ++i)
#pragma unroll
        for (int j = 0; j < 5; ++j)
          acc[i][j] = __builtin_amdgcn_mfma_f32_16x16x32_bf16(__builtin_bit_cast(s8, fb[s % R][j]),
                                                              __builtin_bit_cast(s8, fa[s % R][i]), acc[i][j], 0, 0, 0);
    } else {
      const int h = s - NF;
      u32x2 af[4];
      af[0] = odd ? (u32x2){ha[h][0][2], ha[h][0][3]} : (u32x2){ha[h][0][0], ha[h][0][1]};
      af[1] = odd ? (u32x2){ha[h][1][0], ha[h][1][1]} : (u32x2){ha[h][0][2], ha[h][0][3]};
      af[2] = odd ? (u32x2){ha[h][1][2], ha[h][1][3]} : (u32x2){ha[h][1][0], ha[h][1][1]};
      af[3] = odd ? (u32x2){ha[h][0][0], ha[h][0][1]} : (u32x2){ha[h][1][2], ha[h][1][3]};
#pragma unroll
      for (int i = 0; i < 4; ++i)
#pragma unroll
        for (int j = 0; j < 5; ++j)
          acc[i][j] = __builtin_amdgcn_mfma_f32_16x16x16bf16_1k(__builtin_bit_cast(s4, hb[h][j]),
                                                               __builtin_bit_cast(s4, af[i]), acc[i][j], 0, 0, 0);
    }
    SB0();
    if (s + R < NST) issue(s + R);
    SB0();
    if (s == 4) {
      WAIT_V(R * 9 > 60 ? 60 : R * 9);
      __builtin_amdgcn_s_barrier();
      SB0();
    }
    if (s >= 5 && s < 10) { resid(s - 5); SB0(); }
    if (s == 10) { __builtin_amdgcn_s_barrier(); SB0(); }
    if (s == K3YS) { yload(); SB0(); STAMP(3); }
    if (s == 0) { STAMP(2); }
    if (s == 2) { STAMP(4); }
    if (s == 6) { STAMP(5); }
    if (s == 10) { STAMP(6); }
  }
  STAMP(7);
  char* red = shm + 20 * 1024;
#pragma unroll
  for (int i = 1; i < 4; ++i)
#pragma unroll
    for (int j = 0; j < 5; ++j)
      *(f4*)(red + ((((wave + i) & 3) * 3 + (i - 1)) * 5 + j) * 1024 + lane * 16) = acc[i][j];
  WAIT_V(0);
  __syncthreads();
  STAMP(9);
  u32x2 ov[5];
#pragma unroll
  for (int j = 0; j < 5; ++j) {
    resid2(j);
    f4 p0 = *(const f4*)(red + ((wave * 3 + 0) * 5 + j) * 1024 + lane * 16);
    f4 p1 = *(const f4*)(red + ((wave * 3 + 1) * 5 + j) * 1024 + lane * 16);
    f4 p2 = *(const f4*)(red + ((wave * 3 + 2) * 5 + j) * 1024 + lane * 16);
    f4 sum = (acc[0][j] + p0) + (p1 + p2);
    u32 m0p = pk_bf(sum[0], sum[1]), m1p = pk_bf(sum[2], sum[3]);
    ov[j] = (u32x2){pk_bf(lo_f(s1p[j][0]) + lo_f(m0p), hi_f(s1p[j][0]) + hi_f(m0p)),
                    pk_bf(lo_f(s1p[j][1]) + lo_f(m1p), hi_f(s1p[j][1]) + hi_f(m1p))};
  }
  u16* orow = out + (size_t)(m0 + ml) * DM + n0;
#pragma unroll
  for (int jp = 0; jp < 2; ++jp) {
    auto r0 = __builtin_amdgcn_permlane16_swap(ov[2 * jp][0], ov[2 * jp + 1][0], false, false);
    auto r1 = __builtin_amdgcn_permlane16_swap(ov[2 * jp][1], ov[2 * jp + 1][1], false, false);
    so16(orow + (2 * jp + (g & 1)) * 16 + 8 * (g >> 1), (u32x4){r0[0], r1[0], r0[1], r1[1]});
  }
  so8(orow + 64 + 4 * g, ov[4]);
  STAMP(8);
  STAMPW();
}

extern "C" __global__ __launch_bounds__(256, 2) void k2_kernel(char* ws, const u16* __restrict__ xin,
                                                    const u16* __restrict__ dskip, u16* __restrict__ out) {
  const u16* xnP = (const u16*)(ws + WS_XNP);
  const u16* w1P = (const u16*)(ws + WS_W1P);
  const float* b1f = (const float*)(ws + WS_B1F);
  const u16* lut = (const u16*)(ws + WS_LUT);
  u16* actF = (u16*)(ws + WS_ACTF);
  u16* actH = (u16*)(ws + WS_ACTH);
  u32* flags = (u32*)(ws + WS_FLAGS);
  __shared__ __attribute__((aligned(1024))) char shm[81920];
  const int t = threadIdx.x, lane = t & 63;
  const int wave = __builtin_amdgcn_readfirstlane(t >> 6);
  const int vb = blockIdx.x;
  if (vb >= K2_G1) {
    const int sb = vb - K2_G1, li = sb >> 3, grp = (sb & 7) * 10 + li / 3, part = li % 3;
    ssm_block<2>(shm, grp, 3 * part, part < 2 ? 3 : 2, xnP, (const u16*)(ws + WS_PG), (u16*)(ws + WS_YT), flags + grp * 3 + part);
    return;
  }
  const int xcd = vb & 7, idx = vb >> 3;
  const int mi = idx & 3, ni = idx >> 2;
  const int bm = (xcd >> 1) * 4 + mi, bn = (xcd & 1) * 8 + ni;
  STAMPB(2, (char*)actF - (16u << 20));
  STAMP(0);
  const int mtw = bm * 8 + 2 * wave, nt0 = 27 * (bn >> 2) + 7 * (bn & 3);
  const char* abase = (const char*)xnP + (size_t)mtw * (20 * 1024);
  const u32 va0 = lane * 16, va1 = lane * 16 + 20 * 1024;
  const u32 lds0 = (u32)(size_t)(const __attribute__((address_space(3))) char*)shm;
  const u32 va4 = lane * 16 + 4096;
  const char* bb1 = (const char*)w1P + (size_t)((nt0 + wave) * 20) * 1024;
  const char* bb2 = (const char*)w1P + (size_t)(nt0 * 20 + 5 * wave) * 1024;
  const u32 mc0 = lds0 + 8192 + wave * 5120;
  const u32 mc2 = wave == 3 ? lds0 + 73728 : mc0 + 40960;
  auto stageB = [&](auto GC, auto IC_) {
    constexpr int gq = decltype(GC)::value, i = decltype(IC_)::value;
    const char* src = gq < NT1 ? bb1 + gq * 5120 : bb2 + (size_t)((gq == 6 && bn == 15) ? 5 : gq) * 20480;
    const u32 m = gq % 3 == 2 ? mc2 : mc0 + (gq % 3) * 20480;
    if constexpr (i < 4) glds_o<i * 1024>(va0, src, m);
    else glds_o<0>(va4, src, m + 4096);
  };
  u32x4 fa[20][2];
  auto loadA = [&](int s) {
    const char* ap = abase + s * 1024;
    GLOAD4(fa[s][0], va0, ap);
    GLOAD4(fa[s][1], va1, ap);
  };
  glds_o<0>((lane < 28 ? lane : 27) * 16, (const char*)(b1f + nt0 * 16), lds0 + BIAS_OFF);
  static_for<5>([&](auto I) { stageB(IC<0>{}, I); });
#pragma unroll
  for (int s = 0; s < 5; ++s) loadA(s);
  static_for<5>([&](auto I) { stageB(IC<1>{}, I); });
#pragma unroll
  for (int s = 5; s < 10; ++s) loadA(s);
  static_for<5>([&](auto I) { stageB(IC<2>{}, I); });
  STAMP(1);
  WAIT_V(30);
  __builtin_amdgcn_s_barrier();
  STAMP(2);
  typedef unsigned short us2 __attribute__((ext_vector_type(2)));
  const u32 lb = lds0 + lane * 16, lbh = lds0 + lane * 16 + 65536, lbb = lds0 + BIAS_OFF + (lane >> 4) * 16;
  f4 acc[7][2];
  u32x4 fb[G1_D + 1];
  f4 bias[7];
  u32 hw[7][4];
  u32 res[7][4], rhi[7][4];
  u32 mx[4] = {0u, 0u, 0u, 0u}, zz, a0 = 0, a1 = 0;
  auto readB = [&](auto PC) {
    constexpr int q = decltype(PC)::value, off = g1_piece_lds(q);
    if constexpr (off < 65536) dsr128<off>(fb[q % (G1_D + 1)], lb);
    else dsr128<off - 65536>(fb[q % (G1_D + 1)], lbh);
  };
  const u32 kb = (0x10000u - (LUT_E0 << 7)) * 0x10001u;
  auto epi_fix = [&](int T) __attribute__((always_inline)) {
#pragma unroll
    for (int q = 0; q < 4; ++q) {
      const u32 z0 = hw[T][q] & 0x7FFFu, z1 = (hw[T][q] >> 16) & 0x7FFFu;
      const bool b0 = (z0 - (LUT_E0 << 7)) >= (u32)LUT_HALF, b1v = (z1 - (LUT_E0 << 7)) >= (u32)LUT_HALF;
      const float h0 = lo_f(hw[T][q]), h1 = hi_f(hw[T][q]);
      const float f0 = (z0 < (LUT_E0 << 7)) ? h0 * 0.59765625f : (h0 > 0.f ? h0 : h0 * 0.0f);
      const float f1 = (z1 < (LUT_E0 << 7)) ? h1 * 0.59765625f : (h1 > 0.f ? h1 : h1 * 0.0f);
      const u32 fw = pk_bf(f0, f1);
      res[T][q] = ((b0 ? fw : res[T][q]) & 0xFFFFu) | ((b1v ? fw : res[T][q]) & 0xFFFF0000u);
    }
  };
  const char* obase = (const char*)actF + ((size_t)mtw * 48 + 3 * bn) * 1024;
#pragma unroll
  for (int T = 0; T < NT1; ++T) DSR128(bias[T], lbb, T * 64);
  static_for<G1_D>(readB);
  static_for<140>([&](auto PC) {
    constexpr int p = decltype(PC)::value;
    constexpr int T = g1_tile(p), kt = g1_kt(p), gq = p / 20, lp = p % 20;
    if constexpr (G1.vS[p] >= 0) {
      WAIT_L(0);
      WAIT_V(G1.vS[p]);
      __builtin_amdgcn_s_barrier();
      if (gq == 0) { STAMP(3); }
      if (gq == 1) { STAMP(4); }
      if (gq == 3) { STAMP(5); }
      if (gq == 5) { STAMP(6); }
    }
    static_for<3>([&](auto RC) {
      constexpr int r = decltype(RC)::value;
      if constexpr (p == G1.sp[r]) {
        if constexpr (!(r < 2 && G1.ws[r < 2 ? 8 * r + 8 : 0] < p)) max3u(mx[r], a0, a1);
        if (__builtin_expect(__builtin_amdgcn_ballot_w64((mx[r] & 0xFFFFu) >= 2u * LUT_HALF) != 0, 0)) {
          epi_fix(2 * r);
          epi_fix(2 * r + 1);
        }
        SB0();
#pragma unroll
        for (int i = 0; i < 2; ++i)
          st16s(va0, obase + (i * 48 + r) * 1024,
                (u32x4){res[2 * r][2 * i], res[2 * r][2 * i + 1], res[2 * r + 1][2 * i], res[2 * r + 1][2 * i + 1]});
      }
    });
    if constexpr (G1.vA[p] >= 0) {
      WAIT_V(G1.vA[p]);
      pin2(fa[kt][0], fa[kt][1]);
    }
    if constexpr (p < 40 && (p & 1) == 0) gload4(fa[10 + p / 4][(p >> 1) & 1], (p & 2) ? va1 : va0, abase + (10 + p / 4) * 1024);
    if constexpr (g1_vmB(p) >= 0) stageB(IC<3 + g1_vmB(p) / 5>{}, IC<g1_vmB(p) % 5>{});
    if constexpr (g1_vmL(p) >= 0) {
      glds_o<g1_vmL(p) * 1024>(va0, (const char*)lut + wave * 4096, lds0 + (wave & 1) * 4096 + (wave >> 1) * LUT_NEG);
    }
    if constexpr (p + G1_D < 140) readB(IC<p + G1_D>{});
    if constexpr (lp == 12 && gq + 1 >= NT1 && gq < 6) dsr128<(gq + 1) * 64>(bias[gq + 1], lbb);
    WAIT_L(G1.lB[p]);
    SB0();
    constexpr int w = g1_word(p), wT = w >= 0 ? w >> 2 : 0, wq = w & 3;
    if constexpr (w < 0) step_p<kt == 0>(acc[T][0], acc[T][1], fb[p % (G1_D + 1)], fa[kt][0], fa[kt][1], bias[T]);
    else if constexpr (p == G1.ws[w])
      step_x<kt == 0>(acc[T][0], acc[T][1], fb[p % (G1_D + 1)], fa[kt][0], fa[kt][1], bias[T], hw[wT][wq], zz, mx[w > 0 ? (w - 1) >> 3 : 0],
                      acc[wT][wq >> 1][2 * (wq & 1)], acc[wT][wq >> 1][2 * (wq & 1) + 1], a0, a1, kb);
    else step_y<kt == 0>(acc[T][0], acc[T][1], fb[p % (G1_D + 1)], fa[kt][0], fa[kt][1], bias[T], a0, a1, res[wT][wq], rhi[wT][wq], zz);
    constexpr int cw = g1_comb(p);
    if constexpr (cw >= 0) comb(res[cw >> 2][cw & 3], rhi[cw >> 2][cw & 3]);
    SB0();
  });
  STAMP(7);
  asm volatile("s_nop 7\n\ts_nop 7\n\ts_nop 7" : "+v"(acc[6][0]), "+v"(acc[6][1]) :: "memory");
  if ((bn & 3) != 3) {
#pragma unroll
    for (int q = 0; q < 4; ++q) epi_word(hw[6][q], res[6][q], rhi[6][q], mx[3], acc[6][q >> 1][2 * (q & 1)], acc[6][q >> 1][2 * (q & 1) + 1], kb);
    asm volatile("s_waitcnt lgkmcnt(0)" : "+v"(res[6][0]), "+v"(res[6][1]), "+v"(res[6][2]), "+v"(res[6][3]),
                 "+v"(rhi[6][0]), "+v"(rhi[6][1]), "+v"(rhi[6][2]), "+v"(rhi[6][3]) :: "memory");
#pragma unroll
    for (int q = 0; q < 4; ++q) res[6][q] |= rhi[6][q] << 16;
    if (__builtin_expect(__builtin_amdgcn_ballot_w64((mx[3] & 0xFFFFu) >= 2u * LUT_HALF) != 0, 0)) epi_fix(6);
    st16s(va0, (const char*)actH + ((size_t)(mtw >> 1) * 12 + 3 * (bn >> 2) + (bn & 3)) * 1024,
          (u32x4){res[6][0], res[6][1], res[6][2], res[6][3]});
  }
  STAMP(8);
  WAIT_V(0);
  if (lane == 0) st4(flags + 1024 + (bm * 4 + wave) * 16 + bn, 1u);
  STAMP(11);
  STAMPW();
  RAW_BARRIER();
  g2_block(shm, vb, ws, xin, dskip, out);
}
extern "C" __global__ void k4_end() {}
extern "C" void kernel_launch(void* const* d_in, const int* in_sizes, int n_in,
                              void* d_out, int out_size, void* d_ws, size_t ws_size,
                              hipStream_t stream) {
  const u16* u = (const u16*)d_in[0];
  const u16* lar = (const u16*)d_in[1];
  const u16* aim = (const u16*)d_in[2];
  const u16* cre = (const u16*)d_in[3];
  const u16* cim = (const u16*)d_in[4];
  const u16* logdt = (const u16*)d_in[5];
  const u16* dskip = (const u16*)d_in[6];
  const u16* w1 = (const u16*)d_in[8];
  const u16* b1 = (const u16*)d_in[9];
  const u16* w2 = (const u16*)d_in[10];
  u16* out = (u16*)d_out;
  char* ws = (char*)d_ws;
  if (ws_size < (32u << 20)) return;
  u16* xnP = (u16*)(ws);
  u16* w1P = (u16*)(ws + (4u << 20));
  float2* stats = (float2*)(ws + (12u << 20) + 4096);
  u16* lut = (u16*)(ws + (12u << 20) + 32768);
  float* b1f = (float*)(ws + (12u << 20) + 65536);
  u32* flags = (u32*)(ws + (12u << 20) + 98304);
  u16* yT = (u16*)(ws + (13u << 20));
  u16* actF = (u16*)(ws + (16u << 20));
  u16* actH = (u16*)(ws + (22u << 20));
  u16* w2F = (u16*)(ws + (24u << 20));
  u16* w2H = (u16*)(ws + (26u << 20));
  u16* Pg = (u16*)(ws + (28u << 20));

  k1_kernel<<<K1_LN + K1_KG + K1_RP + K1_RQ + K1_LUT, 256, 0, stream>>>(u, xnP, stats, lut, w1, w1P, w2, w2F, w2H, b1, b1f,
                                                                        lar, aim, cre, cim, logdt, Pg, flags);
  k2_kernel<<<K2_G1 + K2_SSM, 256, 0, stream>>>(ws, u, dskip, out);
}
```

```cpp
#include <hip/hip_runtime.h>
#include <hip/hip_bf16.h>

#define SEQ 2048
#define DM 640
#define NS 128
#define FF 1728

typedef unsigned short u16;
typedef short s4 __attribute__((ext_vector_type(4)));
typedef short s8 __attribute__((ext_vector_type(8)));
typedef _Float16 h8 __attribute__((ext_vector_type(8)));
typedef float f4 __attribute__((ext_vector_type(4)));
typedef unsigned int u32;
typedef unsigned int u32x4 __attribute__((ext_vector_type(4)));
typedef unsigned int u32x2 __attribute__((ext_vector_type(2)));

__device__ __forceinline__ float bf2f(u16 v) { return __uint_as_float(((u32)v) << 16); }
__device__ __forceinline__ u16 f2bf(float f) {
  u32 u = __float_as_uint(f);
  u32 r = (u + 0x7FFFu + ((u >> 16) & 1u)) >> 16;
  return (u16)r;
}
__device__ __forceinline__ float bfr(float f) { return bf2f(f2bf(f)); }

#define STAMPB(kid, base)
#define STAMP(k)
#define STAMPW()
typedef __bf16 bf2_t __attribute__((ext_vector_type(2)));
typedef float f2_t __attribute__((ext_vector_type(2)));
__device__ __forceinline__ u32 pk_bf(float a, float b) {
  f2_t v = {a, b};
  bf2_t r = __builtin_convertvector(v, bf2_t);
  return __builtin_bit_cast(u32, r);
}
__device__ __forceinline__ float lo_f(u32 w) { return __uint_as_float(w << 16); }
__device__ __forceinline__ float hi_f(u32 w) { return __uint_as_float(w & 0xFFFF0000u); }

#define WAIT_V(n) asm volatile("s_waitcnt vmcnt(%0)" ::"n"(n) : "memory")
#define RAW_BARRIER() do { asm volatile("s_waitcnt lgkmcnt(0)" ::: "memory"); __builtin_amdgcn_s_barrier(); } while (0)
__device__ __forceinline__ void glds16(const void* gsrc, const void* lp) {
  unsigned keep;
  unsigned lds_dst = (unsigned)(size_t)(const __attribute__((address_space(3))) char*)lp;
  asm volatile("s_mov_b32 %0, m0\n\ts_mov_b32 m0, %2\n\ts_nop 0\n\tglobal_load_lds_dwordx4 %1, off\n\ts_mov_b32 m0, %0"
               : "=&s"(keep) : "v"(gsrc), "s"(lds_dst) : "memory");
}
#define GLDS(gp, lp) glds16((const void*)(gp), (const void*)(lp))

#define STF " sc1"
__device__ __forceinline__ void st16(void* p, u32x4 v) { asm volatile("global_store_dwordx4 %0, %1, off" STF :: "v"(p), "v"(v) : "memory"); }
__device__ __forceinline__ void st8(void* p, u32x2 v) { asm volatile("global_store_dwordx2 %0, %1, off" STF :: "v"(p), "v"(v) : "memory"); }
__device__ __forceinline__ void st4(void* p, u32 v) { asm volatile("global_store_dword %0, %1, off" STF :: "v"(p), "v"(v) : "memory"); }
__device__ __forceinline__ void st2(void* p, u32 v) { asm volatile("global_store_short %0, %1, off" STF :: "v"(p), "v"(v) : "memory"); }

#define LUT_E0 102
#define LUT_E1 134
#define LUT_HALF ((LUT_E1 - LUT_E0) * 128)
#define LUT_N (2 * LUT_HALF)

__device__ __forceinline__ float mish_bf(float h) {
  float e = bfr(expf(-fabsf(h)));
  float l = bfr(log1pf(e));
  float sp = bfr(l + (h > 0.f ? h : 0.f));
  float th = bfr(tanhf(sp));
  return bfr(h * th);
}

#define KRLEN 2080
#define WFENCE() do { asm volatile("" ::: "memory"); __builtin_amdgcn_wave_barrier(); asm volatile("" ::: "memory"); } while (0)
__device__ __forceinline__ void cexp_p(float arl2, float w, float p, float& re, float& im) {
  float mag = __builtin_amdgcn_exp2f(arl2 * p);
  float rev = w * p;
  rev = rev - floorf(rev);
  re = mag * __builtin_amdgcn_cosf(rev);
  im = mag * __builtin_amdgcn_sinf(rev);
}
__device__ __forceinline__ void cmul(float ar, float ai, float br, float bi, float& cr, float& ci) {
  float r = fmaf(ar, br, -(ai * bi));
  float i = fmaf(ar, bi, ai * br);
  cr = r; ci = i;
}
__device__ __forceinline__ u32x2 split2(float a, float b) {
  u32 h = pk_bf(a, b);
  u32 l = pk_bf(a - lo_f(h), b - hi_f(h));
  return (u32x2){h, l};
}

#define CODE_TOUCH(sym0, sym1, line, d0) do { \
    unsigned long long pc_; int o0_, o1_; \
    asm volatile("s_getpc_b64 %0\n.Lpc_here%=:\n\ts_mov_b32 %1, " sym0 "-.Lpc_here%=\n\ts_mov_b32 %2, " sym1 "-.Lpc_here%=" \
                 : "=s"(pc_), "=s"(o0_), "=s"(o1_)); \
    int len_ = o1_ - o0_; \
    if (len_ < 4 || len_ > 65536) { len_ = 4; o0_ = 0; } \
    const char* cb_ = (const char*)(pc_ + (long long)o0_); \
    const int oa_ = (line) * 128 < len_ - 4 ? (line) * 128 : len_ - 4; \
    asm volatile("global_load_dword %0, %1, %2" : "=v"(d0) : "v"(oa_ & ~3), "s"(cb_) : "memory"); \
  } while (0)

#define WS_XNP 0u
#define WS_W1P (4u << 20)
#define WS_STATS ((12u << 20) + 4096)
#define WS_LUT ((12u << 20) + 32768)
#define WS_B1F ((12u << 20) + 65536)
#define WS_FLAGS ((12u << 20) + 98304)
#define WS_YT (13u << 20)
#define WS_ACTF (16u << 20)
#define WS_ACTH (22u << 20)
#define WS_W2F (24u << 20)
#define WS_W2H (26u << 20)
#define WS_PG (28u << 20)
#define K1_LN 256
#define K1_KG 640
#define K1_RP 135
#define K1_RQ 160
#define PPITCH 768
#define K1_LUT 32
#define WPITCH 544
typedef _Float16 h2_t __attribute__((ext_vector_type(2)));
__device__ __forceinline__ u32 pk_h(float a, float b) {
  f2_t v = {a, b};
  h2_t r = __builtin_convertvector(v, h2_t);
  return __builtin_bit_cast(u32, r);
}
#define TRREAD(p) __builtin_amdgcn_ds_read_tr16_b64_v4i16((__attribute__((address_space(3))) s4*)(p))

__device__ __forceinline__ void repack_load(const u16* __restrict__ w, int N, int ku, int nu, char* lds, int tl) {
#pragma unroll
  for (int i = 0; i < 2; ++i) {
    int idx = tl + 256 * i, r = idx >> 3, c = idx & 7;
    u32x4 v = *(const u32x4*)(w + (size_t)(ku * 64 + r) * N + nu * 64 + c * 8);
    *(u32x4*)(lds + r * 160 + c * 16) = v;
  }
}
__device__ __forceinline__ void repack_store(u16* __restrict__ wP, int KB, int ku, int nu, const char* lds, int tl) {
  const int wl = tl >> 6, lane = tl & 63, g = lane >> 4, tq = (lane & 15) >> 2, tp = lane & 3;
#pragma unroll
  for (int kb2 = 0; kb2 < 2; ++kb2) {
    const char* p0 = lds + (kb2 * 32 + 4 * g + tq) * 160 + wl * 32 + tp * 8;
    s4 b0 = TRREAD(p0);
    s4 b1 = TRREAD(p0 + 16 * 160);
    s8 bf = {b0[0], b0[1], b0[2], b0[3], b1[0], b1[1], b1[2], b1[3]};
    st16(wP + (((size_t)(nu * 4 + wl) * KB + ku * 2 + kb2) * 64 + lane) * 8, __builtin_bit_cast(u32x4, bf));
  }
}

extern "C" __global__ __launch_bounds__(256) void k1_kernel(const u16* __restrict__ u, u16* __restrict__ xnP,
                                                 float2* __restrict__ stats, u16* __restrict__ lut,
                                                 const u16* __restrict__ w1, u16* __restrict__ w1P,
                                                 const u16* __restrict__ w2, u16* __restrict__ w2F, u16* __restrict__ w2H,
                                                 const u16* __restrict__ b1, float* __restrict__ b1f,
                                                 const u16* __restrict__ lar, const u16* __restrict__ aim,
                                                 const u16* __restrict__ cre, const u16* __restrict__ cim,
                                                 const u16* __restrict__ logdt, u16* __restrict__ Pg, u32* __restrict__ flags,
                                                 const u16* outp, const u16* dskip) {
  __shared__ __attribute__((aligned(16))) char k1s[57 * WPITCH];
  const int t = threadIdx.x, lane = t & 63;
  const int wave = __builtin_amdgcn_readfirstlane(t >> 6);
  STAMPB(0, xnP);
  STAMP(0);
#define CT1 32
  u32 tc = 0;
  if (__builtin_expect(blockIdx.x < K1_LN, 1)) {
    if (t < 96 ? blockIdx.x < CT1 : t < 112) CODE_TOUCH("k1_kernel", "k4_end", t < 96 ? t : (int)(blockIdx.x >> 3) * 16 + t - 16, tc);
  }
  if (blockIdx.x < K1_LN) {
    u16 (*tile)[DM + 8] = (u16 (*)[DM + 8])k1s;
    const int r = wave * 2 + (lane >> 5), q = lane & 31;
    const int rg = blockIdx.x;
    const int row = rg * 8 + r;
    float v[20];
    const u32x2* src = (const u32x2*)(u + (size_t)row * DM);
#pragma unroll
    for (int i = 0; i < 5; ++i) {
      u32x2 w = src[q + 32 * i];
      v[i * 4 + 0] = lo_f(w[0]); v[i * 4 + 1] = hi_f(w[0]);
      v[i * 4 + 2] = lo_f(w[1]); v[i * 4 + 3] = hi_f(w[1]);
    }
    float s = 0.f;
#pragma unroll
    for (int i = 0; i < 20; ++i) s += v[i];
    STAMP(1);
#pragma unroll
    for (int o = 1; o < 32; o <<= 1) s += __shfl_xor(s, o);
    const float mean = s * (1.0f / DM);
    float ss = 0.f;
#pragma unroll
    for (int i = 0; i < 20; ++i) { float d = v[i] - mean; ss += d * d; }
#pragma unroll
    for (int o = 1; o < 32; o <<= 1) ss += __shfl_xor(ss, o);
    const float var = ss * (1.0f / DM);
    const float mb = bfr(mean);
    const float vb = bfr(var);
    const float den = bfr(sqrtf(bfr(vb + 1e-5f)));
    const float rden = 1.0f / den;
    if (q == 0) st8(stats + row, (u32x2){__float_as_uint(mb), __float_as_uint(rden)});
#pragma unroll
    for (int i = 0; i < 5; ++i) {
      u32 d0 = pk_bf(v[i * 4 + 0] - mb, v[i * 4 + 1] - mb);
      u32 d1 = pk_bf(v[i * 4 + 2] - mb, v[i * 4 + 3] - mb);
      u32x2 w = {pk_bf(lo_f(d0) * rden, hi_f(d0) * rden), pk_bf(lo_f(d1) * rden, hi_f(d1) * rden)};
      *(u32x2*)&tile[r][(q + 32 * i) * 4] = w;
    }
    __syncthreads();
    asm volatile("" :: "v"(tc));
    STAMP(2);
    const int mt = rg >> 1, half = rg & 1;
#pragma unroll
    for (int i = 0; i < 3; ++i) {
      int idx = t + 256 * i;
      if (idx < 640) {
        int kb = idx >> 5, g = (idx >> 3) & 3, fr = idx & 7;
        u32x2 a = *(const u32x2*)&tile[fr][kb * 32 + 4 * g];
        u32x2 b = *(const u32x2*)&tile[fr][kb * 32 + 16 + 4 * g];
        u32x4 w = {a[0], a[1], b[0], b[1]};
        st16(xnP + (((size_t)mt * 20 + kb) * 64 + g * 16 + half * 8 + fr) * 8, w);
      }
    }
    STAMP(3);
    STAMPW();
    return;
  }
  if (blockIdx.x < K1_LN + K1_KG) {
    const int d = blockIdx.x - K1_LN;
    const int n = t & 127;
    char* shm = k1s;
    const u16 r_dt = logdt[d];
    const u16 r_ar = lar[d * NS + n], r_ai = aim[d * NS + n];
    const u16 r_cr = cre[d * NS + n], r_ci = cim[d * NS + n];
    const u16 r_cr2 = cre[d * NS + (n ^ 64)], r_ci2 = cim[d * NS + (n ^ 64)];
    const float dt = bfr(expf(bf2f(r_dt)));
    const float ar = -bfr(expf(bf2f(r_ar))) * dt;
    const float ai = bf2f(r_ai) * dt;
    float cr = bf2f(r_cr) * dt, ci = bf2f(r_ci) * dt;
    float bs = (fabsf(cr) + fabsf(ci)) + (fabsf(bf2f(r_cr2) * dt) + fabsf(bf2f(r_ci2) * dt));
#pragma unroll
    for (int o = 1; o < 64; o <<= 1) bs += __shfl_xor(bs, o);
    const float bound = 2.0f * bs;
    int ex = 0;
    if (bound > 0.f && bound < 3.0e38f) { (void)frexpf(bound, &ex); }
    int es = 15 - ex;
    es = es > 100 ? 100 : (es < -100 ? -100 : es);
    const float sc2 = ldexpf(2.0f, es);
    const float kscale = ldexpf(1.0f, -es);
    cr *= sc2; ci *= sc2;
    const float arl2 = ar * 1.4426950408889634f;
    const float w = ai * 0.15915494309189535f;
    STAMP(1);
    char* wr = shm + n * 4;
    float eHr, eHi;
    cexp_p(arl2, w, 128.0f, eHr, eHi);
    if (wave < 2) {
      float e8r, e8i, e4r, e4i, gr, gi;
      cexp_p(arl2, w, 8.0f, e8r, e8i);
      cexp_p(arl2, w, 4.0f, e4r, e4i);
      cexp_p(arl2, w, 512.0f, gr, gi);
      cmul(gr, gi, cr, ci, gr, gi);
      float lr = 1.f, lim = 0.f;
      *(u32*)(wr) = pk_h(1.f, 0.f);
#pragma unroll
      for (int j = 1; j < 16; ++j) {
        cmul(lr, lim, e8r, e8i, lr, lim);
        *(u32*)(wr + j * WPITCH) = pk_h(lr, -lim);
        if (j < 5) {
          float hr, hi;
          *(u32*)(wr + (40 + (j - 1) * 2) * WPITCH) = pk_h(gr, gi);
          cmul(gr, gi, e4r, e4i, hr, hi);
          *(u32*)(wr + (41 + (j - 1) * 2) * WPITCH) = pk_h(hr, hi);
          cmul(gr, gi, eHr, eHi, gr, gi);
        } else if (j < 14) {
          *(u32*)(wr + (48 + j - 5) * WPITCH) = pk_h(gr, gi);
          if (j < 13) cmul(gr, gi, eHr, eHi, gr, gi);
        }
      }
    } else {
      float Rr[8], Ri[8];
      Rr[0] = 1.f; Ri[0] = 0.f;
      cexp_p(arl2, w, 1.0f, Rr[1], Ri[1]);
#pragma unroll
      for (int r = 2; r < 8; ++r) cmul(Rr[r - 1], Ri[r - 1], Rr[1], Ri[1], Rr[r], Ri[r]);
      float gr = cr, gi = ci;
#pragma unroll
      for (int ii = 0; ii < 2; ++ii) {
        *(u32*)(wr + (16 + ii * 8) * WPITCH) = pk_h(gr, gi);
#pragma unroll
        for (int r = 1; r < 8; ++r) {
          float hr, hi;
          cmul(gr, gi, Rr[r], Ri[r], hr, hi);
          *(u32*)(wr + (16 + ii * 8 + r) * WPITCH) = pk_h(hr, hi);
        }
        cmul(gr, gi, eHr, eHi, gr, gi);
      }
#pragma unroll
      for (int ii = 2; ii < 4; ++ii) {
        *(u32*)(wr + (32 + (ii - 2) * 4) * WPITCH) = pk_h(gr, gi);
#pragma unroll
        for (int r = 1; r < 4; ++r) {
          float hr, hi;
          cmul(gr, gi, Rr[2 * r], Ri[2 * r], hr, hi);
          *(u32*)(wr + (32 + (ii - 2) * 4 + r) * WPITCH) = pk_h(hr, hi);
        }
        if (ii < 3) cmul(gr, gi, eHr, eHi, gr, gi);
      }
    }
    __syncthreads();
    STAMP(2);
    const int b = lane & 15, g = lane >> 4;
    u16* P = Pg + (size_t)d * PPITCH;
    if (wave < 3) {
      const int rowa = 16 + 16 * wave + b;
      const char* ra = shm + (rowa > 56 ? 56 : rowa) * WPITCH + g * 16;
      const char* rb = shm + b * WPITCH + g * 16;
      f4 ka = (f4){0.f, 0.f, 0.f, 0.f};
#pragma unroll
      for (int k8 = 0; k8 < 8; ++k8) {
        h8 a = *(const h8*)(ra + k8 * 64);
        h8 bb = *(const h8*)(rb + k8 * 64);
        ka = __builtin_amdgcn_mfma_f32_16x16x32_f16(a, bb, ka, 0, 0, 0);
      }
      const u32 k01 = pk_h(ka[0], ka[1]), k23 = pk_h(ka[2], ka[3]);
      if (wave == 0) {
        st8(&P[(g >> 1) * 128 + (g & 1) * 4 + 8 * b], (u32x2){k01, k23});
      } else if (wave == 1) {
        const int i1 = g < 2 ? 256 + 64 * g + 4 * b : 384 + 64 * (g - 2) + 2 * b;
        st4(&P[i1], k01);
        st4(&P[i1 + (g < 2 ? 2 : 32)], k23);
      } else {
        const int i2 = 512 + 64 * g + b;
        if (g < 3) st2(&P[i2], k01);
        if (g < 2) {
          st2(&P[i2 + 16], k01 >> 16);
          st2(&P[i2 + 32], k23); st2(&P[i2 + 48], k23 >> 16);
        }
      }
    } else if (lane == 0) {
      st4(&P[704], __float_as_uint(kscale));
    }
    STAMP(3);
    STAMPW();
    return;
  }
  if (blockIdx.x < K1_LN + K1_KG + K1_RP) {
    const int u0 = (blockIdx.x - (K1_LN + K1_KG)) * 2;
    repack_load(w1, FF, u0 / 27, u0 % 27, k1s, t);
    repack_load(w1, FF, (u0 + 1) / 27, (u0 + 1) % 27, k1s + 10240, t);
    __syncthreads();
    repack_store(w1P, DM / 32, u0 / 27, u0 % 27, k1s, t);
    repack_store(w1P, DM / 32, (u0 + 1) / 27, (u0 + 1) % 27, k1s + 10240, t);
    STAMP(3);
    STAMPW();
    return;
  }
  if (blockIdx.x < K1_LN + K1_KG + K1_RP + K1_RQ) {
    const int un = blockIdx.x - (K1_LN + K1_KG + K1_RP);
    const int bn = un / 10, nu = un % 10;
    const int f0 = 16 * (27 * (bn >> 2) + 7 * (bn & 3));
    const int rows = (bn & 3) == 3 ? 96 : 112;
    u32x4 v[4];
#pragma unroll
    for (int i = 0; i < 4; ++i) {
      const int r = (t >> 3) + 32 * i;
      v[i] = *(const u32x4*)(w2 + (size_t)(f0 + (r < rows ? r : rows - 1)) * DM + nu * 64 + (t & 7) * 8);
    }
#pragma unroll
    for (int i = 0; i < 4; ++i) *(u32x4*)(k1s + ((t >> 3) + 32 * i) * 160 + (t & 7) * 16) = v[i];
    __syncthreads();
    const int g = lane >> 4, tq = (lane & 15) >> 2, tp = lane & 3;
    const int nt = nu * 4 + wave;
    const char* p0 = k1s + (4 * g + tq) * 160 + wave * 32 + tp * 8;
#pragma unroll
    for (int p = 0; p < 3; ++p) {
      s4 b0 = TRREAD(p0 + (32 * p) * 160);
      s4 b1v = TRREAD(p0 + (32 * p + 16) * 160);
      s8 bf = {b0[0], b0[1], b0[2], b0[3], b1v[0], b1v[1], b1v[2], b1v[3]};
      st16(w2F + (((size_t)nt * 48 + 3 * bn + p) * 64 + lane) * 8, __builtin_bit_cast(u32x4, bf));
    }
    if (rows == 112) {
      s4 b0 = TRREAD(p0 + 96 * 160);
      st8(w2H + (((size_t)nt * 12 + 3 * (bn >> 2) + (bn & 3)) * 64 + lane) * 4, __builtin_bit_cast(u32x2, b0));
    }
    STAMP(3);
    STAMPW();
    return;
  }
  {
    const int idx = (blockIdx.x - (K1_LN + K1_KG + K1_RP + K1_RQ)) * 256 + t;
    const int sgn = idx >= 4096, i = idx - sgn * 4096;
    u32 td = 0;
    if (t < 72) {
      const char* pa = t < 60 ? (const char*)xnP + (size_t)t * (512u << 10)
                              : (t < 66 ? (const char*)outp + (t - 60) * 524284 : (const char*)dskip);
      asm volatile("global_load_dword %0, %1, off" : "=v"(td) : "v"(pa) : "memory");
    }
    if (idx < FF) st4(b1f + idx, (u32)b1[idx] << 16);
    if (idx >= 2048 && idx < 4096) st4(flags + (idx - 2048), 0u);
    u32 r = 0;
    if (i < LUT_HALF) r = f2bf(mish_bf(bf2f((u16)((i + (LUT_E0 << 7)) | (sgn << 15)))));
    st2(lut + idx, r);
    asm volatile("s_waitcnt vmcnt(0)" :: "v"(td) : "memory");
  }
}

#define K2_G1 256
#define K2_SSM 240
#define KCPITCH 2080
#define SB0() __builtin_amdgcn_sched_barrier(0)
template <int V> struct IC { static constexpr int value = V; };
template <int I, int N, class F> __device__ __forceinline__ void static_for_i(F&& f) {
  if constexpr (I < N) { f(IC<I>{}); static_for_i<I + 1, N>(f); }
}
template <int N, class F> __device__ __forceinline__ void static_for(F&& f) { static_for_i<0, N>(f); }
__device__ __forceinline__ void glds16s(u32 voff, const void* sbase, const void* lp) {
  unsigned keep;
  unsigned lds_dst = (unsigned)(size_t)(const __attribute__((address_space(3))) char*)lp;
  asm volatile("s_mov_b32 %0, m0\n\ts_mov_b32 m0, %3\n\ts_nop 0\n\tglobal_load_lds_dwordx4 %1, %2\n\ts_mov_b32 m0, %0"
               : "=&s"(keep) : "v"(voff), "s"(sbase), "s"(lds_dst) : "memory");
}
#define MFMA_BF(acc, a, b) \
  asm volatile("v_mfma_f32_16x16x32_bf16 %0, %1, %2, %0" : "+v"(acc) : "v"(a), "v"(b))
#define MFMA_BF0(acc, a, b) \
  asm volatile("v_mfma_f32_16x16x32_bf16 %0, %1, %2, 0" : "=&v"(acc) : "v"(a), "v"(b))
#define GLOAD4(dst, voff, sbase) \
  asm volatile("global_load_dwordx4 %0, %1, %2" : "=v"(dst) : "v"(voff), "s"(sbase) : "memory")
#define DSR64(dst, addr, off) asm volatile("ds_read_b64 %0, %1 offset:%2" : "=v"(dst) : "v"(addr), "n"(off) : "memory")
#define DSR128(dst, addr, off) asm volatile("ds_read_b128 %0, %1 offset:%2" : "=v"(dst) : "v"(addr), "n"(off) : "memory")

#define NT1 4
#define G1_D 4
#define LUT_NEG 65536
#define BIAS_OFF 78848
constexpr int g1_chunk(int slot, int w) { return (slot == 2 && w == 3) ? 73728 : 8192 + (slot * 4 + w) * 5120; }
constexpr int g1_piece_lds(int p) {
  return p < 20 * NT1 ? g1_chunk((p / 20) % 3, p % 4) + ((p % 20) / 4) * 1024
                      : g1_chunk((p / 20) % 3, (p % 20) / 5) + (p % 5) * 1024;
}
constexpr int g1_tile(int p) { return p < 20 * NT1 ? p % NT1 : p / 20; }
constexpr int g1_kt(int p) { return p < 20 * NT1 ? p / NT1 : p % 20; }
constexpr int g1_fin(int T) { return T < NT1 ? 19 * NT1 + T : 20 * T + 19; }
constexpr int g1_vmB(int p) { return (p & 1) && p >= 17 && p < 86 && (p - 17) % 20 < 10 ? ((p - 17) / 20) * 5 + ((p - 17) % 20) / 2 : -1; }
constexpr int g1_vmL(int p) { return (p == 47 || p == 49 || p == 51 || p == 53) ? (p - 47) / 2 : -1; }
struct G1S { int vA[140], vS[140], lB[140], ws[24], cs[24], sp[3]; };
constexpr G1S g1_sched() {
  G1S S{};
  for (int w = 0; w < 24; ++w) {
    const int e = g1_fin(w >> 2) + 2;
    S.ws[w] = (w && S.ws[w - 1] + 2 > e) ? S.ws[w - 1] + 2 : e;
  }
  for (int w = 0; w < 24; ++w) S.cs[w] = S.ws[w] + G1_D + 2;
  for (int r = 0; r < 3; ++r) S.sp[r] = S.cs[8 * r + 7] + 1;
  int idxA[20] = {}, idxB[7] = {}, idxL = 0, ldB[140] = {};
  int v = 1;
  for (int h = 0; h < 3; ++h) {
    v += 5; idxB[h] = v;
    if (h < 2) for (int k = 0; k < 5; ++k) { v += 2; idxA[5 * h + k] = v; }
  }
  int l = NT1;
  for (int q = 0; q < G1_D; ++q) ldB[q] = ++l;
  for (int q = 0; q < 140; ++q) {
    const int gq = q / 20, lp = q % 20;
    S.vA[q] = -1; S.vS[q] = -1;
    if (lp == 20 - G1_D && gq < 6) {
      int need = idxB[gq + 1];
      if (gq + 1 == NT1 && idxL > need) need = idxL;
      S.vS[q] = v - need;
    }
    for (int r = 0; r < 3; ++r) if (q == S.sp[r]) v += 2;
    if (q < 20 * NT1 && q % NT1 == 0) S.vA[q] = v - idxA[q / NT1];
    if (q < 40 && (q & 1) == 0) { v += 1; idxA[10 + q / 4] = v; }
    if (g1_vmB(q) >= 0) { v += 1; idxB[3 + g1_vmB(q) / 5] = v; }
    if (g1_vmL(q) >= 0) { v += 1; idxL = v; }
    if (q + G1_D < 140) ldB[q + G1_D] = ++l;
    if (lp == 12 && gq + 1 >= NT1 && gq < 6) ++l;
    S.lB[q] = l - ldB[q];
    for (int w = 0; w < 24; ++w) if (S.ws[w] + 1 == q) l += 2;
  }
  return S;
}
constexpr G1S G1 = g1_sched();
constexpr int g1_word(int p) { for (int w = 0; w < 24; ++w) if (p >= G1.ws[w] && p < G1.ws[w] + 2) return w; return -1; }
constexpr int g1_comb(int p) { for (int w = 0; w < 24; ++w) if (p == G1.cs[w]) return w; return -1; }
static_assert(G1.sp[2] < 140 && G1.ws[0] >= 20 * NT1 - G1_D, "schedule");
#define WAIT_L(n) __builtin_amdgcn_s_waitcnt(0xC07F | (((n) > 15 ? 15 : (n)) << 8))
#define MF_ "v_mfma_f32_16x16x32_bf16 "
#define SDWA_(h) " dst_sel:DWORD dst_unused:UNUSED_PAD src0_sel:DWORD src1_sel:WORD_" #h "\n\t"
template <bool F> __device__ __forceinline__ void step_p(f4& c0, f4& c1, const u32x4& b, const u32x4& x0, const u32x4& x1, const f4& bs) {
  if constexpr (F) asm volatile(MF_ "%0, %2, %3, %5\n\t" MF_ "%1, %2, %4, %5" : "=&v"(c0), "=&v"(c1) : "v"(b), "v"(x0), "v"(x1), "v"(bs));
  else asm volatile(MF_ "%0, %2, %3, %0\n\t" MF_ "%1, %2, %4, %1" : "+v"(c0), "+v"(c1) : "v"(b), "v"(x0), "v"(x1));
}
template <bool F> __device__ __forceinline__ void step_x(f4& c0, f4& c1, const u32x4& b, const u32x4& x0, const u32x4& x1, const f4& bs,
                                                         u32& w, u32& z, u32& mx, float e0, float e1, u32 a0, u32 a1, u32 kb) {
#define X_MID "v_cvt_pk_bf16_f32 %2, %8, %9\n\tv_pk_add_u16 %3, %2, %12\n\t"
#define X_END "\n\tv_max3_u16 %4, %4, %10, %11"
  if constexpr (F) asm volatile(MF_ "%0, %5, %6, %13\n\t" X_MID MF_ "%1, %5, %7, %13" X_END
                                : "=&v"(c0), "=&v"(c1), "=&v"(w), "=&v"(z), "+v"(mx)
                                : "v"(b), "v"(x0), "v"(x1), "v"(e0), "v"(e1), "v"(a0), "v"(a1), "s"(kb), "v"(bs));
  else asm volatile(MF_ "%0, %5, %6, %0\n\t" X_MID MF_ "%1, %5, %7, %1" X_END
                    : "+v"(c0), "+v"(c1), "=&v"(w), "=&v"(z), "+v"(mx)
                    : "v"(b), "v"(x0), "v"(x1), "v"(e0), "v"(e1), "v"(a0), "v"(a1), "s"(kb));
}
template <bool F> __device__ __forceinline__ void step_y(f4& c0, f4& c1, const u32x4& b, const u32x4& x0, const u32x4& x1, const f4& bs,
                                                         u32& a0, u32& a1, u32& r0, u32& r1, u32 z) {
#define Y_MID "v_lshlrev_b32_sdwa %2, 1, %9" SDWA_(0) "v_lshlrev_b32_sdwa %3, 1, %9" SDWA_(1)
#define Y_END "\n\tds_read_u16 %4, %2\n\tds_read_u16 %5, %3"
  if constexpr (F) asm volatile(MF_ "%0, %6, %7, %10\n\t" Y_MID MF_ "%1, %6, %8, %10" Y_END
                                : "=&v"(c0), "=&v"(c1), "=&v"(a0), "=&v"(a1), "=&v"(r0), "=&v"(r1)
                                : "v"(b), "v"(x0), "v"(x1), "v"(z), "v"(bs) : "memory");
  else asm volatile(MF_ "%0, %6, %7, %0\n\t" Y_MID MF_ "%1, %6, %8, %1" Y_END
                    : "+v"(c0), "+v"(c1), "=&v"(a0), "=&v"(a1), "=&v"(r0), "=&v"(r1)
                    : "v"(b), "v"(x0), "v"(x1), "v"(z) : "memory");
}
__device__ __forceinline__ void comb(u32& r, u32 hi) { asm volatile("v_lshl_or_b32 %0, %1, 16, %0" : "+v"(r) : "v"(hi)); }
__device__ __forceinline__ void max3u(u32& mx, u32 a0, u32 a1) { asm volatile("v_max3_u16 %0, %0, %1, %2" : "+v"(mx) : "v"(a0), "v"(a1)); }
__device__ __forceinline__ void epi_word(u32& w, u32& r0, u32& r1, u32& mx, float e0, float e1, u32 kb) {
  u32 z;
  asm volatile("v_cvt_pk_bf16_f32 %0, %5, %6\n\tv_pk_add_u16 %4, %0, %7\n\t"
               "v_lshlrev_b32_sdwa %1, 1, %4" SDWA_(0) "v_lshlrev_b32_sdwa %2, 1, %4" SDWA_(1)
               "v_max3_u16 %3, %3, %1, %2\n\tds_read_u16 %1, %1\n\tds_read_u16 %2, %2"
               : "=&v"(w), "=&v"(r0), "=&v"(r1), "+v"(mx), "=&v"(z) : "v"(e0), "v"(e1), "s"(kb) : "memory");
}
__device__ __forceinline__ void mfma_c(f4& acc, const u32x4& a, const u32x4& b, const f4& c) {
  asm volatile("v_mfma_f32_16x16x32_bf16 %0, %1, %2, %3" : "=&v"(acc) : "v"(a), "v"(b), "v"(c));
}
__device__ __forceinline__ void mfma_a(f4& acc, const u32x4& a, const u32x4& b) { MFMA_BF(acc, a, b); }
template <int OFF, class V> __device__ __forceinline__ void dsr128(V& d, u32 addr) { DSR128(d, addr, OFF); }
__device__ __forceinline__ void pin2(u32x4& a, u32x4& b) { asm volatile("" : "+v"(a), "+v"(b)); }
template <int N> __device__ __forceinline__ void wait_res(u32 (&a)[4], u32 (&b)[4]) {
  asm volatile("s_waitcnt lgkmcnt(%8)"
               : "+v"(a[0]), "+v"(a[1]), "+v"(a[2]), "+v"(a[3]), "+v"(b[0]), "+v"(b[1]), "+v"(b[2]), "+v"(b[3])
               : "n"(N > 15 ? 15 : N) : "memory");
}
template <int OFF> __device__ __forceinline__ void glds_o(u32 voff, const void* sbase, u32 m) {
  asm volatile("s_mov_b32 m0, %2\n\ts_nop 0\n\tglobal_load_lds_dwordx4 %0, %1 offset:%3"
               :: "v"(voff), "s"(sbase), "s"(m), "n"(OFF) : "memory", "m0");
}
__device__ __forceinline__ void gload4(u32x4& d, u32 voff, const void* sbase) { GLOAD4(d, voff, sbase); }
__device__ __forceinline__ void st16s(u32 voff, const void* sbase, u32x4 v) {
  asm volatile("global_store_dwordx4 %0, %1, %2" STF :: "v"(voff), "v"(v), "s"(sbase) : "memory");
}

template <int KID>
__device__ __forceinline__ void ssm_block(char* shm, const int grp, const int e0, const int nch, const u16* __restrict__ xnP,
                                          const u16* __restrict__ Pg, u16* __restrict__ yT, u32* flag) {
    const int t = threadIdx.x, lane = t & 63;
    const int wave = __builtin_amdgcn_readfirstlane(t >> 6);
    STAMPB(KID, (char*)yT - (13u << 20));
    STAMP(0);
    const int eo = (e0 & ~1) > 4 ? 4 : (e0 & ~1);
    const int dbase = (grp >> 2) * 32 + (grp & 3) * 4;
    u16* Kc = (u16*)shm;
    float* red = (float*)(shm + 16896);
#pragma unroll
    for (int c = 0; c < 3; ++c) {
      if (c < nch) {
        const int e = e0 + c;
        const int d = dbase + (e >> 2) * 16 + (e & 3);
        char* buf = shm + 41472 + c * 6144;
        if (wave == 0) glds16s(lane * 16, (const char*)(Pg + (size_t)d * PPITCH), buf);
        if (wave == 1 && lane < 32) glds16s(lane * 16, (const char*)(Pg + (size_t)d * PPITCH) + 1024, buf + 1024);
      }
    }
    {
      u32x2 xv[8];
      const char* xp = (const char*)xnP + ((((size_t)(t >> 3) * 20 + (grp >> 2)) * 64 + (grp & 3) * 16 + ((2 * t) & 15)) * 16 + eo * 2);
#pragma unroll
      for (int i = 0; i < 4; ++i) {
        xv[2 * i] = *(const u32x2*)(xp + (size_t)i * 655360);
        xv[2 * i + 1] = *(const u32x2*)(xp + (size_t)i * 655360 + 16);
      }
#pragma unroll
      for (int c = 0; c < 3; ++c) {
        if (c < nch) {
          const u32 pos2 = 2 * (e0 + c - eo);
          const u32 sel = 0x00000c0cu | (pos2 << 16) | ((pos2 + 1) << 24);
          u16* xs = (u16*)(shm + 41472 + c * 6144 + 2048);
#pragma unroll
          for (int i = 0; i < 4; ++i) {
            const float x0 = __uint_as_float(__builtin_amdgcn_perm(xv[2 * i][1], xv[2 * i][0], sel));
            const float x1 = __uint_as_float(__builtin_amdgcn_perm(xv[2 * i + 1][1], xv[2 * i + 1][0], sel));
            *(u32*)&xs[2 * t + 512 * i] = pk_h(x0, x1);
          }
        }
      }
    }
    if (t < 96) *(u32x4*)(shm + 41472 + (t >> 5) * 6144 + 1536 + (t & 31) * 16) = (u32x4){0u, 0u, 0u, 0u};
    if (t < 8) *(u32x4*)&Kc[(t >> 1) * KCPITCH + (256 + (t & 1)) * 8] = (u32x4){0u, 0u, 0u, 0u};
    WAIT_V(0);
    RAW_BARRIER();
    STAMP(3);
    const int b = lane & 15, g = lane >> 4;
    const int m = 255 - t;
    const u32 lds0 = (u32)(size_t)(const __attribute__((address_space(3))) char*)shm;
    const int abase = ((3 - b) & 3) * KCPITCH + 2028 - 4 * (b >> 2) + 8 * g;
    const int bbase = 256 - 16 + 16 * b + 8 * g;
#pragma unroll 1
    for (int c = 0; c < nch; ++c) {
      const int e = e0 + c;
      const int d = dbase + (e >> 2) * 16 + (e & 3);
      const u16* P = (const u16*)(shm + 41472 + c * 6144);
      const float kscale = *(const float*)&P[704];
      {
        u32 wv[6];
        if (wave < 2) {
          const int i = 512 + m - 128;
          const u32 pc = P[i - 1], pa = P[i], pb = P[i + 1];
          const u32 aa = pa | (pa << 16);
          wv[0] = pb | (pb << 16);
          wv[1] = pb | (((m & 1) ? pb : pa) << 16);
          wv[2] = aa; wv[3] = aa; wv[4] = aa;
          wv[5] = m == 128 ? pc : pa;
        } else if (wave == 2) {
          const int i = 384 + 2 * (m - 64);
          const u32 q0 = *(const u32*)&P[i - 2], q1 = *(const u32*)&P[i], q2 = *(const u32*)&P[i + 2];
          wv[0] = __builtin_amdgcn_perm(q2, q2, 0x01000100u);
          wv[1] = __builtin_amdgcn_perm(q1, q1, 0x03020302u);
          wv[2] = __builtin_amdgcn_perm(q1, q1, 0x01000302u);
          wv[3] = __builtin_amdgcn_perm(q1, q1, 0x01000100u);
          wv[4] = m == 64 ? __builtin_amdgcn_perm(q0, q1, 0x07060100u) : wv[3];
          wv[5] = m == 64 ? q0 : (q0 >> 16);
        } else {
#pragma unroll
          for (int hh = 0; hh < 2; ++hh) {
            const int mm = m - hh;
            const int sg = mm >= 32 ? 1 : 0;
#pragma unroll
            for (int j = 0; j < (hh ? 2 : 4); ++j) {
              u32 pk = 0;
#pragma unroll
              for (int h = 0; h < 2; ++h) {
                if (hh == 0 || 2 * j + h < 3) {
                  int q = 8 * mm + 7 - (2 * j + h);
                  q = q < 0 ? 0 : q;
                  int idx = sg ? 128 + ((q + ((q >> 1) & 1)) >> 1) : q;
                  pk |= ((u32)P[idx]) << (16 * h);
                }
              }
              wv[hh * 4 + j] = (hh && mm < 0) ? 0u : pk;
            }
          }
        }
        *(u32x4*)&Kc[0 * KCPITCH + t * 8] = (u32x4){wv[0], wv[1], wv[2], wv[3]};
        *(u32x4*)&Kc[1 * KCPITCH + t * 8] = (u32x4){__builtin_amdgcn_alignbit(wv[1], wv[0], 16), __builtin_amdgcn_alignbit(wv[2], wv[1], 16),
                                                    __builtin_amdgcn_alignbit(wv[3], wv[2], 16), __builtin_amdgcn_alignbit(wv[4], wv[3], 16)};
        *(u32x4*)&Kc[2 * KCPITCH + t * 8] = (u32x4){wv[1], wv[2], wv[3], wv[4]};
        *(u32x4*)&Kc[3 * KCPITCH + t * 8] = (u32x4){__builtin_amdgcn_alignbit(wv[2], wv[1], 16), __builtin_amdgcn_alignbit(wv[3], wv[2], 16),
                                                    __builtin_amdgcn_alignbit(wv[4], wv[3], 16), __builtin_amdgcn_alignbit(wv[5], wv[4], 16)};
      }
      __syncthreads();
      if (c == 0) { STAMP(4); }
      if (c == 2) { STAMP(9); }
      f4 acc[8];
#pragma unroll
      for (int i = 0; i < 8; ++i) acc[i] = (f4){0.f, 0.f, 0.f, 0.f};
      {
        u32x2 a0[2][8], a1[2][8];
        u32x4 bq[2][8];
        const u32 aaddr = lds0 + (u32)(abase - 64 * wave - 32 - 256 * 7) * 2;
        const u32 baddr = lds0 + 41472 + c * 6144 + 1536 + (u32)(bbase - 64 * wave - 32) * 2;
#pragma unroll
        for (int rr = 0; rr < 2; ++rr) {
#pragma unroll
          for (int u = 0; u < 8; ++u) {
            DSR64(a0[rr][u], aaddr, (7 - u) * 512 + (1 - rr) * 64);
            DSR64(a1[rr][u], aaddr, (7 - u) * 512 + (1 - rr) * 64 + 8);
            DSR128(bq[rr][u], baddr, u * 512 + (1 - rr) * 64);
            if (rr == 1 && u == 3) {
              asm volatile("s_waitcnt lgkmcnt(12)"
                           : "+v"(a0[0][0]), "+v"(a1[0][0]), "+v"(a0[0][1]), "+v"(a1[0][1]), "+v"(a0[0][2]), "+v"(a1[0][2]),
                             "+v"(a0[0][3]), "+v"(a1[0][3]), "+v"(a0[0][4]), "+v"(a1[0][4]), "+v"(a0[0][5]), "+v"(a1[0][5]),
                             "+v"(a0[0][6]), "+v"(a1[0][6]), "+v"(a0[0][7]), "+v"(a1[0][7]),
                             "+v"(bq[0][0]), "+v"(bq[0][1]), "+v"(bq[0][2]), "+v"(bq[0][3]),
                             "+v"(bq[0][4]), "+v"(bq[0][5]), "+v"(bq[0][6]), "+v"(bq[0][7])
                           :: "memory");
            }
          }
        }
        SB0();
#pragma unroll
        for (int rr = 0; rr < 2; ++rr) {
          if (rr == 1) {
            asm volatile("s_waitcnt lgkmcnt(0)"
                         : "+v"(a0[1][0]), "+v"(a1[1][0]), "+v"(a0[1][1]), "+v"(a1[1][1]), "+v"(a0[1][2]), "+v"(a1[1][2]),
                           "+v"(a0[1][3]), "+v"(a1[1][3]), "+v"(a0[1][4]), "+v"(a1[1][4]), "+v"(a0[1][5]), "+v"(a1[1][5]),
                           "+v"(a0[1][6]), "+v"(a1[1][6]), "+v"(a0[1][7]), "+v"(a1[1][7]),
                           "+v"(bq[1][0]), "+v"(bq[1][1]), "+v"(bq[1][2]), "+v"(bq[1][3]),
                           "+v"(bq[1][4]), "+v"(bq[1][5]), "+v"(bq[1][6]), "+v"(bq[1][7])
                         :: "memory");
          }
#pragma unroll
          for (int u = 0; u < 8; ++u) {
            const h8 A = __builtin_bit_cast(h8, (u32x4){a0[rr][u][0], a0[rr][u][1], a1[rr][u][0], a1[rr][u][1]});
#pragma unroll
            for (int v = 0; v + u < 8; ++v)
              acc[u + v] = __builtin_amdgcn_mfma_f32_16x16x32_f16(A, __builtin_bit_cast(h8, bq[rr][v]), acc[u + v], 0, 0, 0);
          }
          SB0();
        }
      }
      if (c == 0) { STAMP(5); }
      if (c == 2) { STAMP(10); }
      auto red_phase = [&](auto WC) {
        constexpr int W = decltype(WC)::value;
#pragma unroll
        for (int o = 1; o < 4; ++o) {
          const int ow = (W + o) & 3;
#pragma unroll
          for (int i = 0; i < 2; ++i) *(f4*)&red[(((ow * 3 + (o - 1)) * 2 + i) * 64 + lane) * 4] = acc[ow * 2 + i];
        }
        __syncthreads();
        u32x2 o[2];
#pragma unroll
        for (int i = 0; i < 2; ++i) {
          f4 s1 = *(const f4*)&red[(((W * 3 + 0) * 2 + i) * 64 + lane) * 4];
          f4 s2 = *(const f4*)&red[(((W * 3 + 1) * 2 + i) * 64 + lane) * 4];
          f4 s3 = *(const f4*)&red[(((W * 3 + 2) * 2 + i) * 64 + lane) * 4];
          f4 sum = ((acc[W * 2 + i] + s1) + (s2 + s3)) * kscale;
          o[i] = (u32x2){pk_bf(sum[0], sum[1]), pk_bf(sum[2], sum[3])};
        }
#pragma unroll
        for (int i = 0; i < 2; ++i) st8(yT + (size_t)d * SEQ + 256 * (W * 2 + i) + 16 * b + 4 * g, o[i]);
      };
      if (wave == 0) red_phase(IC<0>{});
      else if (wave == 1) red_phase(IC<1>{});
      else if (wave == 2) red_phase(IC<2>{});
      else red_phase(IC<3>{});
      if (c == 0) { STAMP(6); }
      if (c == 1) { STAMP(7); }
      if (c == 2) { STAMP(11); }
    }
    if (flag) {
      WAIT_V(0);
      if (lane == 0) st4(flag + wave * 256, 1u);
    }
    STAMP(8);
    STAMPW();
}

#define K3R 3
#define K3HB 1
#define K3NB 3
#define K3SLEEP1 8
#define K3YS 14
__device__ __forceinline__ void glds16c(const void* gsrc, const void* lp) {
  unsigned keep;
  unsigned lds_dst = __builtin_amdgcn_readfirstlane((unsigned)(size_t)(const __attribute__((address_space(3))) char*)lp);
  asm volatile("s_mov_b32 %0, m0\n\ts_mov_b32 m0, %2\n\ts_nop 0\n\tglobal_load_lds_dwordx4 %1, off sc1\n\ts_mov_b32 m0, %0"
               : "=&s"(keep) : "v"(gsrc), "s"(lds_dst) : "memory");
}
#define OUTF ""
__device__ __forceinline__ void so16(void* p, u32x4 v) { asm volatile("global_store_dwordx4 %0, %1, off" OUTF :: "v"(p), "v"(v) : "memory"); }
__device__ __forceinline__ void so8(void* p, u32x2 v) { asm volatile("global_store_dwordx2 %0, %1, off" OUTF :: "v"(p), "v"(v) : "memory"); }
__device__ __forceinline__ void g2_block(char* shm, const int bid, char* ws, const u16* __restrict__ xin,
                                         const u16* __restrict__ dskip, u16* __restrict__ out) {
  constexpr int R = K3R;
  constexpr int NB = K3NB;
  constexpr int NF = 12, NST = 15;
  const u16* w2F = (const u16*)(ws + WS_W2F);
  const u16* w2H = (const u16*)(ws + WS_W2H);
  const float2* stats = (const float2*)(ws + WS_STATS);
  const u16* yT = (const u16*)(ws + WS_YT);
  const u32* flags = (const u32*)(ws + WS_FLAGS);
  const int t = threadIdx.x, lane = t & 63;
  const int wave = __builtin_amdgcn_readfirstlane(t >> 6);
  const int xcd = bid & 7, idx = bid >> 3;
  const int mi = idx & 7, ni = idx >> 3;
  const int bm = (xcd >> 1) * 8 + mi, bn = (xcd & 1) * 4 + ni;
  const int mt0 = bm * 4, m0 = bm * 64, n0 = bn * 80, nt0 = bn * 5;
  STAMPB(3, ws);
  STAMP(0);
  const char* abase = ws + WS_ACTF + ((size_t)mt0 * 48 + wave) * 1024;
  const char* bbase = (const char*)w2F + ((size_t)nt0 * 48 + wave) * 1024;
  const char* ahb = ws + WS_ACTH + ((size_t)(mt0 >> 1) * 12 + wave) * 1024;
  const char* bhb = (const char*)w2H + ((size_t)nt0 * 12 + wave) * 512;
  u32 va[4], vb[5], vh[2];
  const __amdgpu_buffer_rsrc_t ra = __builtin_amdgcn_make_buffer_rsrc((void*)abase, 0, 4 * 48 * 1024, 0x00020000);
  const __amdgpu_buffer_rsrc_t rh = __builtin_amdgcn_make_buffer_rsrc((void*)ahb, 0, 2 * 12 * 1024, 0x00020000);
#define LDA(r, p, v, so) __builtin_bit_cast(u32x4, __builtin_amdgcn_raw_buffer_load_b128(r, v, so, 16))
#pragma unroll
  for (int j = 0; j < 5; ++j) vb[j] = lane * 16 + j * (48 * 1024);
  u32x4 fa[R][4], fb[R][5];
  u32x4 ha[3][2];
  u32x2 hb[3][5];
  auto issue = [&](int s, int what = 3) {
    if (s < NF) {
      const char* ap = abase + s * 4096;
      const char* bp = bbase + s * 4096;
      if (what & 1) {
#pragma unroll
        for (int i = 0; i < 4; ++i) fa[s % R][i] = LDA(ra, ap, va[i], s * 4096);
      }
      if (what & 2) {
        if (s >= R && s < R + NB) {
#pragma unroll
          for (int j = 0; j < 5; ++j) fb[s % R][j] = *(const u32x4*)(shm + 20480 + (((s - R) * 4 + wave) * 5 + j) * 1024 + lane * 16);
        } else {
#pragma unroll
          for (int j = 0; j < 5; ++j) fb[s % R][j] = *(const u32x4*)(bp + vb[j]);
        }
      }
    } else {
      const int h = s - NF;
#pragma unroll
      for (int i = 0; i < 2; ++i) ha[h][i] = LDA(rh, ahb + h * 4096, vh[i], h * 4096);
      if (h >= K3HB) {
#pragma unroll
        for (int j = 0; j < 5; ++j) hb[h][j] = *(const u32x2*)(bhb + h * 2048 + (lane * 8 + j * (12 * 512)));
      }
    }
  };
  auto aux = [&]() {
#pragma unroll
    for (int i = 0; i < 3; ++i) {
      int p = wave + 4 * i;
      if (p > 10) p = 10;
      const int c = p * 64 + lane;
      const u16* src;
      if (p < 10) src = xin + (size_t)(m0 + c / 10) * DM + n0 + (c % 10) * 8;
      else src = (lane < 32) ? (const u16*)(stats + m0) + lane * 8 : dskip + n0 + (lane < 42 ? lane - 32 : 9) * 8;
      GLDS(src, shm + p * 1024);
    }
  };
  auto yload = [&]() {
    const u32* fp = flags + (bm >> 3) * 256 + ((n0 >> 5) * 4) * 3 + (lane < 12 * 3 ? lane : 0);
#define K3SLEEP 16
    if (wave == 0)
    for (int it = 0; it < (1 << 20); ++it) {
      u32 f;
      asm volatile("global_load_dword %0, %1, off sc1\n\ts_waitcnt vmcnt(0)" : "=v"(f) : "v"(fp) : "memory");
      if (__builtin_amdgcn_ballot_w64(f != 1u) == 0) break;
      __builtin_amdgcn_s_sleep(K3SLEEP);
    }
    __builtin_amdgcn_s_barrier();
#pragma unroll
    for (int i = 0; i < 3; ++i) {
      int p = wave + 4 * i;
      if (p > 9) p = 9;
      const int c = p * 64 + lane;
      glds16c(yT + (size_t)(n0 + (c >> 3)) * SEQ + m0 + (c & 7) * 8, shm + (10 + p) * 1024);
    }
  };
  SB0();
  const u32* fp1 = flags + 1024 + bm * 32 + (lane & 31);
  u32 f1 = 0;
  SB0();
#pragma unroll
  for (int s = 0; s < R; ++s) { issue(s, 2); SB0(); }
#pragma unroll
  for (int h = 0; h < K3HB; ++h) {
#pragma unroll
    for (int j = 0; j < 5; ++j) hb[h][j] = *(const u32x2*)(bhb + h * 2048 + (lane * 8 + j * (12 * 512)));
    SB0();
  }
  aux();
  SB0();
  {
    const u32 lb0 = (u32)(size_t)(const __attribute__((address_space(3))) char*)shm + 20480 + wave * 5120;
#pragma unroll
    for (int s = 0; s < NB; ++s)
#pragma unroll
      for (int j = 0; j < 5; ++j) glds_o<0>(vb[j], bbase + (R + s) * 4096, lb0 + s * 20480 + j * 1024);
  }
  SB0();
  if (wave == 0) {
    asm volatile("global_load_dword %0, %1, off sc1\n\ts_waitcnt vmcnt(0)" : "=v"(f1) : "v"(fp1) : "memory");
    for (int it = 0; it < (1 << 20); ++it) {
      if (__builtin_amdgcn_ballot_w64(f1 != 1u) == 0) break;
      __builtin_amdgcn_s_sleep(K3SLEEP1);
      asm volatile("global_load_dword %0, %1, off sc1\n\ts_waitcnt vmcnt(0)" : "=v"(f1) : "v"(fp1) : "memory");
    }
  }
  __builtin_amdgcn_s_barrier();
  {
    u32 l16 = lane * 16;
    asm volatile("" : "+v"(l16) :: "memory");
#pragma unroll
    for (int i = 0; i < 4; ++i) va[i] = l16 + ((wave + i) & 3) * (48 * 1024);
#pragma unroll
    for (int i = 0; i < 2; ++i) vh[i] = l16 + (((wave >> 1) + i) & 1) * (12 * 1024);
  }
  STAMP(10);
  SB0();
#pragma unroll
  for (int s = 0; s < R; ++s) { issue(s, 1); SB0(); }
  STAMP(1);
  const int fr = lane & 15, g = lane >> 4, tq = (lane & 15) >> 2, tp = lane & 3;
  const int ml = wave * 16 + fr;
  u32x2 s1p[5];
  float2 st;
  auto resid = [&](int j) {
    const char* sx = shm;
    const char* sm = shm + 10 * 1024;
    if (j == 0) st = *(const float2*)(sm + ml * 8);
    const int nl = j * 16;
    u32x2 xx = *(const u32x2*)(sx + ml * 160 + (nl + 4 * g) * 2);
    u32x2 dd = *(const u32x2*)(sm + 512 + (nl + 4 * g) * 2);
#pragma unroll
    for (int h = 0; h < 2; ++h) {
      const float x0 = lo_f(xx[h]), x1 = hi_f(xx[h]);
      u32 d = pk_bf(x0 - st.x, x1 - st.x);
      u32 xn = pk_bf(lo_f(d) * st.y, hi_f(d) * st.y);
      s1p[j][h] = pk_bf(lo_f(xn) * lo_f(dd[h]), hi_f(xn) * hi_f(dd[h]));
    }
    asm volatile("" : "+v"(s1p[j]));
  };
  auto resid2 = [&](int j) {
    const char* sx = shm;
    const char* sy = shm + 10 * 1024;
    const int nl = j * 16;
    u32x2 xx = *(const u32x2*)(sx + ml * 160 + (nl + 4 * g) * 2);
    s4 yy = TRREAD(sy + (nl + 4 * g + tq) * 128 + (wave * 16 + 4 * tp) * 2);
    const u32x2 yw = __builtin_bit_cast(u32x2, yy);
#pragma unroll
    for (int h = 0; h < 2; ++h) {
      const float x0 = lo_f(xx[h]), x1 = hi_f(xx[h]);
      u32 a = pk_bf(lo_f(yw[h]) + lo_f(s1p[j][h]), hi_f(yw[h]) + hi_f(s1p[j][h]));
      s1p[j][h] = pk_bf(x0 + lo_f(a), x1 + hi_f(a));
    }
  };
  f4 acc[4][5];
#pragma unroll
  for (int i = 0; i < 4; ++i)
#pragma unroll
    for (int j = 0; j < 5; ++j) acc[i][j] = (f4){0.f, 0.f, 0.f, 0.f};
  const bool odd = wave & 1;
  SB0();
#pragma unroll
  for (int s = 0; s < NST; ++s) {
    if (s < NF) {
#pragma unroll
      for (int i = 0; i < 4; ++i)
#pragma unroll
        for (int j = 0; j < 5; ++j)
          acc[i][j] = __builtin_amdgcn_mfma_f32_16x16x32_bf16(__builtin_bit_cast(s8, fb[s % R][j]),
                                                              __builtin_bit_cast(s8, fa[s % R][i]), acc[i][j], 0, 0, 0);
    } else {
      const int h = s - NF;
      u32x2 af[4];
      af[0] = odd ? (u32x2){ha[h][0][2], ha[h][0][3]} : (u32x2){ha[h][0][0], ha[h][0][1]};
      af[1] = odd ? (u32x2){ha[h][1][0], ha[h][1][1]} : (u32x2){ha[h][0][2], ha[h][0][3]};
      af[2] = odd ? (u32x2){ha[h][1][2], ha[h][1][3]} : (u32x2){ha[h][1][0], ha[h][1][1]};
      af[3] = odd ? (u32x2){ha[h][0][0], ha[h][0][1]} : (u32x2){ha[h][1][2], ha[h][1][3]};
#pragma unroll
      for (int i = 0; i < 4; ++i)
#pragma unroll
        for (int j = 0; j < 5; ++j)
          acc[i][j] = __builtin_amdgcn_mfma_f32_16x16x16bf16_1k(__builtin_bit_cast(s4, hb[h][j]),
                                                               __builtin_bit_cast(s4, af[i]), acc[i][j], 0, 0, 0);
    }
    SB0();
    if (s + R < NST) issue(s + R);
    SB0();
    if (s == 4) {
      WAIT_V(R * 9 > 60 ? 60 : R * 9);
      __builtin_amdgcn_s_barrier();
      SB0();
    }
    if (s >= 5 && s < 10) { resid(s - 5); SB0(); }
    if (s == 10) { __builtin_amdgcn_s_barrier(); SB0(); }
    if (s == K3YS) { yload(); SB0(); STAMP(3); }
    if (s == 0) { STAMP(2); }
    if (s == 2) { STAMP(4); }
    if (s == 6) { STAMP(5); }
    if (s == 10) { STAMP(6); }
  }
  STAMP(7);
  char* red = shm + 20 * 1024;
#pragma unroll
  for (int i = 1; i < 4; ++i)
#pragma unroll
    for (int j = 0; j < 5; ++j)
      *(f4*)(red + ((((wave + i) & 3) * 3 + (i - 1)) * 5 + j) * 1024 + lane * 16) = acc[i][j];
  WAIT_V(0);
  __syncthreads();
  STAMP(9);
  u32x2 ov[5];
#pragma unroll
  for (int j = 0; j < 5; ++j) {
    resid2(j);
    f4 p0 = *(const f4*)(red + ((wave * 3 + 0) * 5 + j) * 1024 + lane * 16);
    f4 p1 = *(const f4*)(red + ((wave * 3 + 1) * 5 + j) * 1024 + lane * 16);
    f4 p2 = *(const f4*)(red + ((wave * 3 + 2) * 5 + j) * 1024 + lane * 16);
    f4 sum = (acc[0][j] + p0) + (p1 + p2);
    u32 m0p = pk_bf(sum[0], sum[1]), m1p = pk_bf(sum[2], sum[3]);
    ov[j] = (u32x2){pk_bf(lo_f(s1p[j][0]) + lo_f(m0p), hi_f(s1p[j][0]) + hi_f(m0p)),
                    pk_bf(lo_f(s1p[j][1]) + lo_f(m1p), hi_f(s1p[j][1]) + hi_f(m1p))};
  }
  u16* orow = out + (size_t)(m0 + ml) * DM + n0;
#pragma unroll
  for (int jp = 0; jp < 2; ++jp) {
    auto r0 = __builtin_amdgcn_permlane16_swap(ov[2 * jp][0], ov[2 * jp + 1][0], false, false);
    auto r1 = __builtin_amdgcn_permlane16_swap(ov[2 * jp][1], ov[2 * jp + 1][1], false, false);
    so16(orow + (2 * jp + (g & 1)) * 16 + 8 * (g >> 1), (u32x4){r0[0], r1[0], r0[1], r1[1]});
  }
  so8(orow + 64 + 4 * g, ov[4]);
  STAMP(8);
  STAMPW();
}

extern "C" __global__ __launch_bounds__(256, 2) void k2_kernel(char* ws, const u16* __restrict__ xin,
                                                    const u16* __restrict__ dskip, u16* __restrict__ out) {
  const u16* xnP = (const u16*)(ws + WS_XNP);
  const u16* w1P = (const u16*)(ws + WS_W1P);
  const float* b1f = (const float*)(ws + WS_B1F);
  const u16* lut = (const u16*)(ws + WS_LUT);
  u16* actF = (u16*)(ws + WS_ACTF);
  u16* actH = (u16*)(ws + WS_ACTH);
  u32* flags = (u32*)(ws + WS_FLAGS);
  __shared__ __attribute__((aligned(1024))) char shm[81920];
  const int t = threadIdx.x, lane = t & 63;
  const int wave = __builtin_amdgcn_readfirstlane(t >> 6);
  const int vb = blockIdx.x;
  if (vb >= K2_G1) {
    const int sb = vb - K2_G1, li = sb >> 3, grp = (sb & 7) * 10 + li / 3, part = li % 3;
    ssm_block<2>(shm, grp, 3 * part, part < 2 ? 3 : 2, xnP, (const u16*)(ws + WS_PG), (u16*)(ws + WS_YT), flags + grp * 3 + part);
    return;
  }
  const int xcd = vb & 7, idx = vb >> 3;
  const int mi = idx & 3, ni = idx >> 2;
  const int bm = (xcd >> 1) * 4 + mi, bn = (xcd & 1) * 8 + ni;
  STAMPB(2, (char*)actF - (16u << 20));
  STAMP(0);
  const int mtw = bm * 8 + 2 * wave, nt0 = 27 * (bn >> 2) + 7 * (bn & 3);
  const char* abase = (const char*)xnP + (size_t)mtw * (20 * 1024);
  const u32 va0 = lane * 16, va1 = lane * 16 + 20 * 1024;
  const u32 lds0 = (u32)(size_t)(const __attribute__((address_space(3))) char*)shm;
  const u32 va4 = lane * 16 + 4096;
  const char* bb1 = (const char*)w1P + (size_t)((nt0 + wave) * 20) * 1024;
  const char* bb2 = (const char*)w1P + (size_t)(nt0 * 20 + 5 * wave) * 1024;
  const u32 mc0 = lds0 + 8192 + wave * 5120;
  const u32 mc2 = wave == 3 ? lds0 + 73728 : mc0 + 40960;
  auto stageB = [&](auto GC, auto IC_) {
    constexpr int gq = decltype(GC)::value, i = decltype(IC_)::value;
    const char* src = gq < NT1 ? bb1 + gq * 5120 : bb2 + (size_t)((gq == 6 && bn == 15) ? 5 : gq) * 20480;
    const u32 m = gq % 3 == 2 ? mc2 : mc0 + (gq % 3) * 20480;
    if constexpr (i < 4) glds_o<i * 1024>(va0, src, m);
    else glds_o<0>(va4, src, m + 4096);
  };
  u32x4 fa[20][2];
  auto loadA = [&](int s) {
    const char* ap = abase + s * 1024;
    GLOAD4(fa[s][0], va0, ap);
    GLOAD4(fa[s][1], va1, ap);
  };
  glds_o<0>((lane < 28 ? lane : 27) * 16, (const char*)(b1f + nt0 * 16), lds0 + BIAS_OFF);
  static_for<5>([&](auto I) { stageB(IC<0>{}, I); });
#pragma unroll
  for (int s = 0; s < 5; ++s) loadA(s);
  static_for<5>([&](auto I) { stageB(IC<1>{}, I); });
#pragma unroll
  for (int s = 5; s < 10; ++s) loadA(s);
  static_for<5>([&](auto I) { stageB(IC<2>{}, I); });
  STAMP(1);
  WAIT_V(30);
  __builtin_amdgcn_s_barrier();
  STAMP(2);
  typedef unsigned short us2 __attribute__((ext_vector_type(2)));
  const u32 lb = lds0 + lane * 16, lbh = lds0 + lane * 16 + 65536, lbb = lds0 + BIAS_OFF + (lane >> 4) * 16;
  f4 acc[7][2];
  u32x4 fb[G1_D + 1];
  f4 bias[7];
  u32 hw[7][4];
  u32 res[7][4], rhi[7][4];
  u32 mx[4] = {0u, 0u, 0u, 0u}, zz, a0 = 0, a1 = 0;
  auto readB = [&](auto PC) {
    constexpr int q = decltype(PC)::value, off = g1_piece_lds(q);
    if constexpr (off < 65536) dsr128<off>(fb[q % (G1_D + 1)], lb);
    else dsr128<off - 65536>(fb[q % (G1_D + 1)], lbh);
  };
  const u32 kb = (0x10000u - (LUT_E0 << 7)) * 0x10001u;
  auto epi_fix = [&](int T) __attribute__((always_inline)) {
#pragma unroll
    for (int q = 0; q < 4; ++q) {
      const u32 z0 = hw[T][q] & 0x7FFFu, z1 = (hw[T][q] >> 16) & 0x7FFFu;
      const bool b0 = (z0 - (LUT_E0 << 7)) >= (u32)LUT_HALF, b1v = (z1 - (LUT_E0 << 7)) >= (u32)LUT_HALF;
      const float h0 = lo_f(hw[T][q]), h1 = hi_f(hw[T][q]);
      const float f0 = (z0 < (LUT_E0 << 7)) ? h0 * 0.59765625f : (h0 > 0.f ? h0 : h0 * 0.0f);
      const float f1 = (z1 < (LUT_E0 << 7)) ? h1 * 0.59765625f : (h1 > 0.f ? h1 : h1 * 0.0f);
      const u32 fw = pk_bf(f0, f1);
      res[T][q] = ((b0 ? fw : res[T][q]) & 0xFFFFu) | ((b1v ? fw : res[T][q]) & 0xFFFF0000u);
    }
  };
  const char* obase = (const char*)actF + ((size_t)mtw * 48 + 3 * bn) * 1024;
#pragma unroll
  for (int T = 0; T < NT1; ++T) DSR128(bias[T], lbb, T * 64);
  static_for<G1_D>(readB);
  const unsigned long long sbi = wave == 0 ? (unsigned long long)ws : (wave == 1 ? (unsigned long long)out : (unsigned long long)xin);
  const char* sb = (const char*)sbi;
  const u32 lim = wave == 0 ? 59 : 5;
  static_for<140>([&](auto PC) {
    constexpr int p = decltype(PC)::value;
    constexpr int T = g1_tile(p), kt = g1_kt(p), gq = p / 20, lp = p % 20;
    if constexpr (G1.vS[p] >= 0) {
      WAIT_L(0);
      WAIT_V(G1.vS[p]);
      __builtin_amdgcn_s_barrier();
      if (gq == 0) { STAMP(3); }
      if (gq == 1) { STAMP(4); }
      if (gq == 3) { STAMP(5); }
      if (gq == 5) { STAMP(6); }
    }
    static_for<3>([&](auto RC) {
      constexpr int r = decltype(RC)::value;
      if constexpr (p == G1.sp[r]) {
        if constexpr (!(r < 2 && G1.ws[r < 2 ? 8 * r + 8 : 0] < p)) max3u(mx[r], a0, a1);
        if (__builtin_expect(__builtin_amdgcn_ballot_w64((mx[r] & 0xFFFFu) >= 2u * LUT_HALF) != 0, 0)) {
          epi_fix(2 * r);
          epi_fix(2 * r + 1);
        }
        SB0();
#pragma unroll
        for (int i = 0; i < 2; ++i)
          st16s(va0, obase + (i * 48 + r) * 1024,
                (u32x4){res[2 * r][2 * i], res[2 * r][2 * i + 1], res[2 * r + 1][2 * i], res[2 * r + 1][2 * i + 1]});
      }
    });
    if constexpr (G1.vA[p] >= 0) {
      WAIT_V(G1.vA[p]);
      pin2(fa[kt][0], fa[kt][1]);
    }
    if constexpr (p < 40 && (p & 1) == 0) gload4(fa[10 + p / 4][(p >> 1) & 1], (p & 2) ? va1 : va0, abase + (10 + p / 4) * 1024);
    if constexpr (g1_vmB(p) >= 0) stageB(IC<3 + g1_vmB(p) / 5>{}, IC<g1_vmB(p) % 5>{});
    if constexpr (g1_vmL(p) >= 0) {
      glds_o<g1_vmL(p) * 1024>(va0, (const char*)lut + wave * 4096, lds0 + (wave & 1) * 4096 + (wave >> 1) * LUT_NEG);
    }
#define TP2 41
    if constexpr (p == TP2) {
      const u32 vo = ((u32)lane < lim ? (u32)lane : lim) * 524284u;
      unsigned keep;
      asm volatile("s_mov_b32 %0, m0\n\ts_mov_b32 m0, %3\n\ts_nop 0\n\tglobal_load_lds_dword %1, %2\n\ts_mov_b32 m0, %0"
                   : "=&s"(keep) : "v"(vo), "s"(sb), "s"(lds0 + 79872 + wave * 256) : "memory");
    }
    if constexpr (p + G1_D < 140) readB(IC<p + G1_D>{});
    if constexpr (lp == 12 && gq + 1 >= NT1 && gq < 6) dsr128<(gq + 1) * 64>(bias[gq + 1], lbb);
    WAIT_L(G1.lB[p]);
    SB0();
    constexpr int w = g1_word(p), wT = w >= 0 ? w >> 2 : 0, wq = w & 3;
    if constexpr (w < 0) step_p<kt == 0>(acc[T][0], acc[T][1], fb[p % (G1_D + 1)], fa[kt][0], fa[kt][1], bias[T]);
    else if constexpr (p == G1.ws[w])
      step_x<kt == 0>(acc[T][0], acc[T][1], fb[p % (G1_D + 1)], fa[kt][0], fa[kt][1], bias[T], hw[wT][wq], zz, mx[w > 0 ? (w - 1) >> 3 : 0],
                      acc[wT][wq >> 1][2 * (wq & 1)], acc[wT][wq >> 1][2 * (wq & 1) + 1], a0, a1, kb);
    else step_y<kt == 0>(acc[T][0], acc[T][1], fb[p % (G1_D + 1)], fa[kt][0], fa[kt][1], bias[T], a0, a1, res[wT][wq], rhi[wT][wq], zz);
    constexpr int cw = g1_comb(p);
    if constexpr (cw >= 0) comb(res[cw >> 2][cw & 3], rhi[cw >> 2][cw & 3]);
    SB0();
  });
  STAMP(7);
  asm volatile("s_nop 7\n\ts_nop 7\n\ts_nop 7" : "+v"(acc[6][0]), "+v"(acc[6][1]) :: "memory");
  if ((bn & 3) != 3) {
#pragma unroll
    for (int q = 0; q < 4; ++q) epi_word(hw[6][q], res[6][q], rhi[6][q], mx[3], acc[6][q >> 1][2 * (q & 1)], acc[6][q >> 1][2 * (q & 1) + 1], kb);
    asm volatile("s_waitcnt lgkmcnt(0)" : "+v"(res[6][0]), "+v"(res[6][1]), "+v"(res[6][2]), "+v"(res[6][3]),
                 "+v"(rhi[6][0]), "+v"(rhi[6][1]), "+v"(rhi[6][2]), "+v"(rhi[6][3]) :: "memory");
#pragma unroll
    for (int q = 0; q < 4; ++q) res[6][q] |= rhi[6][q] << 16;
    if (__builtin_expect(__builtin_amdgcn_ballot_w64((mx[3] & 0xFFFFu) >= 2u * LUT_HALF) != 0, 0)) epi_fix(6);
    st16s(va0, (const char*)actH + ((size_t)(mtw >> 1) * 12 + 3 * (bn >> 2) + (bn & 3)) * 1024,
          (u32x4){res[6][0], res[6][1], res[6][2], res[6][3]});
  }
  STAMP(8);
  WAIT_V(0);
  if (lane == 0) st4(flags + 1024 + (bm * 4 + wave) * 16 + bn, 1u);
  STAMP(11);
  STAMPW();
  RAW_BARRIER();
  g2_block(shm, vb, ws, xin, dskip, out);
}
extern "C" __global__ void k4_end() {}
extern "C" void kernel_launch(void* const* d_in, const int* in_sizes, int n_in,
                              void* d_out, int out_size, void* d_ws, size_t ws_size,
                              hipStream_t stream) {
  const u16* u = (const u16*)d_in[0];
  const u16* lar = (const u16*)d_in[1];
  const u16* aim = (const u16*)d_in[2];
  const u16* cre = (const u16*)d_in[3];
  const u16* cim = (const u16*)d_in[4];
  const u16* logdt = (const u16*)d_in[5];
  const u16* dskip = (const u16*)d_in[6];
  const u16* w1 = (const u16*)d_in[8];
  const u16* b1 = (const u16*)d_in[9];
  const u16* w2 = (const u16*)d_in[10];
  u16* out = (u16*)d_out;
  char* ws = (char*)d_ws;
  if (ws_size < (32u << 20)) return;
  u16* xnP = (u16*)(ws);
  u16* w1P = (u16*)(ws + (4u << 20));
  float2* stats = (float2*)(ws + (12u << 20) + 4096);
  u16* lut = (u16*)(ws + (12u << 20) + 32768);
  float* b1f = (float*)(ws + (12u << 20) + 65536);
  u32* flags = (u32*)(ws + (12u << 20) + 98304);
  u16* yT = (u16*)(ws + (13u << 20));
  u16* actF = (u16*)(ws + (16u << 20));
  u16* actH = (u16*)(ws + (22u << 20));
  u16* w2F = (u16*)(ws + (24u << 20));
  u16* w2H = (u16*)(ws + (26u << 20));
  u16* Pg = (u16*)(ws + (28u << 20));

  k1_kernel<<<K1_LN + K1_KG + K1_RP + K1_RQ + K1_LUT, 256, 0, stream>>>(u, xnP, stats, lut, w1, w1P, w2, w2F, w2H, b1, b1f,
                                                                        lar, aim, cre, cim, logdt, Pg, flags, out, dskip);
  k2_kernel<<<K2_G1 + K2_SSM, 256, 0, stream>>>(ws, u, dskip, out);
}
```

```cpp
#include <hip/hip_runtime.h>
#include <hip/hip_bf16.h>

#define SEQ 2048
#define DM 640
#define NS 128
#define FF 1728

typedef unsigned short u16;
typedef short s4 __attribute__((ext_vector_type(4)));
typedef short s8 __attribute__((ext_vector_type(8)));
typedef _Float16 h8 __attribute__((ext_vector_type(8)));
typedef float f4 __attribute__((ext_vector_type(4)));
typedef unsigned int u32;
typedef unsigned int u32x4 __attribute__((ext_vector_type(4)));
typedef unsigned int u32x2 __attribute__((ext_vector_type(2)));

__device__ __forceinline__ float bf2f(u16 v) { return __uint_as_float(((u32)v) << 16); }
__device__ __forceinline__ u16 f2bf(float f) {
  u32 u = __float_as_uint(f);
  u32 r = (u + 0x7FFFu + ((u >> 16) & 1u)) >> 16;
  return (u16)r;
}
__device__ __forceinline__ float bfr(float f) { return bf2f(f2bf(f)); }

#define STAMPB(kid, base)
#define STAMP(k)
#define STAMPW()
typedef __bf16 bf2_t __attribute__((ext_vector_type(2)));
typedef float f2_t __attribute__((ext_vector_type(2)));
__device__ __forceinline__ u32 pk_bf(float a, float b) {
  f2_t v = {a, b};
  bf2_t r = __builtin_convertvector(v, bf2_t);
  return __builtin_bit_cast(u32, r);
}
__device__ __forceinline__ float lo_f(u32 w) { return __uint_as_float(w << 16); }
__device__ __forceinline__ float hi_f(u32 w) { return __uint_as_float(w & 0xFFFF0000u); }

#define WAIT_V(n) asm volatile("s_waitcnt vmcnt(%0)" ::"n"(n) : "memory")
#define RAW_BARRIER() do { asm volatile("s_waitcnt lgkmcnt(0)" ::: "memory"); __builtin_amdgcn_s_barrier(); } while (0)
__device__ __forceinline__ void glds16(const void* gsrc, const void* lp) {
  unsigned keep;
  unsigned lds_dst = (unsigned)(size_t)(const __attribute__((address_space(3))) char*)lp;
  asm volatile("s_mov_b32 %0, m0\n\ts_mov_b32 m0, %2\n\ts_nop 0\n\tglobal_load_lds_dwordx4 %1, off\n\ts_mov_b32 m0, %0"
               : "=&s"(keep) : "v"(gsrc), "s"(lds_dst) : "memory");
}
#define GLDS(gp, lp) glds16((const void*)(gp), (const void*)(lp))

#define STF " sc1"
__device__ __forceinline__ void st16(void* p, u32x4 v) { asm volatile("global_store_dwordx4 %0, %1, off" STF :: "v"(p), "v"(v) : "memory"); }
__device__ __forceinline__ void st8(void* p, u32x2 v) { asm volatile("global_store_dwordx2 %0, %1, off" STF :: "v"(p), "v"(v) : "memory"); }
__device__ __forceinline__ void st4(void* p, u32 v) { asm volatile("global_store_dword %0, %1, off" STF :: "v"(p), "v"(v) : "memory"); }
__device__ __forceinline__ void st2(void* p, u32 v) { asm volatile("global_store_short %0, %1, off" STF :: "v"(p), "v"(v) : "memory"); }

#define LUT_E0 102
#define LUT_E1 134
#define LUT_HALF ((LUT_E1 - LUT_E0) * 128)
#define LUT_N (2 * LUT_HALF)

__device__ __forceinline__ float mish_bf(float h) {
  float e = bfr(expf(-fabsf(h)));
  float l = bfr(log1pf(e));
  float sp = bfr(l + (h > 0.f ? h : 0.f));
  float th = bfr(tanhf(sp));
  return bfr(h * th);
}

#define KRLEN 2080
#define WFENCE() do { asm volatile("" ::: "memory"); __builtin_amdgcn_wave_barrier(); asm volatile("" ::: "memory"); } while (0)
__device__ __forceinline__ void cexp_p(float arl2, float w, float p, float& re, float& im) {
  float mag = __builtin_amdgcn_exp2f(arl2 * p);
  float rev = w * p;
  rev = rev - floorf(rev);
  re = mag * __builtin_amdgcn_cosf(rev);
  im = mag * __builtin_amdgcn_sinf(rev);
}
__device__ __forceinline__ void cmul(float ar, float ai, float br, float bi, float& cr, float& ci) {
  float r = fmaf(ar, br, -(ai * bi));
  float i = fmaf(ar, bi, ai * br);
  cr = r; ci = i;
}
__device__ __forceinline__ u32x2 split2(float a, float b) {
  u32 h = pk_bf(a, b);
  u32 l = pk_bf(a - lo_f(h), b - hi_f(h));
  return (u32x2){h, l};
}

#define CODE_TOUCH(sym0, sym1, line, d0) do { \
    unsigned long long pc_; int o0_, o1_; \
    asm volatile("s_getpc_b64 %0\n.Lpc_here%=:\n\ts_mov_b32 %1, " sym0 "-.Lpc_here%=\n\ts_mov_b32 %2, " sym1 "-.Lpc_here%=" \
                 : "=s"(pc_), "=s"(o0_), "=s"(o1_)); \
    int len_ = o1_ - o0_; \
    if (len_ < 4 || len_ > 65536) { len_ = 4; o0_ = 0; } \
    const char* cb_ = (const char*)(pc_ + (long long)o0_); \
    const int oa_ = (line) * 128 < len_ - 4 ? (line) * 128 : len_ - 4; \
    asm volatile("global_load_dword %0, %1, %2" : "=v"(d0) : "v"(oa_ & ~3), "s"(cb_) : "memory"); \
  } while (0)

#define WS_XNP 0u
#define WS_W1P (4u << 20)
#define WS_STATS ((12u << 20) + 4096)
#define WS_LUT ((12u << 20) + 32768)
#define WS_B1F ((12u << 20) + 65536)
#define WS_FLAGS ((12u << 20) + 98304)
#define WS_YT (13u << 20)
#define WS_ACTF (16u << 20)
#define WS_ACTH (22u << 20)
#define WS_W2F (24u << 20)
#define WS_W2H (26u << 20)
#define WS_PG (28u << 20)
#define K1_LN 256
#define K1_KG 640
#define K1_RP 135
#define K1_RQ 160
#define PPITCH 768
#define K1_LUT 32
#define WPITCH 544
typedef _Float16 h2_t __attribute__((ext_vector_type(2)));
__device__ __forceinline__ u32 pk_h(float a, float b) {
  f2_t v = {a, b};
  h2_t r = __builtin_convertvector(v, h2_t);
  return __builtin_bit_cast(u32, r);
}
#define TRREAD(p) __builtin_amdgcn_ds_read_tr16_b64_v4i16((__attribute__((address_space(3))) s4*)(p))

__device__ __forceinline__ void repack_load(const u16* __restrict__ w, int N, int ku, int nu, char* lds, int tl) {
#pragma unroll
  for (int i = 0; i < 2; ++i) {
    int idx = tl + 256 * i, r = idx >> 3, c = idx & 7;
    u32x4 v = *(const u32x4*)(w + (size_t)(ku * 64 + r) * N + nu * 64 + c * 8);
    *(u32x4*)(lds + r * 160 + c * 16) = v;
  }
}
__device__ __forceinline__ void repack_store(u16* __restrict__ wP, int KB, int ku, int nu, const char* lds, int tl) {
  const int wl = tl >> 6, lane = tl & 63, g = lane >> 4, tq = (lane & 15) >> 2, tp = lane & 3;
#pragma unroll
  for (int kb2 = 0; kb2 < 2; ++kb2) {
    const char* p0 = lds + (kb2 * 32 + 4 * g + tq) * 160 + wl * 32 + tp * 8;
    s4 b0 = TRREAD(p0);
    s4 b1 = TRREAD(p0 + 16 * 160);
    s8 bf = {b0[0], b0[1], b0[2], b0[3], b1[0], b1[1], b1[2], b1[3]};
    st16(wP + (((size_t)(nu * 4 + wl) * KB + ku * 2 + kb2) * 64 + lane) * 8, __builtin_bit_cast(u32x4, bf));
  }
}

extern "C" __global__ __launch_bounds__(256) void k1_kernel(const u16* __restrict__ u, u16* __restrict__ xnP,
                                                 float2* __restrict__ stats, u16* __restrict__ lut,
                                                 const u16* __restrict__ w1, u16* __restrict__ w1P,
                                                 const u16* __restrict__ w2, u16* __restrict__ w2F, u16* __restrict__ w2H,
                                                 const u16* __restrict__ b1, float* __restrict__ b1f,
                                                 const u16* __restrict__ lar, const u16* __restrict__ aim,
                                                 const u16* __restrict__ cre, const u16* __restrict__ cim,
                                                 const u16* __restrict__ logdt, u16* __restrict__ Pg, u32* __restrict__ flags,
                                                 const u16* outp, const u16* dskip) {
  __shared__ __attribute__((aligned(16))) char k1s[57 * WPITCH];
  const int t = threadIdx.x, lane = t & 63;
  const int wave = __builtin_amdgcn_readfirstlane(t >> 6);
  STAMPB(0, xnP);
  STAMP(0);
#define CT1 32
  u32 tc = 0;
  if (__builtin_expect(blockIdx.x < K1_LN, 1)) {
    if (t < 96 ? blockIdx.x < CT1 : t < 112) CODE_TOUCH("k1_kernel", "k4_end", t < 96 ? t : (int)(blockIdx.x >> 3) * 16 + t - 16, tc);
#define TT1 16
    if (blockIdx.x < TT1 && t >= 192 && t < 210) {
      const int l = t - 192;
      const u16* pb = l < 6 ? w1 : (l < 12 ? w2 : (l == 12 ? lar : (l == 13 ? aim : (l == 14 ? cre : (l == 15 ? cim : (l == 16 ? logdt : b1))))));
      const char* pa = (const char*)pb + (l < 6 ? l : (l < 12 ? l - 6 : 0)) * 442364;
      asm volatile("global_load_ushort %0, %1, off" : "=v"(tc) : "v"(pa) : "memory");
    }
  }
  if (blockIdx.x < K1_LN) {
    u16 (*tile)[DM + 8] = (u16 (*)[DM + 8])k1s;
    const int r = wave * 2 + (lane >> 5), q = lane & 31;
    const int rg = blockIdx.x;
    const int row = rg * 8 + r;
    float v[20];
    const u32x2* src = (const u32x2*)(u + (size_t)row * DM);
#pragma unroll
    for (int i = 0; i < 5; ++i) {
      u32x2 w = src[q + 32 * i];
      v[i * 4 + 0] = lo_f(w[0]); v[i * 4 + 1] = hi_f(w[0]);
      v[i * 4 + 2] = lo_f(w[1]); v[i * 4 + 3] = hi_f(w[1]);
    }
    float s = 0.f;
#pragma unroll
    for (int i = 0; i < 20; ++i) s += v[i];
    STAMP(1);
#pragma unroll
    for (int o = 1; o < 32; o <<= 1) s += __shfl_xor(s, o);
    const float mean = s * (1.0f / DM);
    float ss = 0.f;
#pragma unroll
    for (int i = 0; i < 20; ++i) { float d = v[i] - mean; ss += d * d; }
#pragma unroll
    for (int o = 1; o < 32; o <<= 1) ss += __shfl_xor(ss, o);
    const float var = ss * (1.0f / DM);
    const float mb = bfr(mean);
    const float vb = bfr(var);
    const float den = bfr(sqrtf(bfr(vb + 1e-5f)));
    const float rden = 1.0f / den;
    if (q == 0) st8(stats + row, (u32x2){__float_as_uint(mb), __float_as_uint(rden)});
#pragma unroll
    for (int i = 0; i < 5; ++i) {
      u32 d0 = pk_bf(v[i * 4 + 0] - mb, v[i * 4 + 1] - mb);
      u32 d1 = pk_bf(v[i * 4 + 2] - mb, v[i * 4 + 3] - mb);
      u32x2 w = {pk_bf(lo_f(d0) * rden, hi_f(d0) * rden), pk_bf(lo_f(d1) * rden, hi_f(d1) * rden)};
      *(u32x2*)&tile[r][(q + 32 * i) * 4] = w;
    }
    __syncthreads();
    asm volatile("" :: "v"(tc));
    STAMP(2);
    const int mt = rg >> 1, half = rg & 1;
#pragma unroll
    for (int i = 0; i < 3; ++i) {
      int idx = t + 256 * i;
      if (idx < 640) {
        int kb = idx >> 5, g = (idx >> 3) & 3, fr = idx & 7;
        u32x2 a = *(const u32x2*)&tile[fr][kb * 32 + 4 * g];
        u32x2 b = *(const u32x2*)&tile[fr][kb * 32 + 16 + 4 * g];
        u32x4 w = {a[0], a[1], b[0], b[1]};
        st16(xnP + (((size_t)mt * 20 + kb) * 64 + g * 16 + half * 8 + fr) * 8, w);
      }
    }
    STAMP(3);
    STAMPW();
    return;
  }
  if (blockIdx.x < K1_LN + K1_KG) {
    const int d = blockIdx.x - K1_LN;
    const int n = t & 127;
    char* shm = k1s;
    const u16 r_dt = logdt[d];
    const u16 r_ar = lar[d * NS + n], r_ai = aim[d * NS + n];
    const u16 r_cr = cre[d * NS + n], r_ci = cim[d * NS + n];
    const u16 r_cr2 = cre[d * NS + (n ^ 64)], r_ci2 = cim[d * NS + (n ^ 64)];
    const float dt = bfr(expf(bf2f(r_dt)));
    const float ar = -bfr(expf(bf2f(r_ar))) * dt;
    const float ai = bf2f(r_ai) * dt;
    float cr = bf2f(r_cr) * dt, ci = bf2f(r_ci) * dt;
    float bs = (fabsf(cr) + fabsf(ci)) + (fabsf(bf2f(r_cr2) * dt) + fabsf(bf2f(r_ci2) * dt));
#pragma unroll
    for (int o = 1; o < 64; o <<= 1) bs += __shfl_xor(bs, o);
    const float bound = 2.0f * bs;
    int ex = 0;
    if (bound > 0.f && bound < 3.0e38f) { (void)frexpf(bound, &ex); }
    int es = 15 - ex;
    es = es > 100 ? 100 : (es < -100 ? -100 : es);
    const float sc2 = ldexpf(2.0f, es);
    const float kscale = ldexpf(1.0f, -es);
    cr *= sc2; ci *= sc2;
    const float arl2 = ar * 1.4426950408889634f;
    const float w = ai * 0.15915494309189535f;
    STAMP(1);
    char* wr = shm + n * 4;
    float eHr, eHi;
    cexp_p(arl2, w, 128.0f, eHr, eHi);
    if (wave < 2) {
      float e8r, e8i, e4r, e4i, gr, gi;
      cexp_p(arl2, w, 8.0f, e8r, e8i);
      cexp_p(arl2, w, 4.0f, e4r, e4i);
      cexp_p(arl2, w, 512.0f, gr, gi);
      cmul(gr, gi, cr, ci, gr, gi);
      float lr = 1.f, lim = 0.f;
      *(u32*)(wr) = pk_h(1.f, 0.f);
#pragma unroll
      for (int j = 1; j < 16; ++j) {
        cmul(lr, lim, e8r, e8i, lr, lim);
        *(u32*)(wr + j * WPITCH) = pk_h(lr, -lim);
        if (j < 5) {
          float hr, hi;
          *(u32*)(wr + (40 + (j - 1) * 2) * WPITCH) = pk_h(gr, gi);
          cmul(gr, gi, e4r, e4i, hr, hi);
          *(u32*)(wr + (41 + (j - 1) * 2) * WPITCH) = pk_h(hr, hi);
          cmul(gr, gi, eHr, eHi, gr, gi);
        } else if (j < 14) {
          *(u32*)(wr + (48 + j - 5) * WPITCH) = pk_h(gr, gi);
          if (j < 13) cmul(gr, gi, eHr, eHi, gr, gi);
        }
      }
    } else {
      float Rr[8], Ri[8];
      Rr[0] = 1.f; Ri[0] = 0.f;
      cexp_p(arl2, w, 1.0f, Rr[1], Ri[1]);
#pragma unroll
      for (int r = 2; r < 8; ++r) cmul(Rr[r - 1], Ri[r - 1], Rr[1], Ri[1], Rr[r], Ri[r]);
      float gr = cr, gi = ci;
#pragma unroll
      for (int ii = 0; ii < 2; ++ii) {
        *(u32*)(wr + (16 + ii * 8) * WPITCH) = pk_h(gr, gi);
#pragma unroll
        for (int r = 1; r < 8; ++r) {
          float hr, hi;
          cmul(gr, gi, Rr[r], Ri[r], hr, hi);
          *(u32*)(wr + (16 + ii * 8 + r) * WPITCH) = pk_h(hr, hi);
        }
        cmul(gr, gi, eHr, eHi, gr, gi);
      }
#pragma unroll
      for (int ii = 2; ii < 4; ++ii) {
        *(u32*)(wr + (32 + (ii - 2) * 4) * WPITCH) = pk_h(gr, gi);
#pragma unroll
        for (int r = 1; r < 4; ++r) {
          float hr, hi;
          cmul(gr, gi, Rr[2 * r], Ri[2 * r], hr, hi);
          *(u32*)(wr + (32 + (ii - 2) * 4 + r) * WPITCH) = pk_h(hr, hi);
        }
        if (ii < 3) cmul(gr, gi, eHr, eHi, gr, gi);
      }
    }
    __syncthreads();
    STAMP(2);
    const int b = lane & 15, g = lane >> 4;
    u16* P = Pg + (size_t)d * PPITCH;
    if (wave < 3) {
      const int rowa = 16 + 16 * wave + b;
      const char* ra = shm + (rowa > 56 ? 56 : rowa) * WPITCH + g * 16;
      const char* rb = shm + b * WPITCH + g * 16;
      f4 ka = (f4){0.f, 0.f, 0.f, 0.f};
#pragma unroll
      for (int k8 = 0; k8 < 8; ++k8) {
        h8 a = *(const h8*)(ra + k8 * 64);
        h8 bb = *(const h8*)(rb + k8 * 64);
        ka = __builtin_amdgcn_mfma_f32_16x16x32_f16(a, bb, ka, 0, 0, 0);
      }
      const u32 k01 = pk_h(ka[0], ka[1]), k23 = pk_h(ka[2], ka[3]);
      if (wave == 0) {
        st8(&P[(g >> 1) * 128 + (g & 1) * 4 + 8 * b], (u32x2){k01, k23});
      } else if (wave == 1) {
        const int i1 = g < 2 ? 256 + 64 * g + 4 * b : 384 + 64 * (g - 2) + 2 * b;
        st4(&P[i1], k01);
        st4(&P[i1 + (g < 2 ? 2 : 32)], k23);
      } else {
        const int i2 = 512 + 64 * g + b;
        if (g < 3) st2(&P[i2], k01);
        if (g < 2) {
          st2(&P[i2 + 16], k01 >> 16);
          st2(&P[i2 + 32], k23); st2(&P[i2 + 48], k23 >> 16);
        }
      }
    } else if (lane == 0) {
      st4(&P[704], __float_as_uint(kscale));
    }
    STAMP(3);
    STAMPW();
    return;
  }
  if (blockIdx.x < K1_LN + K1_KG + K1_RP) {
    const int u0 = (blockIdx.x - (K1_LN + K1_KG)) * 2;
    repack_load(w1, FF, u0 / 27, u0 % 27, k1s, t);
    repack_load(w1, FF, (u0 + 1) / 27, (u0 + 1) % 27, k1s + 10240, t);
    __syncthreads();
    repack_store(w1P, DM / 32, u0 / 27, u0 % 27, k1s, t);
    repack_store(w1P, DM / 32, (u0 + 1) / 27, (u0 + 1) % 27, k1s + 10240, t);
    STAMP(3);
    STAMPW();
    return;
  }
  if (blockIdx.x < K1_LN + K1_KG + K1_RP + K1_RQ) {
    const int un = blockIdx.x - (K1_LN + K1_KG + K1_RP);
    const int bn = un / 10, nu = un % 10;
    const int f0 = 16 * (27 * (bn >> 2) + 7 * (bn & 3));
    const int rows = (bn & 3) == 3 ? 96 : 112;
    u32x4 v[4];
#pragma unroll
    for (int i = 0; i < 4; ++i) {
      const int r = (t >> 3) + 32 * i;
      v[i] = *(const u32x4*)(w2 + (size_t)(f0 + (r < rows ? r : rows - 1)) * DM + nu * 64 + (t & 7) * 8);
    }
#pragma unroll
    for (int i = 0; i < 4; ++i) *(u32x4*)(k1s + ((t >> 3) + 32 * i) * 160 + (t & 7) * 16) = v[i];
    __syncthreads();
    const int g = lane >> 4, tq = (lane & 15) >> 2, tp = lane & 3;
    const int nt = nu * 4 + wave;
    const char* p0 = k1s + (4 * g + tq) * 160 + wave * 32 + tp * 8;
#pragma unroll
    for (int p = 0; p < 3; ++p) {
      s4 b0 = TRREAD(p0 + (32 * p) * 160);
      s4 b1v = TRREAD(p0 + (32 * p + 16) * 160);
      s8 bf = {b0[0], b0[1], b0[2], b0[3], b1v[0], b1v[1], b1v[2], b1v[3]};
      st16(w2F + (((size_t)nt * 48 + 3 * bn + p) * 64 + lane) * 8, __builtin_bit_cast(u32x4, bf));
    }
    if (rows == 112) {
      s4 b0 = TRREAD(p0 + 96 * 160);
      st8(w2H + (((size_t)nt * 12 + 3 * (bn >> 2) + (bn & 3)) * 64 + lane) * 4, __builtin_bit_cast(u32x2, b0));
    }
    STAMP(3);
    STAMPW();
    return;
  }
  {
    const int idx = (blockIdx.x - (K1_LN + K1_KG + K1_RP + K1_RQ)) * 256 + t;
    const int sgn = idx >= 4096, i = idx - sgn * 4096;
    u32 td = 0;
    if (t < 72) {
      const char* pa = t < 60 ? (const char*)xnP + (size_t)t * (512u << 10)
                              : (t < 66 ? (const char*)outp + (t - 60) * 524284 : (const char*)dskip);
      asm volatile("global_load_dword %0, %1, off" : "=v"(td) : "v"(pa) : "memory");
    }
    if (idx < FF) st4(b1f + idx, (u32)b1[idx] << 16);
    if (idx >= 2048 && idx < 4096) st4(flags + (idx - 2048), 0u);
    u32 r = 0;
    if (i < LUT_HALF) r = f2bf(mish_bf(bf2f((u16)((i + (LUT_E0 << 7)) | (sgn << 15)))));
    st2(lut + idx, r);
    asm volatile("s_waitcnt vmcnt(0)" :: "v"(td) : "memory");
  }
}

#define K2_G1 256
#define K2_SSM 240
#define KCPITCH 2080
#define SB0() __builtin_amdgcn_sched_barrier(0)
template <int V> struct IC { static constexpr int value = V; };
template <int I, int N, class F> __device__ __forceinline__ void static_for_i(F&& f) {
  if constexpr (I < N) { f(IC<I>{}); static_for_i<I + 1, N>(f); }
}
template <int N, class F> __device__ __forceinline__ void static_for(F&& f) { static_for_i<0, N>(f); }
__device__ __forceinline__ void glds16s(u32 voff, const void* sbase, const void* lp) {
  unsigned keep;
  unsigned lds_dst = (unsigned)(size_t)(const __attribute__((address_space(3))) char*)lp;
  asm volatile("s_mov_b32 %0, m0\n\ts_mov_b32 m0, %3\n\ts_nop 0\n\tglobal_load_lds_dwordx4 %1, %2\n\ts_mov_b32 m0, %0"
               : "=&s"(keep) : "v"(voff), "s"(sbase), "s"(lds_dst) : "memory");
}
#define MFMA_BF(acc, a, b) \
  asm volatile("v_mfma_f32_16x16x32_bf16 %0, %1, %2, %0" : "+v"(acc) : "v"(a), "v"(b))
#define MFMA_BF0(acc, a, b) \
  asm volatile("v_mfma_f32_16x16x32_bf16 %0, %1, %2, 0" : "=&v"(acc) : "v"(a), "v"(b))
#define GLOAD4(dst, voff, sbase) \
  asm volatile("global_load_dwordx4 %0, %1, %2" : "=v"(dst) : "v"(voff), "s"(sbase) : "memory")
#define DSR64(dst, addr, off) asm volatile("ds_read_b64 %0, %1 offset:%2" : "=v"(dst) : "v"(addr), "n"(off) : "memory")
#define DSR128(dst, addr, off) asm volatile("ds_read_b128 %0, %1 offset:%2" : "=v"(dst) : "v"(addr), "n"(off) : "memory")

#define NT1 4
#define G1_D 4
#define LUT_NEG 65536
#define BIAS_OFF 78848
constexpr int g1_chunk(int slot, int w) { return (slot == 2 && w == 3) ? 73728 : 8192 + (slot * 4 + w) * 5120; }
constexpr int g1_piece_lds(int p) {
  return p < 20 * NT1 ? g1_chunk((p / 20) % 3, p % 4) + ((p % 20) / 4) * 1024
                      : g1_chunk((p / 20) % 3, (p % 20) / 5) + (p % 5) * 1024;
}
constexpr int g1_tile(int p) { return p < 20 * NT1 ? p % NT1 : p / 20; }
constexpr int g1_kt(int p) { return p < 20 * NT1 ? p / NT1 : p % 20; }
constexpr int g1_fin(int T) { return T < NT1 ? 19 * NT1 + T : 20 * T + 19; }
constexpr int g1_vmB(int p) { return (p & 1) && p >= 17 && p < 86 && (p - 17) % 20 < 10 ? ((p - 17) / 20) * 5 + ((p - 17) % 20) / 2 : -1; }
constexpr int g1_vmL(int p) { return (p == 47 || p == 49 || p == 51 || p == 53) ? (p - 47) / 2 : -1; }
struct G1S { int vA[140], vS[140], lB[140], ws[24], cs[24], sp[3]; };
constexpr G1S g1_sched() {
  G1S S{};
  for (int w = 0; w < 24; ++w) {
    const int e = g1_fin(w >> 2) + 2;
    S.ws[w] = (w && S.ws[w - 1] + 2 > e) ? S.ws[w - 1] + 2 : e;
  }
  for (int w = 0; w < 24; ++w) S.cs[w] = S.ws[w] + G1_D + 2;
  for (int r = 0; r < 3; ++r) S.sp[r] = S.cs[8 * r + 7] + 1;
  int idxA[20] = {}, idxB[7] = {}, idxL = 0, ldB[140] = {};
  int v = 1;
  for (int h = 0; h < 3; ++h) {
    v += 5; idxB[h] = v;
    if (h < 2) for (int k = 0; k < 5; ++k) { v += 2; idxA[5 * h + k] = v; }
  }
  int l = NT1;
  for (int q = 0; q < G1_D; ++q) ldB[q] = ++l;
  for (int q = 0; q < 140; ++q) {
    const int gq = q / 20, lp = q % 20;
    S.vA[q] = -1; S.vS[q] = -1;
    if (lp == 20 - G1_D && gq < 6) {
      int need = idxB[gq + 1];
      if (gq + 1 == NT1 && idxL > need) need = idxL;
      S.vS[q] = v - need;
    }
    for (int r = 0; r < 3; ++r) if (q == S.sp[r]) v += 2;
    if (q < 20 * NT1 && q % NT1 == 0) S.vA[q] = v - idxA[q / NT1];
    if (q < 40 && (q & 1) == 0) { v += 1; idxA[10 + q / 4] = v; }
    if (g1_vmB(q) >= 0) { v += 1; idxB[3 + g1_vmB(q) / 5] = v; }
    if (g1_vmL(q) >= 0) { v += 1; idxL = v; }
    if (q + G1_D < 140) ldB[q + G1_D] = ++l;
    if (lp == 12 && gq + 1 >= NT1 && gq < 6) ++l;
    S.lB[q] = l - ldB[q];
    for (int w = 0; w < 24; ++w) if (S.ws[w] + 1 == q) l += 2;
  }
  return S;
}
constexpr G1S G1 = g1_sched();
constexpr int g1_word(int p) { for (int w = 0; w < 24; ++w) if (p >= G1.ws[w] && p < G1.ws[w] + 2) return w; return -1; }
constexpr int g1_comb(int p) { for (int w = 0; w < 24; ++w) if (p == G1.cs[w]) return w; return -1; }
static_assert(G1.sp[2] < 140 && G1.ws[0] >= 20 * NT1 - G1_D, "schedule");
#define WAIT_L(n) __builtin_amdgcn_s_waitcnt(0xC07F | (((n) > 15 ? 15 : (n)) << 8))
#define MF_ "v_mfma_f32_16x16x32_bf16 "
#define SDWA_(h) " dst_sel:DWORD dst_unused:UNUSED_PAD src0_sel:DWORD src1_sel:WORD_" #h "\n\t"
template <bool F> __device__ __forceinline__ void step_p(f4& c0, f4& c1, const u32x4& b, const u32x4& x0, const u32x4& x1, const f4& bs) {
  if constexpr (F) asm volatile(MF_ "%0, %2, %3, %5\n\t" MF_ "%1, %2, %4, %5" : "=&v"(c0), "=&v"(c1) : "v"(b), "v"(x0), "v"(x1), "v"(bs));
  else asm volatile(MF_ "%0, %2, %3, %0\n\t" MF_ "%1, %2, %4, %1" : "+v"(c0), "+v"(c1) : "v"(b), "v"(x0), "v"(x1));
}
template <bool F> __device__ __forceinline__ void step_x(f4& c0, f4& c1, const u32x4& b, const u32x4& x0, const u32x4& x1, const f4& bs,
                                                         u32& w, u32& z, u32& mx, float e0, float e1, u32 a0, u32 a1, u32 kb) {
#define X_MID "v_cvt_pk_bf16_f32 %2, %8, %9\n\tv_pk_add_u16 %3, %2, %12\n\t"
#define X_END "\n\tv_max3_u16 %4, %4, %10, %11"
  if constexpr (F) asm volatile(MF_ "%0, %5, %6, %13\n\t" X_MID MF_ "%1, %5, %7, %13" X_END
                                : "=&v"(c0), "=&v"(c1), "=&v"(w), "=&v"(z), "+v"(mx)
                                : "v"(b), "v"(x0), "v"(x1), "v"(e0), "v"(e1), "v"(a0), "v"(a1), "s"(kb), "v"(bs));
  else asm volatile(MF_ "%0, %5, %6, %0\n\t" X_MID MF_ "%1, %5, %7, %1" X_END
                    : "+v"(c0), "+v"(c1), "=&v"(w), "=&v"(z), "+v"(mx)
                    : "v"(b), "v"(x0), "v"(x1), "v"(e0), "v"(e1), "v"(a0), "v"(a1), "s"(kb));
}
template <bool F> __device__ __forceinline__ void step_y(f4& c0, f4& c1, const u32x4& b, const u32x4& x0, const u32x4& x1, const f4& bs,
                                                         u32& a0, u32& a1, u32& r0, u32& r1, u32 z) {
#define Y_MID "v_lshlrev_b32_sdwa %2, 1, %9" SDWA_(0) "v_lshlrev_b32_sdwa %3, 1, %9" SDWA_(1)
#define Y_END "\n\tds_read_u16 %4, %2\n\tds_read_u16 %5, %3"
  if constexpr (F) asm volatile(MF_ "%0, %6, %7, %10\n\t" Y_MID MF_ "%1, %6, %8, %10" Y_END
                                : "=&v"(c0), "=&v"(c1), "=&v"(a0), "=&v"(a1), "=&v"(r0), "=&v"(r1)
                                : "v"(b), "v"(x0), "v"(x1), "v"(z), "v"(bs) : "memory");
  else asm volatile(MF_ "%0, %6, %7, %0\n\t" Y_MID MF_ "%1, %6, %8, %1" Y_END
                    : "+v"(c0), "+v"(c1), "=&v"(a0), "=&v"(a1), "=&v"(r0), "=&v"(r1)
                    : "v"(b), "v"(x0), "v"(x1), "v"(z) : "memory");
}
__device__ __forceinline__ void comb(u32& r, u32 hi) { asm volatile("v_lshl_or_b32 %0, %1, 16, %0" : "+v"(r) : "v"(hi)); }
__device__ __forceinline__ void max3u(u32& mx, u32 a0, u32 a1) { asm volatile("v_max3_u16 %0, %0, %1, %2" : "+v"(mx) : "v"(a0), "v"(a1)); }
__device__ __forceinline__ void epi_word(u32& w, u32& r0, u32& r1, u32& mx, float e0, float e1, u32 kb) {
  u32 z;
  asm volatile("v_cvt_pk_bf16_f32 %0, %5, %6\n\tv_pk_add_u16 %4, %0, %7\n\t"
               "v_lshlrev_b32_sdwa %1, 1, %4" SDWA_(0) "v_lshlrev_b32_sdwa %2, 1, %4" SDWA_(1)
               "v_max3_u16 %3, %3, %1, %2\n\tds_read_u16 %1, %1\n\tds_read_u16 %2, %2"
               : "=&v"(w), "=&v"(r0), "=&v"(r1), "+v"(mx), "=&v"(z) : "v"(e0), "v"(e1), "s"(kb) : "memory");
}
__device__ __forceinline__ void mfma_c(f4& acc, const u32x4& a, const u32x4& b, const f4& c) {
  asm volatile("v_mfma_f32_16x16x32_bf16 %0, %1, %2, %3" : "=&v"(acc) : "v"(a), "v"(b), "v"(c));
}
__device__ __forceinline__ void mfma_a(f4& acc, const u32x4& a, const u32x4& b) { MFMA_BF(acc, a, b); }
template <int OFF, class V> __device__ __forceinline__ void dsr128(V& d, u32 addr) { DSR128(d, addr, OFF); }
__device__ __forceinline__ void pin2(u32x4& a, u32x4& b) { asm volatile("" : "+v"(a), "+v"(b)); }
template <int N> __device__ __forceinline__ void wait_res(u32 (&a)[4], u32 (&b)[4]) {
  asm volatile("s_waitcnt lgkmcnt(%8)"
               : "+v"(a[0]), "+v"(a[1]), "+v"(a[2]), "+v"(a[3]), "+v"(b[0]), "+v"(b[1]), "+v"(b[2]), "+v"(b[3])
               : "n"(N > 15 ? 15 : N) : "memory");
}
template <int OFF> __device__ __forceinline__ void glds_o(u32 voff, const void* sbase, u32 m) {
  asm volatile("s_mov_b32 m0, %2\n\ts_nop 0\n\tglobal_load_lds_dwordx4 %0, %1 offset:%3"
               :: "v"(voff), "s"(sbase), "s"(m), "n"(OFF) : "memory", "m0");
}
__device__ __forceinline__ void gload4(u32x4& d, u32 voff, const void* sbase) { GLOAD4(d, voff, sbase); }
__device__ __forceinline__ void st16s(u32 voff, const void* sbase, u32x4 v) {
  asm volatile("global_store_dwordx4 %0, %1, %2" STF :: "v"(voff), "v"(v), "s"(sbase) : "memory");
}

template <int KID>
__device__ __forceinline__ void ssm_block(char* shm, const int grp, const int e0, const int nch, const u16* __restrict__ xnP,
                                          const u16* __restrict__ Pg, u16* __restrict__ yT, u32* flag) {
    const int t = threadIdx.x, lane = t & 63;
    const int wave = __builtin_amdgcn_readfirstlane(t >> 6);
    STAMPB(KID, (char*)yT - (13u << 20));
    STAMP(0);
    const int eo = (e0 & ~1) > 4 ? 4 : (e0 & ~1);
    const int dbase = (grp >> 2) * 32 + (grp & 3) * 4;
    u16* Kc = (u16*)shm;
    float* red = (float*)(shm + 16896);
#pragma unroll
    for (int c = 0; c < 3; ++c) {
      if (c < nch) {
        const int e = e0 + c;
        const int d = dbase + (e >> 2) * 16 + (e & 3);
        char* buf = shm + 41472 + c * 6144;
        if (wave == 0) glds16s(lane * 16, (const char*)(Pg + (size_t)d * PPITCH), buf);
        if (wave == 1 && lane < 32) glds16s(lane * 16, (const char*)(Pg + (size_t)d * PPITCH) + 1024, buf + 1024);
      }
    }
    {
      u32x2 xv[8];
      const char* xp = (const char*)xnP + ((((size_t)(t >> 3) * 20 + (grp >> 2)) * 64 + (grp & 3) * 16 + ((2 * t) & 15)) * 16 + eo * 2);
#pragma unroll
      for (int i = 0; i < 4; ++i) {
        xv[2 * i] = *(const u32x2*)(xp + (size_t)i * 655360);
        xv[2 * i + 1] = *(const u32x2*)(xp + (size_t)i * 655360 + 16);
      }
#pragma unroll
      for (int c = 0; c < 3; ++c) {
        if (c < nch) {
          const u32 pos2 = 2 * (e0 + c - eo);
          const u32 sel = 0x00000c0cu | (pos2 << 16) | ((pos2 + 1) << 24);
          u16* xs = (u16*)(shm + 41472 + c * 6144 + 2048);
#pragma unroll
          for (int i = 0; i < 4; ++i) {
            const float x0 = __uint_as_float(__builtin_amdgcn_perm(xv[2 * i][1], xv[2 * i][0], sel));
            const float x1 = __uint_as_float(__builtin_amdgcn_perm(xv[2 * i + 1][1], xv[2 * i + 1][0], sel));
            *(u32*)&xs[2 * t + 512 * i] = pk_h(x0, x1);
          }
        }
      }
    }
    if (t < 96) *(u32x4*)(shm + 41472 + (t >> 5) * 6144 + 1536 + (t & 31) * 16) = (u32x4){0u, 0u, 0u, 0u};
    if (t < 8) *(u32x4*)&Kc[(t >> 1) * KCPITCH + (256 + (t & 1)) * 8] = (u32x4){0u, 0u, 0u, 0u};
    WAIT_V(0);
    RAW_BARRIER();
    STAMP(3);
    const int b = lane & 15, g = lane >> 4;
    const int m = 255 - t;
    const u32 lds0 = (u32)(size_t)(const __attribute__((address_space(3))) char*)shm;
    const int abase = ((3 - b) & 3) * KCPITCH + 2028 - 4 * (b >> 2) + 8 * g;
    const int bbase = 256 - 16 + 16 * b + 8 * g;
#pragma unroll 1
    for (int c = 0; c < nch; ++c) {
      const int e = e0 + c;
      const int d = dbase + (e >> 2) * 16 + (e & 3);
      const u16* P = (const u16*)(shm + 41472 + c * 6144);
      const float kscale = *(const float*)&P[704];
      {
        u32 wv[6];
        if (wave < 2) {
          const int i = 512 + m - 128;
          const u32 pc = P[i - 1], pa = P[i], pb = P[i + 1];
          const u32 aa = pa | (pa << 16);
          wv[0] = pb | (pb << 16);
          wv[1] = pb | (((m & 1) ? pb : pa) << 16);
          wv[2] = aa; wv[3] = aa; wv[4] = aa;
          wv[5] = m == 128 ? pc : pa;
        } else if (wave == 2) {
          const int i = 384 + 2 * (m - 64);
          const u32 q0 = *(const u32*)&P[i - 2], q1 = *(const u32*)&P[i], q2 = *(const u32*)&P[i + 2];
          wv[0] = __builtin_amdgcn_perm(q2, q2, 0x01000100u);
          wv[1] = __builtin_amdgcn_perm(q1, q1, 0x03020302u);
          wv[2] = __builtin_amdgcn_perm(q1, q1, 0x01000302u);
          wv[3] = __builtin_amdgcn_perm(q1, q1, 0x01000100u);
          wv[4] = m == 64 ? __builtin_amdgcn_perm(q0, q1, 0x07060100u) : wv[3];
          wv[5] = m == 64 ? q0 : (q0 >> 16);
        } else {
#pragma unroll
          for (int hh = 0; hh < 2; ++hh) {
            const int mm = m - hh;
            const int sg = mm >= 32 ? 1 : 0;
#pragma unroll
            for (int j = 0; j < (hh ? 2 : 4); ++j) {
              u32 pk = 0;
#pragma unroll
              for (int h = 0; h < 2; ++h) {
                if (hh == 0 || 2 * j + h < 3) {
                  int q = 8 * mm + 7 - (2 * j + h);
                  q = q < 0 ? 0 : q;
                  int idx = sg ? 128 + ((q + ((q >> 1) & 1)) >> 1) : q;
                  pk |= ((u32)P[idx]) << (16 * h);
                }
              }
              wv[hh * 4 + j] = (hh && mm < 0) ? 0u : pk;
            }
          }
        }
        *(u32x4*)&Kc[0 * KCPITCH + t * 8] = (u32x4){wv[0], wv[1], wv[2], wv[3]};
        *(u32x4*)&Kc[1 * KCPITCH + t * 8] = (u32x4){__builtin_amdgcn_alignbit(wv[1], wv[0], 16), __builtin_amdgcn_alignbit(wv[2], wv[1], 16),
                                                    __builtin_amdgcn_alignbit(wv[3], wv[2], 16), __builtin_amdgcn_alignbit(wv[4], wv[3], 16)};
        *(u32x4*)&Kc[2 * KCPITCH + t * 8] = (u32x4){wv[1], wv[2], wv[3], wv[4]};
        *(u32x4*)&Kc[3 * KCPITCH + t * 8] = (u32x4){__builtin_amdgcn_alignbit(wv[2], wv[1], 16), __builtin_amdgcn_alignbit(wv[3], wv[2], 16),
                                                    __builtin_amdgcn_alignbit(wv[4], wv[3], 16), __builtin_amdgcn_alignbit(wv[5], wv[4], 16)};
      }
      __syncthreads();
      if (c == 0) { STAMP(4); }
      if (c == 2) { STAMP(9); }
      f4 acc[8];
#pragma unroll
      for (int i = 0; i < 8; ++i) acc[i] = (f4){0.f, 0.f, 0.f, 0.f};
      {
        u32x2 a0[2][8], a1[2][8];
        u32x4 bq[2][8];
        const u32 aaddr = lds0 + (u32)(abase - 64 * wave - 32 - 256 * 7) * 2;
        const u32 baddr = lds0 + 41472 + c * 6144 + 1536 + (u32)(bbase - 64 * wave - 32) * 2;
#pragma unroll
        for (int rr = 0; rr < 2; ++rr) {
#pragma unroll
          for (int u = 0; u < 8; ++u) {
            DSR64(a0[rr][u], aaddr, (7 - u) * 512 + (1 - rr) * 64);
            DSR64(a1[rr][u], aaddr, (7 - u) * 512 + (1 - rr) * 64 + 8);
            DSR128(bq[rr][u], baddr, u * 512 + (1 - rr) * 64);
            if (rr == 1 && u == 3) {
              asm volatile("s_waitcnt lgkmcnt(12)"
                           : "+v"(a0[0][0]), "+v"(a1[0][0]), "+v"(a0[0][1]), "+v"(a1[0][1]), "+v"(a0[0][2]), "+v"(a1[0][2]),
                             "+v"(a0[0][3]), "+v"(a1[0][3]), "+v"(a0[0][4]), "+v"(a1[0][4]), "+v"(a0[0][5]), "+v"(a1[0][5]),
                             "+v"(a0[0][6]), "+v"(a1[0][6]), "+v"(a0[0][7]), "+v"(a1[0][7]),
                             "+v"(bq[0][0]), "+v"(bq[0][1]), "+v"(bq[0][2]), "+v"(bq[0][3]),
                             "+v"(bq[0][4]), "+v"(bq[0][5]), "+v"(bq[0][6]), "+v"(bq[0][7])
                           :: "memory");
            }
          }
        }
        SB0();
#pragma unroll
        for (int rr = 0; rr < 2; ++rr) {
          if (rr == 1) {
            asm volatile("s_waitcnt lgkmcnt(0)"
                         : "+v"(a0[1][0]), "+v"(a1[1][0]), "+v"(a0[1][1]), "+v"(a1[1][1]), "+v"(a0[1][2]), "+v"(a1[1][2]),
                           "+v"(a0[1][3]), "+v"(a1[1][3]), "+v"(a0[1][4]), "+v"(a1[1][4]), "+v"(a0[1][5]), "+v"(a1[1][5]),
                           "+v"(a0[1][6]), "+v"(a1[1][6]), "+v"(a0[1][7]), "+v"(a1[1][7]),
                           "+v"(bq[1][0]), "+v"(bq[1][1]), "+v"(bq[1][2]), "+v"(bq[1][3]),
                           "+v"(bq[1][4]), "+v"(bq[1][5]), "+v"(bq[1][6]), "+v"(bq[1][7])
                         :: "memory");
          }
#pragma unroll
          for (int u = 0; u < 8; ++u) {
            const h8 A = __builtin_bit_cast(h8, (u32x4){a0[rr][u][0], a0[rr][u][1], a1[rr][u][0], a1[rr][u][1]});
#pragma unroll
            for (int v = 0; v + u < 8; ++v)
              acc[u + v] = __builtin_amdgcn_mfma_f32_16x16x32_f16(A, __builtin_bit_cast(h8, bq[rr][v]), acc[u + v], 0, 0, 0);
          }
          SB0();
        }
      }
      if (c == 0) { STAMP(5); }
      if (c == 2) { STAMP(10); }
      auto red_phase = [&](auto WC) {
        constexpr int W = decltype(WC)::value;
#pragma unroll
        for (int o = 1; o < 4; ++o) {
          const int ow = (W + o) & 3;
#pragma unroll
          for (int i = 0; i < 2; ++i) *(f4*)&red[(((ow * 3 + (o - 1)) * 2 + i) * 64 + lane) * 4] = acc[ow * 2 + i];
        }
        __syncthreads();
        u32x2 o[2];
#pragma unroll
        for (int i = 0; i < 2; ++i) {
          f4 s1 = *(const f4*)&red[(((W * 3 + 0) * 2 + i) * 64 + lane) * 4];
          f4 s2 = *(const f4*)&red[(((W * 3 + 1) * 2 + i) * 64 + lane) * 4];
          f4 s3 = *(const f4*)&red[(((W * 3 + 2) * 2 + i) * 64 + lane) * 4];
          f4 sum = ((acc[W * 2 + i] + s1) + (s2 + s3)) * kscale;
          o[i] = (u32x2){pk_bf(sum[0], sum[1]), pk_bf(sum[2], sum[3])};
        }
#pragma unroll
        for (int i = 0; i < 2; ++i) st8(yT + (size_t)d * SEQ + 256 * (W * 2 + i) + 16 * b + 4 * g, o[i]);
      };
      if (wave == 0) red_phase(IC<0>{});
      else if (wave == 1) red_phase(IC<1>{});
      else if (wave == 2) red_phase(IC<2>{});
      else red_phase(IC<3>{});
      if (c == 0) { STAMP(6); }
      if (c == 1) { STAMP(7); }
      if (c == 2) { STAMP(11); }
    }
    if (flag) {
      WAIT_V(0);
      if (lane == 0) st4(flag + wave * 256, 1u);
    }
    STAMP(8);
    STAMPW();
}

#define K3R 3
#define K3HB 1
#define K3NB 3
#define K3SLEEP1 8
#define K3YS 14
__device__ __forceinline__ void glds16c(const void* gsrc, const void* lp) {
  unsigned keep;
  unsigned lds_dst = __builtin_amdgcn_readfirstlane((unsigned)(size_t)(const __attribute__((address_space(3))) char*)lp);
  asm volatile("s_mov_b32 %0, m0\n\ts_mov_b32 m0, %2\n\ts_nop 0\n\tglobal_load_lds_dwordx4 %1, off sc1\n\ts_mov_b32 m0, %0"
               : "=&s"(keep) : "v"(gsrc), "s"(lds_dst) : "memory");
}
#define OUTF ""
__device__ __forceinline__ void so16(void* p, u32x4 v) { asm volatile("global_store_dwordx4 %0, %1, off" OUTF :: "v"(p), "v"(v) : "memory"); }
__device__ __forceinline__ void so8(void* p, u32x2 v) { asm volatile("global_store_dwordx2 %0, %1, off" OUTF :: "v"(p), "v"(v) : "memory"); }
__device__ __forceinline__ void g2_block(char* shm, const int bid, char* ws, const u16* __restrict__ xin,
                                         const u16* __restrict__ dskip, u16* __restrict__ out) {
  constexpr int R = K3R;
  constexpr int NB = K3NB;
  constexpr int NF = 12, NST = 15;
  const u16* w2F = (const u16*)(ws + WS_W2F);
  const u16* w2H = (const u16*)(ws + WS_W2H);
  const float2* stats = (const float2*)(ws + WS_STATS);
  const u16* yT = (const u16*)(ws + WS_YT);
  const u32* flags = (const u32*)(ws + WS_FLAGS);
  const int t = threadIdx.x, lane = t & 63;
  const int wave = __builtin_amdgcn_readfirstlane(t >> 6);
  const int xcd = bid & 7, idx = bid >> 3;
  const int mi = idx & 7, ni = idx >> 3;
  const int bm = (xcd >> 1) * 8 + mi, bn = (xcd & 1) * 4 + ni;
  const int mt0 = bm * 4, m0 = bm * 64, n0 = bn * 80, nt0 = bn * 5;
  STAMPB(3, ws);
  STAMP(0);
  const char* abase = ws + WS_ACTF + ((size_t)mt0 * 48 + wave) * 1024;
  const char* bbase = (const char*)w2F + ((size_t)nt0 * 48 + wave) * 1024;
  const char* ahb = ws + WS_ACTH + ((size_t)(mt0 >> 1) * 12 + wave) * 1024;
  const char* bhb = (const char*)w2H + ((size_t)nt0 * 12 + wave) * 512;
  u32 va[4], vb[5], vh[2];
  const __amdgpu_buffer_rsrc_t ra = __builtin_amdgcn_make_buffer_rsrc((void*)abase, 0, 4 * 48 * 1024, 0x00020000);
  const __amdgpu_buffer_rsrc_t rh = __builtin_amdgcn_make_buffer_rsrc((void*)ahb, 0, 2 * 12 * 1024, 0x00020000);
#define LDA(r, p, v, so) __builtin_bit_cast(u32x4, __builtin_amdgcn_raw_buffer_load_b128(r, v, so, 16))
#pragma unroll
  for (int j = 0; j < 5; ++j) vb[j] = lane * 16 + j * (48 * 1024);
  u32x4 fa[R][4], fb[R][5];
  u32x4 ha[3][2];
  u32x2 hb[3][5];
  auto issue = [&](int s, int what = 3) {
    if (s < NF) {
      const char* ap = abase + s * 4096;
      const char* bp = bbase + s * 4096;
      if (what & 1) {
#pragma unroll
        for (int i = 0; i < 4; ++i) fa[s % R][i] = LDA(ra, ap, va[i], s * 4096);
      }
      if (what & 2) {
        if (s >= R && s < R + NB) {
#pragma unroll
          for (int j = 0; j < 5; ++j) fb[s % R][j] = *(const u32x4*)(shm + 20480 + (((s - R) * 4 + wave) * 5 + j) * 1024 + lane * 16);
        } else {
#pragma unroll
          for (int j = 0; j < 5; ++j) fb[s % R][j] = *(const u32x4*)(bp + vb[j]);
        }
      }
    } else {
      const int h = s - NF;
#pragma unroll
      for (int i = 0; i < 2; ++i) ha[h][i] = LDA(rh, ahb + h * 4096, vh[i], h * 4096);
      if (h >= K3HB) {
#pragma unroll
        for (int j = 0; j < 5; ++j) hb[h][j] = *(const u32x2*)(bhb + h * 2048 + (lane * 8 + j * (12 * 512)));
      }
    }
  };
  auto aux = [&]() {
#pragma unroll
    for (int i = 0; i < 3; ++i) {
      int p = wave + 4 * i;
      if (p > 10) p = 10;
      const int c = p * 64 + lane;
      const u16* src;
      if (p < 10) src = xin + (size_t)(m0 + c / 10) * DM + n0 + (c % 10) * 8;
      else src = (lane < 32) ? (const u16*)(stats + m0) + lane * 8 : dskip + n0 + (lane < 42 ? lane - 32 : 9) * 8;
      GLDS(src, shm + p * 1024);
    }
  };
  auto yload = [&]() {
    const u32* fp = flags + (bm >> 3) * 256 + ((n0 >> 5) * 4) * 3 + (lane < 12 * 3 ? lane : 0);
#define K3SLEEP 16
    if (wave == 0)
    for (int it = 0; it < (1 << 20); ++it) {
      u32 f;
      asm volatile("global_load_dword %0, %1, off sc1\n\ts_waitcnt vmcnt(0)" : "=v"(f) : "v"(fp) : "memory");
      if (__builtin_amdgcn_ballot_w64(f != 1u) == 0) break;
      __builtin_amdgcn_s_sleep(K3SLEEP);
    }
    __builtin_amdgcn_s_barrier();
#pragma unroll
    for (int i = 0; i < 3; ++i) {
      int p = wave + 4 * i;
      if (p > 9) p = 9;
      const int c = p * 64 + lane;
      glds16c(yT + (size_t)(n0 + (c >> 3)) * SEQ + m0 + (c & 7) * 8, shm + (10 + p) * 1024);
    }
  };
  SB0();
  const u32* fp1 = flags + 1024 + bm * 32 + (lane & 31);
  u32 f1 = 0;
  SB0();
#pragma unroll
  for (int s = 0; s < R; ++s) { issue(s, 2); SB0(); }
#pragma unroll
  for (int h = 0; h < K3HB; ++h) {
#pragma unroll
    for (int j = 0; j < 5; ++j) hb[h][j] = *(const u32x2*)(bhb + h * 2048 + (lane * 8 + j * (12 * 512)));
    SB0();
  }
  aux();
  SB0();
  {
    const u32 lb0 = (u32)(size_t)(const __attribute__((address_space(3))) char*)shm + 20480 + wave * 5120;
#pragma unroll
    for (int s = 0; s < NB; ++s)
#pragma unroll
      for (int j = 0; j < 5; ++j) glds_o<0>(vb[j], bbase + (R + s) * 4096, lb0 + s * 20480 + j * 1024);
  }
  SB0();
  if (wave == 0) {
    asm volatile("global_load_dword %0, %1, off sc1\n\ts_waitcnt vmcnt(0)" : "=v"(f1) : "v"(fp1) : "memory");
    for (int it = 0; it < (1 << 20); ++it) {
      if (__builtin_amdgcn_ballot_w64(f1 != 1u) == 0) break;
      __builtin_amdgcn_s_sleep(K3SLEEP1);
      asm volatile("global_load_dword %0, %1, off sc1\n\ts_waitcnt vmcnt(0)" : "=v"(f1) : "v"(fp1) : "memory");
    }
  }
  __builtin_amdgcn_s_barrier();
  {
    u32 l16 = lane * 16;
    asm volatile("" : "+v"(l16) :: "memory");
#pragma unroll
    for (int i = 0; i < 4; ++i) va[i] = l16 + ((wave + i) & 3) * (48 * 1024);
#pragma unroll
    for (int i = 0; i < 2; ++i) vh[i] = l16 + (((wave >> 1) + i) & 1) * (12 * 1024);
  }
  STAMP(10);
  SB0();
#pragma unroll
  for (int s = 0; s < R; ++s) { issue(s, 1); SB0(); }
  STAMP(1);
  const int fr = lane & 15, g = lane >> 4, tq = (lane & 15) >> 2, tp = lane & 3;
  const int ml = wave * 16 + fr;
  u32x2 s1p[5];
  float2 st;
  auto resid = [&](int j) {
    const char* sx = shm;
    const char* sm = shm + 10 * 1024;
    if (j == 0) st = *(const float2*)(sm + ml * 8);
    const int nl = j * 16;
    u32x2 xx = *(const u32x2*)(sx + ml * 160 + (nl + 4 * g) * 2);
    u32x2 dd = *(const u32x2*)(sm + 512 + (nl + 4 * g) * 2);
#pragma unroll
    for (int h = 0; h < 2; ++h) {
      const float x0 = lo_f(xx[h]), x1 = hi_f(xx[h]);
      u32 d = pk_bf(x0 - st.x, x1 - st.x);
      u32 xn = pk_bf(lo_f(d) * st.y, hi_f(d) * st.y);
      s1p[j][h] = pk_bf(lo_f(xn) * lo_f(dd[h]), hi_f(xn) * hi_f(dd[h]));
    }
    asm volatile("" : "+v"(s1p[j]));
  };
  auto resid2 = [&](int j) {
    const char* sx = shm;
    const char* sy = shm + 10 * 1024;
    const int nl = j * 16;
    u32x2 xx = *(const u32x2*)(sx + ml * 160 + (nl + 4 * g) * 2);
    s4 yy = TRREAD(sy + (nl + 4 * g + tq) * 128 + (wave * 16 + 4 * tp) * 2);
    const u32x2 yw = __builtin_bit_cast(u32x2, yy);
#pragma unroll
    for (int h = 0; h < 2; ++h) {
      const float x0 = lo_f(xx[h]), x1 = hi_f(xx[h]);
      u32 a = pk_bf(lo_f(yw[h]) + lo_f(s1p[j][h]), hi_f(yw[h]) + hi_f(s1p[j][h]));
      s1p[j][h] = pk_bf(x0 + lo_f(a), x1 + hi_f(a));
    }
  };
  f4 acc[4][5];
#pragma unroll
  for (int i = 0; i < 4; ++i)
#pragma unroll
    for (int j = 0; j < 5; ++j) acc[i][j] = (f4){0.f, 0.f, 0.f, 0.f};
  const bool odd = wave & 1;
  SB0();
#pragma unroll
  for (int s = 0; s < NST; ++s) {
    if (s < NF) {
#pragma unroll
      for (int i = 0; i < 4; ++i)
#pragma unroll
        for (int j = 0; j < 5; ++j)
          acc[i][j] = __builtin_amdgcn_mfma_f32_16x16x32_bf16(__builtin_bit_cast(s8, fb[s % R][j]),
                                                              __builtin_bit_cast(s8, fa[s % R][i]), acc[i][j], 0, 0, 0);
    } else {
      const int h = s - NF;
      u32x2 af[4];
      af[0] = odd ? (u32x2){ha[h][0][2], ha[h][0][3]} : (u32x2){ha[h][0][0], ha[h][0][1]};
      af[1] = odd ? (u32x2){ha[h][1][0], ha[h][1][1]} : (u32x2){ha[h][0][2], ha[h][0][3]};
      af[2] = odd ? (u32x2){ha[h][1][2], ha[h][1][3]} : (u32x2){ha[h][1][0], ha[h][1][1]};
      af[3] = odd ? (u32x2){ha[h][0][0], ha[h][0][1]} : (u32x2){ha[h][1][2], ha[h][1][3]};
#pragma unroll
      for (int i = 0; i < 4; ++i)
#pragma unroll
        for (int j = 0; j < 5; ++j)
          acc[i][j] = __builtin_amdgcn_mfma_f32_16x16x16bf16_1k(__builtin_bit_cast(s4, hb[h][j]),
                                                               __builtin_bit_cast(s4, af[i]), acc[i][j], 0, 0, 0);
    }
    SB0();
    if (s + R < NST) issue(s + R);
    SB0();
    if (s == 4) {
      WAIT_V(R * 9 > 60 ? 60 : R * 9);
      __builtin_amdgcn_s_barrier();
      SB0();
    }
    if (s >= 5 && s < 10) { resid(s - 5); SB0(); }
    if (s == 10) { __builtin_amdgcn_s_barrier(); SB0(); }
    if (s == K3YS) { yload(); SB0(); STAMP(3); }
    if (s == 0) { STAMP(2); }
    if (s == 2) { STAMP(4); }
    if (s == 6) { STAMP(5); }
    if (s == 10) { STAMP(6); }
  }
  STAMP(7);
  char* red = shm + 20 * 1024;
#pragma unroll
  for (int i = 1; i < 4; ++i)
#pragma unroll
    for (int j = 0; j < 5; ++j)
      *(f4*)(red + ((((wave + i) & 3) * 3 + (i - 1)) * 5 + j) * 1024 + lane * 16) = acc[i][j];
  WAIT_V(0);
  __syncthreads();
  STAMP(9);
  u32x2 ov[5];
#pragma unroll
  for (int j = 0; j < 5; ++j) {
    resid2(j);
    f4 p0 = *(const f4*)(red + ((wave * 3 + 0) * 5 + j) * 1024 + lane * 16);
    f4 p1 = *(const f4*)(red + ((wave * 3 + 1) * 5 + j) * 1024 + lane * 16);
    f4 p2 = *(const f4*)(red + ((wave * 3 + 2) * 5 + j) * 1024 + lane * 16);
    f4 sum = (acc[0][j] + p0) + (p1 + p2);
    u32 m0p = pk_bf(sum[0], sum[1]), m1p = pk_bf(sum[2], sum[3]);
    ov[j] = (u32x2){pk_bf(lo_f(s1p[j][0]) + lo_f(m0p), hi_f(s1p[j][0]) + hi_f(m0p)),
                    pk_bf(lo_f(s1p[j][1]) + lo_f(m1p), hi_f(s1p[j][1]) + hi_f(m1p))};
  }
  u16* orow = out + (size_t)(m0 + ml) * DM + n0;
#pragma unroll
  for (int jp = 0; jp < 2; ++jp) {
    auto r0 = __builtin_amdgcn_permlane16_swap(ov[2 * jp][0], ov[2 * jp + 1][0], false, false);
    auto r1 = __builtin_amdgcn_permlane16_swap(ov[2 * jp][1], ov[2 * jp + 1][1], false, false);
    so16(orow + (2 * jp + (g & 1)) * 16 + 8 * (g >> 1), (u32x4){r0[0], r1[0], r0[1], r1[1]});
  }
  so8(orow + 64 + 4 * g, ov[4]);
  STAMP(8);
  STAMPW();
}

extern "C" __global__ __launch_bounds__(256, 2) void k2_kernel(char* ws, const u16* __restrict__ xin,
                                                    const u16* __restrict__ dskip, u16* __restrict__ out) {
  const u16* xnP = (const u16*)(ws + WS_XNP);
  const u16* w1P = (const u16*)(ws + WS_W1P);
  const float* b1f = (const float*)(ws + WS_B1F);
  const u16* lut = (const u16*)(ws + WS_LUT);
  u16* actF = (u16*)(ws + WS_ACTF);
  u16* actH = (u16*)(ws + WS_ACTH);
  u32* flags = (u32*)(ws + WS_FLAGS);
  __shared__ __attribute__((aligned(1024))) char shm[81920];
  const int t = threadIdx.x, lane = t & 63;
  const int wave = __builtin_amdgcn_readfirstlane(t >> 6);
  const int vb = blockIdx.x;
  if (vb >= K2_G1) {
    const int sb = vb - K2_G1, li = sb >> 3, grp = (sb & 7) * 10 + li / 3, part = li % 3;
    ssm_block<2>(shm, grp, 3 * part, part < 2 ? 3 : 2, xnP, (const u16*)(ws + WS_PG), (u16*)(ws + WS_YT), flags + grp * 3 + part);
    return;
  }
  const int xcd = vb & 7, idx = vb >> 3;
  const int mi = idx & 3, ni = idx >> 2;
  const int bm = (xcd >> 1) * 4 + mi, bn = (xcd & 1) * 8 + ni;
  STAMPB(2, (char*)actF - (16u << 20));
  STAMP(0);
  const int mtw = bm * 8 + 2 * wave, nt0 = 27 * (bn >> 2) + 7 * (bn & 3);
  const char* abase = (const char*)xnP + (size_t)mtw * (20 * 1024);
  const u32 va0 = lane * 16, va1 = lane * 16 + 20 * 1024;
  const u32 lds0 = (u32)(size_t)(const __attribute__((address_space(3))) char*)shm;
  const u32 va4 = lane * 16 + 4096;
  const char* bb1 = (const char*)w1P + (size_t)((nt0 + wave) * 20) * 1024;
  const char* bb2 = (const char*)w1P + (size_t)(nt0 * 20 + 5 * wave) * 1024;
  const u32 mc0 = lds0 + 8192 + wave * 5120;
  const u32 mc2 = wave == 3 ? lds0 + 73728 : mc0 + 40960;
  auto stageB = [&](auto GC, auto IC_) {
    constexpr int gq = decltype(GC)::value, i = decltype(IC_)::value;
    const char* src = gq < NT1 ? bb1 + gq * 5120 : bb2 + (size_t)((gq == 6 && bn == 15) ? 5 : gq) * 20480;
    const u32 m = gq % 3 == 2 ? mc2 : mc0 + (gq % 3) * 20480;
    if constexpr (i < 4) glds_o<i * 1024>(va0, src, m);
    else glds_o<0>(va4, src, m + 4096);
  };
  u32x4 fa[20][2];
  auto loadA = [&](int s) {
    const char* ap = abase + s * 1024;
    GLOAD4(fa[s][0], va0, ap);
    GLOAD4(fa[s][1], va1, ap);
  };
  glds_o<0>((lane < 28 ? lane : 27) * 16, (const char*)(b1f + nt0 * 16), lds0 + BIAS_OFF);
  static_for<5>([&](auto I) { stageB(IC<0>{}, I); });
#pragma unroll
  for (int s = 0; s < 5; ++s) loadA(s);
  static_for<5>([&](auto I) { stageB(IC<1>{}, I); });
#pragma unroll
  for (int s = 5; s < 10; ++s) loadA(s);
  static_for<5>([&](auto I) { stageB(IC<2>{}, I); });
  STAMP(1);
  WAIT_V(30);
  __builtin_amdgcn_s_barrier();
  STAMP(2);
  typedef unsigned short us2 __attribute__((ext_vector_type(2)));
  const u32 lb = lds0 + lane * 16, lbh = lds0 + lane * 16 + 65536, lbb = lds0 + BIAS_OFF + (lane >> 4) * 16;
  f4 acc[7][2];
  u32x4 fb[G1_D + 1];
  f4 bias[7];
  u32 hw[7][4];
  u32 res[7][4], rhi[7][4];
  u32 mx[4] = {0u, 0u, 0u, 0u}, zz, a0 = 0, a1 = 0;
  auto readB = [&](auto PC) {
    constexpr int q = decltype(PC)::value, off = g1_piece_lds(q);
    if constexpr (off < 65536) dsr128<off>(fb[q % (G1_D + 1)], lb);
    else dsr128<off - 65536>(fb[q % (G1_D + 1)], lbh);
  };
  const u32 kb = (0x10000u - (LUT_E0 << 7)) * 0x10001u;
  auto epi_fix = [&](int T) __attribute__((always_inline)) {
#pragma unroll
    for (int q = 0; q < 4; ++q) {
      const u32 z0 = hw[T][q] & 0x7FFFu, z1 = (hw[T][q] >> 16) & 0x7FFFu;
      const bool b0 = (z0 - (LUT_E0 << 7)) >= (u32)LUT_HALF, b1v = (z1 - (LUT_E0 << 7)) >= (u32)LUT_HALF;
      const float h0 = lo_f(hw[T][q]), h1 = hi_f(hw[T][q]);
      const float f0 = (z0 < (LUT_E0 << 7)) ? h0 * 0.59765625f : (h0 > 0.f ? h0 : h0 * 0.0f);
      const float f1 = (z1 < (LUT_E0 << 7)) ? h1 * 0.59765625f : (h1 > 0.f ? h1 : h1 * 0.0f);
      const u32 fw = pk_bf(f0, f1);
      res[T][q] = ((b0 ? fw : res[T][q]) & 0xFFFFu) | ((b1v ? fw : res[T][q]) & 0xFFFF0000u);
    }
  };
  const char* obase = (const char*)actF + ((size_t)mtw * 48 + 3 * bn) * 1024;
#pragma unroll
  for (int T = 0; T < NT1; ++T) DSR128(bias[T], lbb, T * 64);
  static_for<G1_D>(readB);
  const unsigned long long sbi = wave == 0 ? (unsigned long long)ws : (wave == 1 ? (unsigned long long)out : (unsigned long long)xin);
  const char* sb = (const char*)sbi;
  const u32 lim = wave == 0 ? 59 : 5;
  static_for<140>([&](auto PC) {
    constexpr int p = decltype(PC)::value;
    constexpr int T = g1_tile(p), kt = g1_kt(p), gq = p / 20, lp = p % 20;
    if constexpr (G1.vS[p] >= 0) {
      WAIT_L(0);
      WAIT_V(G1.vS[p]);
      __builtin_amdgcn_s_barrier();
      if (gq == 0) { STAMP(3); }
      if (gq == 1) { STAMP(4); }
      if (gq == 3) { STAMP(5); }
      if (gq == 5) { STAMP(6); }
    }
    static_for<3>([&](auto RC) {
      constexpr int r = decltype(RC)::value;
      if constexpr (p == G1.sp[r]) {
        if constexpr (!(r < 2 && G1.ws[r < 2 ? 8 * r + 8 : 0] < p)) max3u(mx[r], a0, a1);
        if (__builtin_expect(__builtin_amdgcn_ballot_w64((mx[r] & 0xFFFFu) >= 2u * LUT_HALF) != 0, 0)) {
          epi_fix(2 * r);
          epi_fix(2 * r + 1);
        }
        SB0();
#pragma unroll
        for (int i = 0; i < 2; ++i)
          st16s(va0, obase + (i * 48 + r) * 1024,
                (u32x4){res[2 * r][2 * i], res[2 * r][2 * i + 1], res[2 * r + 1][2 * i], res[2 * r + 1][2 * i + 1]});
      }
    });
    if constexpr (G1.vA[p] >= 0) {
      WAIT_V(G1.vA[p]);
      pin2(fa[kt][0], fa[kt][1]);
    }
    if constexpr (p < 40 && (p & 1) == 0) gload4(fa[10 + p / 4][(p >> 1) & 1], (p & 2) ? va1 : va0, abase + (10 + p / 4) * 1024);
    if constexpr (g1_vmB(p) >= 0) stageB(IC<3 + g1_vmB(p) / 5>{}, IC<g1_vmB(p) % 5>{});
    if constexpr (g1_vmL(p) >= 0) {
      glds_o<g1_vmL(p) * 1024>(va0, (const char*)lut + wave * 4096, lds0 + (wave & 1) * 4096 + (wave >> 1) * LUT_NEG);
    }
#define TP2 41
    if constexpr (p == TP2) {
      const u32 vo = ((u32)lane < lim ? (u32)lane : lim) * 524284u;
      unsigned keep;
      asm volatile("s_mov_b32 %0, m0\n\ts_mov_b32 m0, %3\n\ts_nop 0\n\tglobal_load_lds_dword %1, %2\n\ts_mov_b32 m0, %0"
                   : "=&s"(keep) : "v"(vo), "s"(sb), "s"(lds0 + 79872 + wave * 256) : "memory");
    }
    if constexpr (p + G1_D < 140) readB(IC<p + G1_D>{});
    if constexpr (lp == 12 && gq + 1 >= NT1 && gq < 6) dsr128<(gq + 1) * 64>(bias[gq + 1], lbb);
    WAIT_L(G1.lB[p]);
    SB0();
    constexpr int w = g1_word(p), wT = w >= 0 ? w >> 2 : 0, wq = w & 3;
    if constexpr (w < 0) step_p<kt == 0>(acc[T][0], acc[T][1], fb[p % (G1_D + 1)], fa[kt][0], fa[kt][1], bias[T]);
    else if constexpr (p == G1.ws[w])
      step_x<kt == 0>(acc[T][0], acc[T][1], fb[p % (G1_D + 1)], fa[kt][0], fa[kt][1], bias[T], hw[wT][wq], zz, mx[w > 0 ? (w - 1) >> 3 : 0],
                      acc[wT][wq >> 1][2 * (wq & 1)], acc[wT][wq >> 1][2 * (wq & 1) + 1], a0, a1, kb);
    else step_y<kt == 0>(acc[T][0], acc[T][1], fb[p % (G1_D + 1)], fa[kt][0], fa[kt][1], bias[T], a0, a1, res[wT][wq], rhi[wT][wq], zz);
    constexpr int cw = g1_comb(p);
    if constexpr (cw >= 0) comb(res[cw >> 2][cw & 3], rhi[cw >> 2][cw & 3]);
    SB0();
  });
  STAMP(7);
  asm volatile("s_nop 7\n\ts_nop 7\n\ts_nop 7" : "+v"(acc[6][0]), "+v"(acc[6][1]) :: "memory");
  if ((bn & 3) != 3) {
#pragma unroll
    for (int q = 0; q < 4; ++q) epi_word(hw[6][q], res[6][q], rhi[6][q], mx[3], acc[6][q >> 1][2 * (q & 1)], acc[6][q >> 1][2 * (q & 1) + 1], kb);
    asm volatile("s_waitcnt lgkmcnt(0)" : "+v"(res[6][0]), "+v"(res[6][1]), "+v"(res[6][2]), "+v"(res[6][3]),
                 "+v"(rhi[6][0]), "+v"(rhi[6][1]), "+v"(rhi[6][2]), "+v"(rhi[6][3]) :: "memory");
#pragma unroll
    for (int q = 0; q < 4; ++q) res[6][q] |= rhi[6][q] << 16;
    if (__builtin_expect(__builtin_amdgcn_ballot_w64((mx[3] & 0xFFFFu) >= 2u * LUT_HALF) != 0, 0)) epi_fix(6);
    st16s(va0, (const char*)actH + ((size_t)(mtw >> 1) * 12 + 3 * (bn >> 2) + (bn & 3)) * 1024,
          (u32x4){res[6][0], res[6][1], res[6][2], res[6][3]});
  }
  STAMP(8);
  WAIT_V(0);
  if (lane == 0) st4(flags + 1024 + (bm * 4 + wave) * 16 + bn, 1u);
  STAMP(11);
  STAMPW();
  RAW_BARRIER();
  g2_block(shm, vb, ws, xin, dskip, out);
}
extern "C" __global__ void k4_end() {}
extern "C" void kernel_launch(void* const* d_in, const int* in_sizes, int n_in,
                              void* d_out, int out_size, void* d_ws, size_t ws_size,
                              hipStream_t stream) {
  const u16* u = (const u16*)d_in[0];
  const u16* lar = (const u16*)d_in[1];
  const u16* aim = (const u16*)d_in[2];
  const u16* cre = (const u16*)d_in[3];
  const u16* cim = (const u16*)d_in[4];
  const u16* logdt = (const u16*)d_in[5];
  const u16* dskip = (const u16*)d_in[6];
  const u16* w1 = (const u16*)d_in[8];
  const u16* b1 = (const u16*)d_in[9];
  const u16* w2 = (const u16*)d_in[10];
  u16* out = (u16*)d_out;
  char* ws = (char*)d_ws;
  if (ws_size < (32u << 20)) return;
  u16* xnP = (u16*)(ws);
  u16* w1P = (u16*)(ws + (4u << 20));
  float2* stats = (float2*)(ws + (12u << 20) + 4096);
  u16* lut = (u16*)(ws + (12u << 20) + 32768);
  float* b1f = (float*)(ws + (12u << 20) + 65536);
  u32* flags = (u32*)(ws + (12u << 20) + 98304);
  u16* yT = (u16*)(ws + (13u << 20));
  u16* actF = (u16*)(ws + (16u << 20));
  u16* actH = (u16*)(ws + (22u << 20));
  u16* w2F = (u16*)(ws + (24u << 20));
  u16* w2H = (u16*)(ws + (26u << 20));
  u16* Pg = (u16*)(ws + (28u << 20));

  k1_kernel<<<K1_LN + K1_KG + K1_RP + K1_RQ + K1_LUT, 256, 0, stream>>>(u, xnP, stats, lut, w1, w1P, w2, w2F, w2H, b1, b1f,
                                                                        lar, aim, cre, cim, logdt, Pg, flags, out, dskip);
  k2_kernel<<<K2_G1 + K2_SSM, 256, 0, stream>>>(ws, u, dskip, out);
}
```
